```python
import torch

def _softmax(x, axis=-1):
    return torch.softmax(x, dim=axis)
def _gelu(x):
    return torch.nn.functional.gelu(x, approximate="tanh")
def _silu(x):
    return torch.nn.functional.silu(x)
def _sigmoid(x):
    return torch.sigmoid(x)
def _layernorm(x, axis=-1, eps=1e-5):
    m = x.mean(dim=axis, keepdim=True)
    v = x.var(dim=axis, keepdim=True, unbiased=False)
    return (x - m) / torch.sqrt(v + eps)
def _scatter_add(tgt, idx, src, axis=0):
    return tgt.clone().index_add_(axis, idx, src)
def _segment_sum(src, seg, n):
    out = torch.zeros((n,) + src.shape[1:], dtype=src.dtype)
    return out.index_add_(0, seg, src)
def _pad(x, pw):
    flat = tuple(v for pair in reversed(pw) for v in pair)
    return torch.nn.functional.pad(x, flat)

kernel_name = 'standalone__weight_only_int_quip_2__prompt'

def setup_inputs():

    _w = (lambda a: (lambda t: t.to(torch.bfloat16) if t.is_floating_point() else t)(torch.as_tensor(a)))
    B, S, D, F = 1, 2048, 576, 4608
    nbits = max(2, min(4, int(3)))
    G = 32 if 32 > 0 else D
    D = (D // G) * G
    ng = D // G
    qmax = 1 << nbits
    qw = torch.randint(0, qmax, (D, F)).to(torch.int32)
    zp = torch.randint(0, qmax, (ng, F)).to(float)
    sc = torch.abs((lambda *s: torch.randn(*s, dtype=torch.bfloat16) * 0.05)(ng, F)).to(float) * 0.1 + 0.02
    return {
        "x": _w((lambda *s: torch.randn(*s, dtype=torch.bfloat16) * 0.05)(B, S, D)),
        "qw": _w(qw),
        "scales": _w(sc),
        "zeros": _w(zp),
    }

def reference(x, qw, scales, zeros):
    B, S, D = x.shape
    ng, F = scales.shape
    G = D // ng
    s = (lambda x, n, axis: torch.repeat_interleave(x, n, dim=axis))(scales, G, axis=0)
    z = (lambda x, n, axis: torch.repeat_interleave(x, n, dim=axis))(zeros, G, axis=0)
    w = ((lambda x, dt: x.to(dt))(qw, x.dtype) - z) * s
    return torch.einsum("bsd,df->bsf", x, w)
```

```cpp
#include <hip/hip_runtime.h>
#include <hip/hip_bf16.h>
#include <stdint.h>

typedef __attribute__((ext_vector_type(8))) short bf16x8;
typedef __attribute__((ext_vector_type(4))) float f32x4;

static __device__ __forceinline__ float bf2f(unsigned short h) { return __uint_as_float(((unsigned)h) << 16); }
static __device__ __forceinline__ unsigned short f2bf(float f) {
  __hip_bfloat16 b = __float2bfloat16(f);
  return *reinterpret_cast<unsigned short*>(&b);
}

__global__ void dequant_kernel(const int* __restrict__ qw, const unsigned short* __restrict__ sc,
                               const unsigned short* __restrict__ zp, unsigned short* __restrict__ wt,
                               int K, int N, int G) {
  int n = blockIdx.x * blockDim.x + threadIdx.x;
  int k = blockIdx.y;
  if (n >= N) return;
  int g = k / G;
  float q = bf2f(f2bf((float)qw[(size_t)k * N + n]));
  float d = bf2f(f2bf(q - bf2f(zp[(size_t)g * N + n])));
  wt[(size_t)n * K + k] = f2bf(d * bf2f(sc[(size_t)g * N + n]));
}

__global__ void gemm_kernel(const unsigned short* __restrict__ x, const unsigned short* __restrict__ wt,
                            unsigned short* __restrict__ out, int M, int N, int K) {
  int lane = threadIdx.x & 63;
  int wave = threadIdx.x >> 6;
  int m0 = blockIdx.y * 64 + wave * 16;
  int n0 = blockIdx.x * 64;
  f32x4 acc[4] = {};
  for (int k = 0; k < K; k += 32) {
    bf16x8 a = *(const bf16x8*)(x + (size_t)(m0 + (lane & 15)) * K + k + 8 * (lane >> 4));
    for (int t = 0; t < 4; ++t) {
      bf16x8 b = *(const bf16x8*)(wt + (size_t)(n0 + t * 16 + (lane & 15)) * K + k + 8 * (lane >> 4));
      acc[t] = __builtin_amdgcn_mfma_f32_16x16x32_bf16(a, b, acc[t], 0, 0, 0);
    }
  }
  for (int t = 0; t < 4; ++t)
    for (int j = 0; j < 4; ++j)
      out[(size_t)(m0 + (lane >> 4) * 4 + j) * N + n0 + t * 16 + (lane & 15)] = f2bf(acc[t][j]);
}

extern "C" void kernel_launch(void* const* d_in, const int* in_sizes, int n_in,
                              void* d_out, int out_size, void* d_ws, size_t ws_size,
                              hipStream_t stream) {
  const unsigned short* x = (const unsigned short*)d_in[0];
  const int* qw = (const int*)d_in[1];
  const unsigned short* sc = (const unsigned short*)d_in[2];
  const unsigned short* zp = (const unsigned short*)d_in[3];
  const int M = 2048, K = 576, N = 4608, G = 32;
  unsigned short* wt = (unsigned short*)d_ws;
  dequant_kernel<<<dim3(N / 256, K), 256, 0, stream>>>(qw, sc, zp, wt, K, N, G);
  gemm_kernel<<<dim3(N / 64, M / 64), 256, 0, stream>>>(x, wt, (unsigned short*)d_out, M, N, K);
}
```

```cpp
#include <hip/hip_runtime.h>
#include <hip/hip_bf16.h>
#include <stdint.h>


typedef unsigned short u16;

static __device__ __forceinline__ float bf2f(u16 h) { return __uint_as_float(((unsigned)h) << 16); }
static __device__ __forceinline__ float rbf(float f) { return __bfloat162float(__float2bfloat16(f)); }

constexpr int M_ = 2048, K_ = 576, N_ = 4608;

__global__ __launch_bounds__(512, 2) void wq_gemm_kernel(const u16* __restrict__ x, const int* __restrict__ qw,
                                                         const u16* __restrict__ sc, const u16* __restrict__ zp,
                                                         u16* __restrict__ out) {
  __shared__ __attribute__((aligned(16))) char lds[153888];
  unsigned bad;
  asm volatile(
      "s_load_dwordx8 s[4:11], %[karg], 0x0\n"
      "s_load_dwordx2 s[12:13], %[karg], 0x20\n"
      "v_lshrrev_b32 v4, 6, %[tid]\n"
      "v_and_b32 v1, 63, %[tid]\n"
      "v_readfirstlane_b32 s39, v4\n"
      "s_lshr_b32 s52, %[bid], 3\n"
      "s_and_b32 s53, %[bid], 7\n"
      "s_getpc_b64 s[50:51]\n"
      "Lhere_%=:\n"
      "v_and_b32 v4, 1, v1\n"
      "v_lshl_add_u32 v4, v4, 3, s39\n"
      "v_lshl_add_u32 v4, v4, 5, s52\n"
      "v_lshlrev_b32 v4, 7, v4\n"
      "v_min_u32 v4, Lend_%=-Lhere_%=-4, v4\n"
      "s_mov_b32 m0, 153632\n"
      "s_mov_b64 exec, 3\n"
      "global_load_lds_dword v4, s[50:51]\n"
      "s_mov_b64 exec, -1\n"
      "s_lshl_b32 s54, s53, 5\n"
      "s_sub_i32 s55, s53, 4\n"
      "s_max_i32 s55, s55, 0\n"
      "s_sub_u32 s54, s54, s55\n"
      "s_add_u32 s54, s54, s52\n"
      "s_mul_i32 s42, s54, 9363\n"
      "s_lshr_b32 s42, s42, 16\n"
      "s_mul_i32 s55, s42, 7\n"
      "s_sub_u32 s41, s54, s55\n"
      "s_lshl_b32 s40, s42, 7\n"
      "s_and_b32 s48, s39, 1\n"
      "s_bfe_u32 s47, s39, 0x10001\n"
      "s_lshr_b32 s46, s39, 2\n"
      "s_and_b32 s49, s39, 3\n"
      "v_and_b32 v2, 15, v1\n"
      "v_lshrrev_b32 v3, 4, v1\n"
      "s_lshl_b32 s55, s48, 6\n"
      "s_add_u32 s55, s55, s40\n"
      "s_mul_i32 s56, s47, 589824\n"
      "s_lshl_b32 s57, s55, 2\n"
      "s_add_u32 s56, s56, s57\n"
      "v_mul_u32_u24 v210, 147456, v3\n"
      "v_lshl_add_u32 v210, v2, 4, v210\n"
      "v_add_u32 v210, s56, v210\n"
      "v_add_u32 v211, 18432, v210\n"
      "v_add_u32 v212, 36864, v210\n"
      "v_add_u32 v213, 55296, v210\n"
      "s_lshl_b32 s56, s46, 1\n"
      "s_add_u32 s56, s56, s47\n"
      "s_mul_i32 s56, s56, 9216\n"
      "s_lshl_b32 s57, s55, 1\n"
      "s_add_u32 s56, s56, s57\n"
      "v_lshrrev_b32 v4, 3, v1\n"
      "v_mul_u32_u24 v4, 36864, v4\n"
      "v_and_b32 v5, 7, v1\n"
      "v_lshl_add_u32 v4, v5, 4, v4\n"
      "v_add_u32 v248, s56, v4\n"
      "s_mul_i32 s44, s39, 1280\n"
      "s_add_u32 s44, s44, 143360\n"
      "s_mul_i32 s56, s46, 1179648\n"
      "s_add_u32 s57, s56, 73728\n"
      "s_cmp_eq_u32 s46, 0\n"
      "s_cselect_b32 s58, 0xff, 0\n"
      "s_mov_b32 s22, 10616832\n"
      "s_mov_b32 s23, 0x20000\n"
      "s_mov_b32 s26, 165888\n"
      "s_mov_b32 s27, 0x20000\n"
      "s_mov_b32 s30, 165888\n"
      "s_mov_b32 s31, 0x20000\n"
      "s_mov_b32 s18, 2359296\n"
      "s_mov_b32 s19, 0x20000\n"
      "s_mov_b32 s70, 18874368\n"
      "s_mov_b32 s71, 0x20000\n"
      "s_mov_b32 m0, s44\n"
      "s_waitcnt lgkmcnt(0)\n"
      "s_mov_b32 s24, s8\n"
      "s_and_b32 s25, s9, 0xffff\n"
      "s_mov_b32 s28, s10\n"
      "s_and_b32 s29, s11, 0xffff\n"
      "s_mov_b32 s20, s6\n"
      "s_and_b32 s21, s7, 0xffff\n"
      "s_mov_b32 s16, s4\n"
      "s_and_b32 s17, s5, 0xffff\n"
      "s_mov_b32 exec_hi, s58\n"
      "buffer_load_dwordx4 v248, s[24:27], 0 offen lds\n"
      "s_add_u32 m0, s44, 640\n"
      "s_nop 0\n"
      "buffer_load_dwordx4 v248, s[28:31], 0 offen lds\n"
      "s_mov_b64 exec, -1\n"
      "buffer_load_dwordx4 v[120:123], v210, s[20:23], s56 offen\n"
      "buffer_load_dwordx4 v[124:127], v211, s[20:23], s56 offen\n"
      "buffer_load_dwordx4 v[128:131], v212, s[20:23], s56 offen\n"
      "buffer_load_dwordx4 v[132:135], v213, s[20:23], s56 offen\n"
      "buffer_load_dwordx4 v[136:139], v210, s[20:23], s57 offen\n"
      "buffer_load_dwordx4 v[140:143], v211, s[20:23], s57 offen\n"
      "buffer_load_dwordx4 v[144:147], v212, s[20:23], s57 offen\n"
      "buffer_load_dwordx4 v[148:151], v213, s[20:23], s57 offen\n"
      "s_mov_b32 s68, s12\n"
      "s_and_b32 s69, s13, 0xffff\n"
      "s_mul_i32 s55, s41, 18\n"
      "s_min_u32 s59, s41, 2\n"
      "s_add_u32 s55, s55, s59\n"
      "s_mul_i32 s36, s55, 18432\n"
      "s_mul_i32 s37, s55, 147456\n"
      "s_cmp_lt_u32 s41, 2\n"
      "s_cselect_b32 s38, 19, 18\n"
      "s_lshr_b32 s55, s39, 1\n"
      "s_cmp_eq_u32 s55, 1\n"
      "s_cbranch_scc1 Lp1_%=\n"
      "s_cmp_eq_u32 s55, 2\n"
      "s_cbranch_scc1 Lp2_%=\n"
      "s_cmp_eq_u32 s55, 3\n"
      "s_cbranch_scc1 Lp3_%=\n"
      "s_lshl_b32 s60, s39, 6\n"
      "v_add_u32 v4, s60, v1\n"
      "v_mul_u32_u24 v4, 7, v4\n"
      "v_add_u32 v4, s41, v4\n"
      "v_min_u32 v4, 2303, v4\n"
      "v_lshrrev_b32 v5, 2, v4\n"
      "v_and_b32 v4, 3, v4\n"
      "v_mul_u32_u24 v5, 18432, v5\n"
      "v_lshl_add_u32 v4, v4, 7, v5\n"
      "s_lshl_b32 s60, s40, 2\n"
      "s_mov_b32 m0, 153632\n"
      "s_nop 0\n"
      "buffer_load_dword v4, s[20:23], s60 offen lds\n"
      "v_lshrrev_b32 v4, 1, v2\n"
      "v_xor_b32 v5, v3, v4\n"
      "v_lshlrev_b32 v5, 4, v5\n"
      "v_lshl_add_u32 v240, v2, 7, v5\n"
      "v_xor_b32 v241, 64, v240\n"
      "v_add_u32 v214, 0x10000, v240\n"
      "v_add_u32 v216, 0x20000, v240\n"
      "v_add_u32 v215, 0x10000, v241\n"
      "v_add_u32 v217, 0x20000, v241\n"
      "v_lshrrev_b32 v4, 3, v1\n"
      "v_and_b32 v5, 1, v4\n"
      "v_xor_b32 v5, v1, v5\n"
      "s_lshl_b32 s60, s49, 12\n"
      "s_add_u32 s60, s60, 110592\n"
      "v_lshlrev_b32 v5, 4, v5\n"
      "v_add_u32 v242, s60, v5\n"
      "s_lshl_b32 s60, s48, 3\n"
      "v_add_u32 v4, s60, v4\n"
      "v_lshrrev_b32 v5, 1, v4\n"
      "v_and_b32 v6, 7, v1\n"
      "v_xor_b32 v5, v5, v6\n"
      "v_and_b32 v5, 7, v5\n"
      "v_mul_u32_u24 v4, 1152, v4\n"
      "v_lshl_add_u32 v243, v5, 4, v4\n"
      "s_lshl_b32 s43, s48, 10\n"
      "v_lshrrev_b32 v4, 3, v2\n"
      "v_lshlrev_b32 v4, 12, v4\n"
      "v_lshl_add_u32 v4, v3, 8, v4\n"
      "v_and_b32 v5, 7, v2\n"
      "v_lshl_add_u32 v4, v5, 5, v4\n"
      "s_lshl_b32 s60, s48, 13\n"
      "s_add_u32 s60, s60, 110592\n"
      "v_add_u32 v4, s60, v4\n"
      "v_lshrrev_b32 v5, 2, v2\n"
      "v_and_b32 v5, 1, v5\n"
      "v_lshl_add_u32 v244, v5, 4, v4\n"
      "v_xor_b32 v245, 16, v244\n"
      "v_lshlrev_b32 v4, 3, v2\n"
      "v_add_u32 v246, s44, v4\n"
      "s_lshl_b32 s60, s49, 5\n"
      "s_add_u32 s60, s60, s40\n"
      "s_lshl_b32 s60, s60, 1\n"
      "v_mul_u32_u24 v4, 36864, v3\n"
      "v_lshl_add_u32 v4, v2, 2, v4\n"
      "v_add_u32 v247, s60, v4\n"
      "s_lshl_b32 s60, s39, 2\n"
      "s_add_u32 s45, s60, 153600\n"
      "v_mov_b32 v208, 0\n"
      "v_mov_b32 v209, 0\n"
      "s_waitcnt vmcnt(9)\n"
      "v_lshlrev_b32 v168, 7, v3\n"
      "v_add_u32 v168, v246, v168\n"
      "ds_read_b64 v[172:173], v168\n"
      "ds_read_b64 v[174:175], v168 offset:640\n"
      "ds_read_b64 v[176:177], v246 offset:512\n"
      "ds_read_b64 v[178:179], v246 offset:1152\n"
      "v_mov_b32 v180, 0\n"
      "v_mov_b32 v181, 0\n"
      "v_mov_b32 v182, 0\n"
      "s_waitcnt lgkmcnt(0)\n"
      "v_lshlrev_b32 v168, 16, v174\n"
      "v_and_b32 v169, 0xffff0000, v174\n"
      "v_max3_f32 v180, |v168|, |v169|, v180\n"
      "v_rndne_f32 v170, v168\n"
      "v_rndne_f32 v171, v169\n"
      "v_sub_f32 v168, v168, v170\n"
      "v_sub_f32 v169, v169, v171\n"
      "v_max3_f32 v181, |v168|, |v169|, v181\n"
      "v_lshlrev_b32 v168, 16, v172\n"
      "v_and_b32 v169, 0xffff0000, v172\n"
      "v_max3_f32 v182, |v168|, |v169|, v182\n"
      "v_lshlrev_b32 v168, 16, v175\n"
      "v_and_b32 v169, 0xffff0000, v175\n"
      "v_max3_f32 v180, |v168|, |v169|, v180\n"
      "v_rndne_f32 v170, v168\n"
      "v_rndne_f32 v171, v169\n"
      "v_sub_f32 v168, v168, v170\n"
      "v_sub_f32 v169, v169, v171\n"
      "v_max3_f32 v181, |v168|, |v169|, v181\n"
      "v_lshlrev_b32 v168, 16, v173\n"
      "v_and_b32 v169, 0xffff0000, v173\n"
      "v_max3_f32 v182, |v168|, |v169|, v182\n"
      "v_lshlrev_b32 v168, 16, v178\n"
      "v_and_b32 v169, 0xffff0000, v178\n"
      "v_max3_f32 v180, |v168|, |v169|, v180\n"
      "v_rndne_f32 v170, v168\n"
      "v_rndne_f32 v171, v169\n"
      "v_sub_f32 v168, v168, v170\n"
      "v_sub_f32 v169, v169, v171\n"
      "v_max3_f32 v181, |v168|, |v169|, v181\n"
      "v_lshlrev_b32 v168, 16, v176\n"
      "v_and_b32 v169, 0xffff0000, v176\n"
      "v_max3_f32 v182, |v168|, |v169|, v182\n"
      "v_lshlrev_b32 v168, 16, v179\n"
      "v_and_b32 v169, 0xffff0000, v179\n"
      "v_max3_f32 v180, |v168|, |v169|, v180\n"
      "v_rndne_f32 v170, v168\n"
      "v_rndne_f32 v171, v169\n"
      "v_sub_f32 v168, v168, v170\n"
      "v_sub_f32 v169, v169, v171\n"
      "v_max3_f32 v181, |v168|, |v169|, v181\n"
      "v_lshlrev_b32 v168, 16, v177\n"
      "v_and_b32 v169, 0xffff0000, v177\n"
      "v_max3_f32 v182, |v168|, |v169|, v182\n"
      "v_cmp_lt_f32 vcc, 0, v181\n"
      "s_mov_b64 s[66:67], vcc\n"
      "v_cmp_lt_f32 vcc, 0x43000000, v180\n"
      "s_or_b64 s[66:67], s[66:67], vcc\n"
      "v_cmp_le_f32 vcc, 0x71800000, v182\n"
      "s_or_b64 s[66:67], s[66:67], vcc\n"
      "ds_read_b64 v[184:185], v246 offset:0\n"
      "ds_read_b64 v[186:187], v246 offset:640\n"
      "s_mov_b32 s63, 2359296\n"
      "buffer_load_dwordx4 v[40:43], v210, s[20:23], s63 offen\n"
      "s_waitcnt lgkmcnt(0)\n"
      "v_lshlrev_b32 v200, 16, v184\n"
      "v_and_b32 v201, 0xffff0000, v184\n"
      "v_lshlrev_b32 v204, 16, v186\n"
      "v_and_b32 v205, 0xffff0000, v186\n"
      "v_lshlrev_b32 v202, 16, v185\n"
      "v_and_b32 v203, 0xffff0000, v185\n"
      "v_lshlrev_b32 v206, 16, v187\n"
      "v_and_b32 v207, 0xffff0000, v187\n"
      "buffer_load_dwordx4 v[44:47], v211, s[20:23], s63 offen\n"
      "v_mul_f32_e64 v204, -v204, v200\n"
      "v_mul_f32_e64 v205, -v205, v201\n"
      "v_mul_f32 v200, 0x4b800000, v200\n"
      "v_mul_f32 v201, 0x4b800000, v201\n"
      "v_mul_f32_e64 v206, -v206, v202\n"
      "v_mul_f32_e64 v207, -v207, v203\n"
      "v_mul_f32 v202, 0x4b800000, v202\n"
      "v_mul_f32 v203, 0x4b800000, v203\n"
      "s_waitcnt vmcnt(9)\n"
      "v_or3_b32 v208, v120, v121, v208\n"
      "v_or3_b32 v208, v122, v123, v208\n"
      "v_or3_b32 v209, v124, v125, v209\n"
      "v_or3_b32 v209, v126, v127, v209\n"
      "buffer_load_dwordx4 v[48:51], v212, s[20:23], s63 offen\n"
      "v_fma_mix_f32 v120, v120, v200, v204 op_sel_hi:[1,0,0]\n"
      "v_fma_mix_f32 v121, v121, v201, v205 op_sel_hi:[1,0,0]\n"
      "v_fma_mix_f32 v122, v122, v202, v206 op_sel_hi:[1,0,0]\n"
      "v_fma_mix_f32 v123, v123, v203, v207 op_sel_hi:[1,0,0]\n"
      "v_fma_mix_f32 v124, v124, v200, v204 op_sel_hi:[1,0,0]\n"
      "v_fma_mix_f32 v125, v125, v201, v205 op_sel_hi:[1,0,0]\n"
      "v_fma_mix_f32 v126, v126, v202, v206 op_sel_hi:[1,0,0]\n"
      "v_fma_mix_f32 v127, v127, v203, v207 op_sel_hi:[1,0,0]\n"
      "v_cvt_pk_bf16_f32 v184, v120, v124\n"
      "v_cvt_pk_bf16_f32 v188, v121, v125\n"
      "v_cvt_pk_bf16_f32 v192, v122, v126\n"
      "v_cvt_pk_bf16_f32 v196, v123, v127\n"
      "buffer_load_dwordx4 v[52:55], v213, s[20:23], s63 offen\n"
      "s_waitcnt vmcnt(9)\n"
      "v_or3_b32 v208, v128, v129, v208\n"
      "v_or3_b32 v208, v130, v131, v208\n"
      "v_or3_b32 v209, v132, v133, v209\n"
      "v_or3_b32 v209, v134, v135, v209\n"
      "v_fma_mix_f32 v128, v128, v200, v204 op_sel_hi:[1,0,0]\n"
      "v_fma_mix_f32 v129, v129, v201, v205 op_sel_hi:[1,0,0]\n"
      "v_fma_mix_f32 v130, v130, v202, v206 op_sel_hi:[1,0,0]\n"
      "v_fma_mix_f32 v131, v131, v203, v207 op_sel_hi:[1,0,0]\n"
      "v_fma_mix_f32 v132, v132, v200, v204 op_sel_hi:[1,0,0]\n"
      "v_fma_mix_f32 v133, v133, v201, v205 op_sel_hi:[1,0,0]\n"
      "v_fma_mix_f32 v134, v134, v202, v206 op_sel_hi:[1,0,0]\n"
      "v_fma_mix_f32 v135, v135, v203, v207 op_sel_hi:[1,0,0]\n"
      "s_mov_b32 s63, 2433024\n"
      "buffer_load_dwordx4 v[56:59], v210, s[20:23], s63 offen\n"
      "v_cvt_pk_bf16_f32 v185, v128, v132\n"
      "v_cvt_pk_bf16_f32 v189, v129, v133\n"
      "v_cvt_pk_bf16_f32 v193, v130, v134\n"
      "v_cvt_pk_bf16_f32 v197, v131, v135\n"
      "s_waitcnt vmcnt(6)\n"
      "buffer_load_dwordx4 v[60:63], v211, s[20:23], s63 offen\n"
      "v_or3_b32 v208, v136, v140, v208\n"
      "v_or3_b32 v209, v144, v148, v209\n"
      "v_fma_mix_f32 v136, v136, v200, v204 op_sel_hi:[1,0,0]\n"
      "v_fma_mix_f32 v140, v140, v200, v204 op_sel_hi:[1,0,0]\n"
      "v_fma_mix_f32 v144, v144, v200, v204 op_sel_hi:[1,0,0]\n"
      "v_fma_mix_f32 v148, v148, v200, v204 op_sel_hi:[1,0,0]\n"
      "v_cvt_pk_bf16_f32 v186, v136, v140\n"
      "v_cvt_pk_bf16_f32 v187, v144, v148\n"
      "ds_write_b128 v244, v[184:187] offset:0\n"
      "v_or3_b32 v208, v137, v141, v208\n"
      "v_or3_b32 v209, v145, v149, v209\n"
      "v_fma_mix_f32 v137, v137, v201, v205 op_sel_hi:[1,0,0]\n"
      "v_fma_mix_f32 v141, v141, v201, v205 op_sel_hi:[1,0,0]\n"
      "buffer_load_dwordx4 v[64:67], v212, s[20:23], s63 offen\n"
      "v_fma_mix_f32 v145, v145, v201, v205 op_sel_hi:[1,0,0]\n"
      "v_fma_mix_f32 v149, v149, v201, v205 op_sel_hi:[1,0,0]\n"
      "v_cvt_pk_bf16_f32 v190, v137, v141\n"
      "v_cvt_pk_bf16_f32 v191, v145, v149\n"
      "ds_write_b128 v244, v[188:191] offset:1024\n"
      "v_or3_b32 v208, v138, v142, v208\n"
      "v_or3_b32 v209, v146, v150, v209\n"
      "v_fma_mix_f32 v138, v138, v202, v206 op_sel_hi:[1,0,0]\n"
      "v_fma_mix_f32 v142, v142, v202, v206 op_sel_hi:[1,0,0]\n"
      "v_fma_mix_f32 v146, v146, v202, v206 op_sel_hi:[1,0,0]\n"
      "v_fma_mix_f32 v150, v150, v202, v206 op_sel_hi:[1,0,0]\n"
      "v_cvt_pk_bf16_f32 v194, v138, v142\n"
      "v_cvt_pk_bf16_f32 v195, v146, v150\n"
      "ds_write_b128 v245, v[192:195] offset:0\n"
      "buffer_load_dwordx4 v[68:71], v213, s[20:23], s63 offen\n"
      "v_or3_b32 v208, v139, v143, v208\n"
      "v_or3_b32 v209, v147, v151, v209\n"
      "v_fma_mix_f32 v139, v139, v203, v207 op_sel_hi:[1,0,0]\n"
      "v_fma_mix_f32 v143, v143, v203, v207 op_sel_hi:[1,0,0]\n"
      "v_fma_mix_f32 v147, v147, v203, v207 op_sel_hi:[1,0,0]\n"
      "v_fma_mix_f32 v151, v151, v203, v207 op_sel_hi:[1,0,0]\n"
      "v_cvt_pk_bf16_f32 v198, v139, v143\n"
      "v_cvt_pk_bf16_f32 v199, v147, v151\n"
      "ds_write_b128 v245, v[196:199] offset:1024\n"
      "s_waitcnt lgkmcnt(0)\n"
      "s_barrier\n"
      "ds_read_b128 v[8:11], v242 offset:0\n"
      "ds_read_b128 v[12:15], v242 offset:1024\n"
      "ds_read_b128 v[16:19], v242 offset:2048\n"
      "ds_read_b128 v[20:23], v242 offset:3072\n"
      "s_mov_b32 s63, 4718592\n"
      "buffer_load_dwordx4 v[72:75], v210, s[20:23], s63 offen\n"
      "buffer_load_dwordx4 v[76:79], v211, s[20:23], s63 offen\n"
      "buffer_load_dwordx4 v[80:83], v212, s[20:23], s63 offen\n"
      "buffer_load_dwordx4 v[84:87], v213, s[20:23], s63 offen\n"
      "ds_read_b64 v[184:185], v246 offset:128\n"
      "ds_read_b64 v[186:187], v246 offset:768\n"
      "s_waitcnt lgkmcnt(0)\n"
      "v_lshlrev_b32 v200, 16, v184\n"
      "v_and_b32 v201, 0xffff0000, v184\n"
      "v_lshlrev_b32 v204, 16, v186\n"
      "v_and_b32 v205, 0xffff0000, v186\n"
      "v_lshlrev_b32 v202, 16, v185\n"
      "v_and_b32 v203, 0xffff0000, v185\n"
      "v_lshlrev_b32 v206, 16, v187\n"
      "v_and_b32 v207, 0xffff0000, v187\n"
      "v_mul_f32_e64 v204, -v204, v200\n"
      "v_mul_f32_e64 v205, -v205, v201\n"
      "v_mul_f32 v200, 0x4b800000, v200\n"
      "v_mul_f32 v201, 0x4b800000, v201\n"
      "v_mul_f32_e64 v206, -v206, v202\n"
      "v_mul_f32_e64 v207, -v207, v203\n"
      "v_mul_f32 v202, 0x4b800000, v202\n"
      "v_mul_f32 v203, 0x4b800000, v203\n"
      "s_waitcnt vmcnt(10)\n"
      "v_or3_b32 v208, v40, v41, v208\n"
      "v_or3_b32 v208, v42, v43, v208\n"
      "v_or3_b32 v209, v44, v45, v209\n"
      "v_or3_b32 v209, v46, v47, v209\n"
      "v_fma_mix_f32 v40, v40, v200, v204 op_sel_hi:[1,0,0]\n"
      "v_fma_mix_f32 v41, v41, v201, v205 op_sel_hi:[1,0,0]\n"
      "v_fma_mix_f32 v42, v42, v202, v206 op_sel_hi:[1,0,0]\n"
      "v_fma_mix_f32 v43, v43, v203, v207 op_sel_hi:[1,0,0]\n"
      "v_fma_mix_f32 v44, v44, v200, v204 op_sel_hi:[1,0,0]\n"
      "v_fma_mix_f32 v45, v45, v201, v205 op_sel_hi:[1,0,0]\n"
      "v_fma_mix_f32 v46, v46, v202, v206 op_sel_hi:[1,0,0]\n"
      "v_fma_mix_f32 v47, v47, v203, v207 op_sel_hi:[1,0,0]\n"
      "v_cvt_pk_bf16_f32 v184, v40, v44\n"
      "v_cvt_pk_bf16_f32 v188, v41, v45\n"
      "v_cvt_pk_bf16_f32 v192, v42, v46\n"
      "v_cvt_pk_bf16_f32 v196, v43, v47\n"
      "s_waitcnt vmcnt(8)\n"
      "v_or3_b32 v208, v48, v49, v208\n"
      "v_or3_b32 v208, v50, v51, v208\n"
      "v_or3_b32 v209, v52, v53, v209\n"
      "v_or3_b32 v209, v54, v55, v209\n"
      "v_fma_mix_f32 v48, v48, v200, v204 op_sel_hi:[1,0,0]\n"
      "v_fma_mix_f32 v49, v49, v201, v205 op_sel_hi:[1,0,0]\n"
      "v_fma_mix_f32 v50, v50, v202, v206 op_sel_hi:[1,0,0]\n"
      "v_fma_mix_f32 v51, v51, v203, v207 op_sel_hi:[1,0,0]\n"
      "v_fma_mix_f32 v52, v52, v200, v204 op_sel_hi:[1,0,0]\n"
      "v_fma_mix_f32 v53, v53, v201, v205 op_sel_hi:[1,0,0]\n"
      "v_fma_mix_f32 v54, v54, v202, v206 op_sel_hi:[1,0,0]\n"
      "v_fma_mix_f32 v55, v55, v203, v207 op_sel_hi:[1,0,0]\n"
      "v_cvt_pk_bf16_f32 v185, v48, v52\n"
      "v_cvt_pk_bf16_f32 v189, v49, v53\n"
      "v_cvt_pk_bf16_f32 v193, v50, v54\n"
      "v_cvt_pk_bf16_f32 v197, v51, v55\n"
      "s_barrier\n"
      "ds_read_b128 v[24:27], v242 offset:16384\n"
      "ds_read_b128 v[28:31], v242 offset:17408\n"
      "ds_read_b128 v[32:35], v242 offset:18432\n"
      "ds_read_b128 v[36:39], v242 offset:19456\n"
      "s_setprio 1\n"
      "s_waitcnt vmcnt(4)\n"
      "v_or3_b32 v208, v56, v60, v208\n"
      "v_or3_b32 v209, v64, v68, v209\n"
      "v_fma_mix_f32 v56, v56, v200, v204 op_sel_hi:[1,0,0]\n"
      "v_fma_mix_f32 v60, v60, v200, v204 op_sel_hi:[1,0,0]\n"
      "v_fma_mix_f32 v64, v64, v200, v204 op_sel_hi:[1,0,0]\n"
      "v_fma_mix_f32 v68, v68, v200, v204 op_sel_hi:[1,0,0]\n"
      "v_cvt_pk_bf16_f32 v186, v56, v60\n"
      "v_cvt_pk_bf16_f32 v187, v64, v68\n"
      "ds_write_b128 v244, v[184:187] offset:0\n"
      "v_or3_b32 v208, v57, v61, v208\n"
      "v_or3_b32 v209, v65, v69, v209\n"
      "v_fma_mix_f32 v57, v57, v201, v205 op_sel_hi:[1,0,0]\n"
      "v_fma_mix_f32 v61, v61, v201, v205 op_sel_hi:[1,0,0]\n"
      "v_fma_mix_f32 v65, v65, v201, v205 op_sel_hi:[1,0,0]\n"
      "v_fma_mix_f32 v69, v69, v201, v205 op_sel_hi:[1,0,0]\n"
      "v_cvt_pk_bf16_f32 v190, v57, v61\n"
      "v_cvt_pk_bf16_f32 v191, v65, v69\n"
      "ds_write_b128 v244, v[188:191] offset:1024\n"
      "v_or3_b32 v208, v58, v62, v208\n"
      "v_or3_b32 v209, v66, v70, v209\n"
      "v_fma_mix_f32 v58, v58, v202, v206 op_sel_hi:[1,0,0]\n"
      "v_fma_mix_f32 v62, v62, v202, v206 op_sel_hi:[1,0,0]\n"
      "v_fma_mix_f32 v66, v66, v202, v206 op_sel_hi:[1,0,0]\n"
      "v_fma_mix_f32 v70, v70, v202, v206 op_sel_hi:[1,0,0]\n"
      "v_cvt_pk_bf16_f32 v194, v58, v62\n"
      "v_cvt_pk_bf16_f32 v195, v66, v70\n"
      "ds_write_b128 v245, v[192:195] offset:0\n"
      "v_or3_b32 v208, v59, v63, v208\n"
      "v_or3_b32 v209, v67, v71, v209\n"
      "v_fma_mix_f32 v59, v59, v203, v207 op_sel_hi:[1,0,0]\n"
      "v_fma_mix_f32 v63, v63, v203, v207 op_sel_hi:[1,0,0]\n"
      "v_fma_mix_f32 v67, v67, v203, v207 op_sel_hi:[1,0,0]\n"
      "v_fma_mix_f32 v71, v71, v203, v207 op_sel_hi:[1,0,0]\n"
      "v_cvt_pk_bf16_f32 v198, v59, v63\n"
      "v_cvt_pk_bf16_f32 v199, v67, v71\n"
      "ds_write_b128 v245, v[196:199] offset:1024\n"
      "s_setprio 0\n"
      "s_mov_b32 s63, 4792320\n"
      "buffer_load_dwordx4 v[88:91], v210, s[20:23], s63 offen\n"
      "buffer_load_dwordx4 v[92:95], v211, s[20:23], s63 offen\n"
      "buffer_load_dwordx4 v[96:99], v212, s[20:23], s63 offen\n"
      "buffer_load_dwordx4 v[100:103], v213, s[20:23], s63 offen\n"
      "s_waitcnt lgkmcnt(0)\n"
      "s_barrier\n"
      "ds_read_b128 v[40:43], v242 offset:0\n"
      "ds_read_b128 v[44:47], v242 offset:1024\n"
      "ds_read_b128 v[48:51], v242 offset:2048\n"
      "ds_read_b128 v[52:55], v242 offset:3072\n"
      "s_mov_b32 s63, 7077888\n"
      "buffer_load_dwordx4 v[104:107], v210, s[20:23], s63 offen\n"
      "buffer_load_dwordx4 v[108:111], v211, s[20:23], s63 offen\n"
      "buffer_load_dwordx4 v[112:115], v212, s[20:23], s63 offen\n"
      "buffer_load_dwordx4 v[116:119], v213, s[20:23], s63 offen\n"
      "ds_read_b64 v[184:185], v246 offset:256\n"
      "ds_read_b64 v[186:187], v246 offset:896\n"
      "s_waitcnt lgkmcnt(0)\n"
      "v_lshlrev_b32 v200, 16, v184\n"
      "v_and_b32 v201, 0xffff0000, v184\n"
      "v_lshlrev_b32 v204, 16, v186\n"
      "v_and_b32 v205, 0xffff0000, v186\n"
      "v_lshlrev_b32 v202, 16, v185\n"
      "v_and_b32 v203, 0xffff0000, v185\n"
      "v_lshlrev_b32 v206, 16, v187\n"
      "v_and_b32 v207, 0xffff0000, v187\n"
      "v_mul_f32_e64 v204, -v204, v200\n"
      "v_mul_f32_e64 v205, -v205, v201\n"
      "v_mul_f32 v200, 0x4b800000, v200\n"
      "v_mul_f32 v201, 0x4b800000, v201\n"
      "v_mul_f32_e64 v206, -v206, v202\n"
      "v_mul_f32_e64 v207, -v207, v203\n"
      "v_mul_f32 v202, 0x4b800000, v202\n"
      "v_mul_f32 v203, 0x4b800000, v203\n"
      "s_waitcnt vmcnt(10)\n"
      "v_or3_b32 v208, v72, v73, v208\n"
      "v_or3_b32 v208, v74, v75, v208\n"
      "v_or3_b32 v209, v76, v77, v209\n"
      "v_or3_b32 v209, v78, v79, v209\n"
      "v_fma_mix_f32 v72, v72, v200, v204 op_sel_hi:[1,0,0]\n"
      "v_fma_mix_f32 v73, v73, v201, v205 op_sel_hi:[1,0,0]\n"
      "v_fma_mix_f32 v74, v74, v202, v206 op_sel_hi:[1,0,0]\n"
      "v_fma_mix_f32 v75, v75, v203, v207 op_sel_hi:[1,0,0]\n"
      "v_fma_mix_f32 v76, v76, v200, v204 op_sel_hi:[1,0,0]\n"
      "v_fma_mix_f32 v77, v77, v201, v205 op_sel_hi:[1,0,0]\n"
      "v_fma_mix_f32 v78, v78, v202, v206 op_sel_hi:[1,0,0]\n"
      "v_fma_mix_f32 v79, v79, v203, v207 op_sel_hi:[1,0,0]\n"
      "v_cvt_pk_bf16_f32 v184, v72, v76\n"
      "v_cvt_pk_bf16_f32 v188, v73, v77\n"
      "v_cvt_pk_bf16_f32 v192, v74, v78\n"
      "v_cvt_pk_bf16_f32 v196, v75, v79\n"
      "s_waitcnt vmcnt(8)\n"
      "v_or3_b32 v208, v80, v81, v208\n"
      "v_or3_b32 v208, v82, v83, v208\n"
      "v_or3_b32 v209, v84, v85, v209\n"
      "v_or3_b32 v209, v86, v87, v209\n"
      "v_fma_mix_f32 v80, v80, v200, v204 op_sel_hi:[1,0,0]\n"
      "v_fma_mix_f32 v81, v81, v201, v205 op_sel_hi:[1,0,0]\n"
      "v_fma_mix_f32 v82, v82, v202, v206 op_sel_hi:[1,0,0]\n"
      "v_fma_mix_f32 v83, v83, v203, v207 op_sel_hi:[1,0,0]\n"
      "v_fma_mix_f32 v84, v84, v200, v204 op_sel_hi:[1,0,0]\n"
      "v_fma_mix_f32 v85, v85, v201, v205 op_sel_hi:[1,0,0]\n"
      "v_fma_mix_f32 v86, v86, v202, v206 op_sel_hi:[1,0,0]\n"
      "v_fma_mix_f32 v87, v87, v203, v207 op_sel_hi:[1,0,0]\n"
      "v_cvt_pk_bf16_f32 v185, v80, v84\n"
      "v_cvt_pk_bf16_f32 v189, v81, v85\n"
      "v_cvt_pk_bf16_f32 v193, v82, v86\n"
      "v_cvt_pk_bf16_f32 v197, v83, v87\n"
      "s_barrier\n"
      "ds_read_b128 v[56:59], v242 offset:16384\n"
      "ds_read_b128 v[60:63], v242 offset:17408\n"
      "ds_read_b128 v[64:67], v242 offset:18432\n"
      "ds_read_b128 v[68:71], v242 offset:19456\n"
      "s_setprio 1\n"
      "s_waitcnt vmcnt(4)\n"
      "v_or3_b32 v208, v88, v92, v208\n"
      "v_or3_b32 v209, v96, v100, v209\n"
      "v_fma_mix_f32 v88, v88, v200, v204 op_sel_hi:[1,0,0]\n"
      "v_fma_mix_f32 v92, v92, v200, v204 op_sel_hi:[1,0,0]\n"
      "v_fma_mix_f32 v96, v96, v200, v204 op_sel_hi:[1,0,0]\n"
      "v_fma_mix_f32 v100, v100, v200, v204 op_sel_hi:[1,0,0]\n"
      "v_cvt_pk_bf16_f32 v186, v88, v92\n"
      "v_cvt_pk_bf16_f32 v187, v96, v100\n"
      "ds_write_b128 v244, v[184:187] offset:0\n"
      "v_or3_b32 v208, v89, v93, v208\n"
      "v_or3_b32 v209, v97, v101, v209\n"
      "v_fma_mix_f32 v89, v89, v201, v205 op_sel_hi:[1,0,0]\n"
      "v_fma_mix_f32 v93, v93, v201, v205 op_sel_hi:[1,0,0]\n"
      "v_fma_mix_f32 v97, v97, v201, v205 op_sel_hi:[1,0,0]\n"
      "v_fma_mix_f32 v101, v101, v201, v205 op_sel_hi:[1,0,0]\n"
      "v_cvt_pk_bf16_f32 v190, v89, v93\n"
      "v_cvt_pk_bf16_f32 v191, v97, v101\n"
      "ds_write_b128 v244, v[188:191] offset:1024\n"
      "v_or3_b32 v208, v90, v94, v208\n"
      "v_or3_b32 v209, v98, v102, v209\n"
      "v_fma_mix_f32 v90, v90, v202, v206 op_sel_hi:[1,0,0]\n"
      "v_fma_mix_f32 v94, v94, v202, v206 op_sel_hi:[1,0,0]\n"
      "v_fma_mix_f32 v98, v98, v202, v206 op_sel_hi:[1,0,0]\n"
      "v_fma_mix_f32 v102, v102, v202, v206 op_sel_hi:[1,0,0]\n"
      "v_cvt_pk_bf16_f32 v194, v90, v94\n"
      "v_cvt_pk_bf16_f32 v195, v98, v102\n"
      "ds_write_b128 v245, v[192:195] offset:0\n"
      "v_or3_b32 v208, v91, v95, v208\n"
      "v_or3_b32 v209, v99, v103, v209\n"
      "v_fma_mix_f32 v91, v91, v203, v207 op_sel_hi:[1,0,0]\n"
      "v_fma_mix_f32 v95, v95, v203, v207 op_sel_hi:[1,0,0]\n"
      "v_fma_mix_f32 v99, v99, v203, v207 op_sel_hi:[1,0,0]\n"
      "v_fma_mix_f32 v103, v103, v203, v207 op_sel_hi:[1,0,0]\n"
      "v_cvt_pk_bf16_f32 v198, v91, v95\n"
      "v_cvt_pk_bf16_f32 v199, v99, v103\n"
      "ds_write_b128 v245, v[196:199] offset:1024\n"
      "s_setprio 0\n"
      "s_mov_b32 s63, 7151616\n"
      "buffer_load_dwordx4 v[120:123], v210, s[20:23], s63 offen\n"
      "buffer_load_dwordx4 v[124:127], v211, s[20:23], s63 offen\n"
      "buffer_load_dwordx4 v[128:131], v212, s[20:23], s63 offen\n"
      "buffer_load_dwordx4 v[132:135], v213, s[20:23], s63 offen\n"
      "s_waitcnt lgkmcnt(0)\n"
      "s_barrier\n"
      "ds_read_b128 v[72:75], v242 offset:0\n"
      "ds_read_b128 v[76:79], v242 offset:1024\n"
      "ds_read_b128 v[80:83], v242 offset:2048\n"
      "ds_read_b128 v[84:87], v242 offset:3072\n"
      "s_mov_b32 s63, 9437184\n"
      "buffer_load_dwordx4 v[136:139], v210, s[20:23], s63 offen\n"
      "buffer_load_dwordx4 v[140:143], v211, s[20:23], s63 offen\n"
      "buffer_load_dwordx4 v[144:147], v212, s[20:23], s63 offen\n"
      "buffer_load_dwordx4 v[148:151], v213, s[20:23], s63 offen\n"
      "ds_read_b64 v[184:185], v246 offset:384\n"
      "ds_read_b64 v[186:187], v246 offset:1024\n"
      "s_waitcnt lgkmcnt(0)\n"
      "v_lshlrev_b32 v200, 16, v184\n"
      "v_and_b32 v201, 0xffff0000, v184\n"
      "v_lshlrev_b32 v204, 16, v186\n"
      "v_and_b32 v205, 0xffff0000, v186\n"
      "v_lshlrev_b32 v202, 16, v185\n"
      "v_and_b32 v203, 0xffff0000, v185\n"
      "v_lshlrev_b32 v206, 16, v187\n"
      "v_and_b32 v207, 0xffff0000, v187\n"
      "v_mul_f32_e64 v204, -v204, v200\n"
      "v_mul_f32_e64 v205, -v205, v201\n"
      "v_mul_f32 v200, 0x4b800000, v200\n"
      "v_mul_f32 v201, 0x4b800000, v201\n"
      "v_mul_f32_e64 v206, -v206, v202\n"
      "v_mul_f32_e64 v207, -v207, v203\n"
      "v_mul_f32 v202, 0x4b800000, v202\n"
      "v_mul_f32 v203, 0x4b800000, v203\n"
      "s_waitcnt vmcnt(10)\n"
      "v_or3_b32 v208, v104, v105, v208\n"
      "v_or3_b32 v208, v106, v107, v208\n"
      "v_or3_b32 v209, v108, v109, v209\n"
      "v_or3_b32 v209, v110, v111, v209\n"
      "v_fma_mix_f32 v104, v104, v200, v204 op_sel_hi:[1,0,0]\n"
      "v_fma_mix_f32 v105, v105, v201, v205 op_sel_hi:[1,0,0]\n"
      "v_fma_mix_f32 v106, v106, v202, v206 op_sel_hi:[1,0,0]\n"
      "v_fma_mix_f32 v107, v107, v203, v207 op_sel_hi:[1,0,0]\n"
      "v_fma_mix_f32 v108, v108, v200, v204 op_sel_hi:[1,0,0]\n"
      "v_fma_mix_f32 v109, v109, v201, v205 op_sel_hi:[1,0,0]\n"
      "v_fma_mix_f32 v110, v110, v202, v206 op_sel_hi:[1,0,0]\n"
      "v_fma_mix_f32 v111, v111, v203, v207 op_sel_hi:[1,0,0]\n"
      "v_cvt_pk_bf16_f32 v184, v104, v108\n"
      "v_cvt_pk_bf16_f32 v188, v105, v109\n"
      "v_cvt_pk_bf16_f32 v192, v106, v110\n"
      "v_cvt_pk_bf16_f32 v196, v107, v111\n"
      "s_waitcnt vmcnt(8)\n"
      "v_or3_b32 v208, v112, v113, v208\n"
      "v_or3_b32 v208, v114, v115, v208\n"
      "v_or3_b32 v209, v116, v117, v209\n"
      "v_or3_b32 v209, v118, v119, v209\n"
      "v_fma_mix_f32 v112, v112, v200, v204 op_sel_hi:[1,0,0]\n"
      "v_fma_mix_f32 v113, v113, v201, v205 op_sel_hi:[1,0,0]\n"
      "v_fma_mix_f32 v114, v114, v202, v206 op_sel_hi:[1,0,0]\n"
      "v_fma_mix_f32 v115, v115, v203, v207 op_sel_hi:[1,0,0]\n"
      "v_fma_mix_f32 v116, v116, v200, v204 op_sel_hi:[1,0,0]\n"
      "v_fma_mix_f32 v117, v117, v201, v205 op_sel_hi:[1,0,0]\n"
      "v_fma_mix_f32 v118, v118, v202, v206 op_sel_hi:[1,0,0]\n"
      "v_fma_mix_f32 v119, v119, v203, v207 op_sel_hi:[1,0,0]\n"
      "v_cvt_pk_bf16_f32 v185, v112, v116\n"
      "v_cvt_pk_bf16_f32 v189, v113, v117\n"
      "v_cvt_pk_bf16_f32 v193, v114, v118\n"
      "v_cvt_pk_bf16_f32 v197, v115, v119\n"
      "s_barrier\n"
      "ds_read_b128 v[88:91], v242 offset:16384\n"
      "ds_read_b128 v[92:95], v242 offset:17408\n"
      "ds_read_b128 v[96:99], v242 offset:18432\n"
      "ds_read_b128 v[100:103], v242 offset:19456\n"
      "s_setprio 1\n"
      "s_waitcnt vmcnt(4)\n"
      "v_or3_b32 v208, v120, v124, v208\n"
      "v_or3_b32 v209, v128, v132, v209\n"
      "v_fma_mix_f32 v120, v120, v200, v204 op_sel_hi:[1,0,0]\n"
      "v_fma_mix_f32 v124, v124, v200, v204 op_sel_hi:[1,0,0]\n"
      "v_fma_mix_f32 v128, v128, v200, v204 op_sel_hi:[1,0,0]\n"
      "v_fma_mix_f32 v132, v132, v200, v204 op_sel_hi:[1,0,0]\n"
      "v_cvt_pk_bf16_f32 v186, v120, v124\n"
      "v_cvt_pk_bf16_f32 v187, v128, v132\n"
      "ds_write_b128 v244, v[184:187] offset:0\n"
      "v_or3_b32 v208, v121, v125, v208\n"
      "v_or3_b32 v209, v129, v133, v209\n"
      "v_fma_mix_f32 v121, v121, v201, v205 op_sel_hi:[1,0,0]\n"
      "v_fma_mix_f32 v125, v125, v201, v205 op_sel_hi:[1,0,0]\n"
      "v_fma_mix_f32 v129, v129, v201, v205 op_sel_hi:[1,0,0]\n"
      "v_fma_mix_f32 v133, v133, v201, v205 op_sel_hi:[1,0,0]\n"
      "v_cvt_pk_bf16_f32 v190, v121, v125\n"
      "v_cvt_pk_bf16_f32 v191, v129, v133\n"
      "ds_write_b128 v244, v[188:191] offset:1024\n"
      "v_or3_b32 v208, v122, v126, v208\n"
      "v_or3_b32 v209, v130, v134, v209\n"
      "v_fma_mix_f32 v122, v122, v202, v206 op_sel_hi:[1,0,0]\n"
      "v_fma_mix_f32 v126, v126, v202, v206 op_sel_hi:[1,0,0]\n"
      "v_fma_mix_f32 v130, v130, v202, v206 op_sel_hi:[1,0,0]\n"
      "v_fma_mix_f32 v134, v134, v202, v206 op_sel_hi:[1,0,0]\n"
      "v_cvt_pk_bf16_f32 v194, v122, v126\n"
      "v_cvt_pk_bf16_f32 v195, v130, v134\n"
      "ds_write_b128 v245, v[192:195] offset:0\n"
      "v_or3_b32 v208, v123, v127, v208\n"
      "v_or3_b32 v209, v131, v135, v209\n"
      "v_fma_mix_f32 v123, v123, v203, v207 op_sel_hi:[1,0,0]\n"
      "v_fma_mix_f32 v127, v127, v203, v207 op_sel_hi:[1,0,0]\n"
      "v_fma_mix_f32 v131, v131, v203, v207 op_sel_hi:[1,0,0]\n"
      "v_fma_mix_f32 v135, v135, v203, v207 op_sel_hi:[1,0,0]\n"
      "v_cvt_pk_bf16_f32 v198, v123, v127\n"
      "v_cvt_pk_bf16_f32 v199, v131, v135\n"
      "ds_write_b128 v245, v[196:199] offset:1024\n"
      "s_setprio 0\n"
      "s_mov_b32 s63, 9510912\n"
      "buffer_load_dwordx4 v[152:155], v210, s[20:23], s63 offen\n"
      "buffer_load_dwordx4 v[156:159], v211, s[20:23], s63 offen\n"
      "buffer_load_dwordx4 v[160:163], v212, s[20:23], s63 offen\n"
      "buffer_load_dwordx4 v[164:167], v213, s[20:23], s63 offen\n"
      "s_add_u32 m0, s43, 0\n"
      "s_add_u32 s61, s36, 0\n"
      "buffer_load_dwordx4 v243, s[16:19], s61 offen lds\n"
      "s_add_u32 m0, s43, 2048\n"
      "s_add_u32 s61, s36, 128\n"
      "buffer_load_dwordx4 v243, s[16:19], s61 offen lds\n"
      "s_add_u32 m0, s43, 4096\n"
      "s_add_u32 s61, s36, 256\n"
      "buffer_load_dwordx4 v243, s[16:19], s61 offen lds\n"
      "s_waitcnt lgkmcnt(0)\n"
      "s_barrier\n"
      "ds_read_b128 v[104:107], v242 offset:0\n"
      "ds_read_b128 v[108:111], v242 offset:1024\n"
      "ds_read_b128 v[112:115], v242 offset:2048\n"
      "ds_read_b128 v[116:119], v242 offset:3072\n"
      "s_add_u32 m0, s43, 18432\n"
      "s_add_u32 s61, s36, 18432\n"
      "buffer_load_dwordx4 v243, s[16:19], s61 offen lds\n"
      "s_add_u32 m0, s43, 20480\n"
      "s_add_u32 s61, s36, 18560\n"
      "buffer_load_dwordx4 v243, s[16:19], s61 offen lds\n"
      "s_add_u32 m0, s43, 22528\n"
      "s_add_u32 s61, s36, 18688\n"
      "buffer_load_dwordx4 v243, s[16:19], s61 offen lds\n"
      "ds_read_b64 v[184:185], v246 offset:512\n"
      "ds_read_b64 v[186:187], v246 offset:1152\n"
      "s_waitcnt lgkmcnt(0)\n"
      "v_lshlrev_b32 v200, 16, v184\n"
      "v_and_b32 v201, 0xffff0000, v184\n"
      "v_lshlrev_b32 v204, 16, v186\n"
      "v_and_b32 v205, 0xffff0000, v186\n"
      "v_lshlrev_b32 v202, 16, v185\n"
      "v_and_b32 v203, 0xffff0000, v185\n"
      "v_lshlrev_b32 v206, 16, v187\n"
      "v_and_b32 v207, 0xffff0000, v187\n"
      "v_mul_f32_e64 v204, -v204, v200\n"
      "v_mul_f32_e64 v205, -v205, v201\n"
      "v_mul_f32 v200, 0x4b800000, v200\n"
      "v_mul_f32 v201, 0x4b800000, v201\n"
      "v_mul_f32_e64 v206, -v206, v202\n"
      "v_mul_f32_e64 v207, -v207, v203\n"
      "v_mul_f32 v202, 0x4b800000, v202\n"
      "v_mul_f32 v203, 0x4b800000, v203\n"
      "s_waitcnt vmcnt(12)\n"
      "v_or3_b32 v208, v136, v137, v208\n"
      "v_or3_b32 v208, v138, v139, v208\n"
      "v_or3_b32 v209, v140, v141, v209\n"
      "v_or3_b32 v209, v142, v143, v209\n"
      "v_fma_mix_f32 v136, v136, v200, v204 op_sel_hi:[1,0,0]\n"
      "v_fma_mix_f32 v137, v137, v201, v205 op_sel_hi:[1,0,0]\n"
      "v_fma_mix_f32 v138, v138, v202, v206 op_sel_hi:[1,0,0]\n"
      "v_fma_mix_f32 v139, v139, v203, v207 op_sel_hi:[1,0,0]\n"
      "v_fma_mix_f32 v140, v140, v200, v204 op_sel_hi:[1,0,0]\n"
      "v_fma_mix_f32 v141, v141, v201, v205 op_sel_hi:[1,0,0]\n"
      "v_fma_mix_f32 v142, v142, v202, v206 op_sel_hi:[1,0,0]\n"
      "v_fma_mix_f32 v143, v143, v203, v207 op_sel_hi:[1,0,0]\n"
      "v_cvt_pk_bf16_f32 v184, v136, v140\n"
      "v_cvt_pk_bf16_f32 v188, v137, v141\n"
      "v_cvt_pk_bf16_f32 v192, v138, v142\n"
      "v_cvt_pk_bf16_f32 v196, v139, v143\n"
      "s_waitcnt vmcnt(10)\n"
      "v_or3_b32 v208, v144, v145, v208\n"
      "v_or3_b32 v208, v146, v147, v208\n"
      "v_or3_b32 v209, v148, v149, v209\n"
      "v_or3_b32 v209, v150, v151, v209\n"
      "v_fma_mix_f32 v144, v144, v200, v204 op_sel_hi:[1,0,0]\n"
      "v_fma_mix_f32 v145, v145, v201, v205 op_sel_hi:[1,0,0]\n"
      "v_fma_mix_f32 v146, v146, v202, v206 op_sel_hi:[1,0,0]\n"
      "v_fma_mix_f32 v147, v147, v203, v207 op_sel_hi:[1,0,0]\n"
      "v_fma_mix_f32 v148, v148, v200, v204 op_sel_hi:[1,0,0]\n"
      "v_fma_mix_f32 v149, v149, v201, v205 op_sel_hi:[1,0,0]\n"
      "v_fma_mix_f32 v150, v150, v202, v206 op_sel_hi:[1,0,0]\n"
      "v_fma_mix_f32 v151, v151, v203, v207 op_sel_hi:[1,0,0]\n"
      "v_cvt_pk_bf16_f32 v185, v144, v148\n"
      "v_cvt_pk_bf16_f32 v189, v145, v149\n"
      "v_cvt_pk_bf16_f32 v193, v146, v150\n"
      "v_cvt_pk_bf16_f32 v197, v147, v151\n"
      "s_waitcnt vmcnt(3)\n"
      "s_barrier\n"
      "ds_read_b128 v[120:123], v242 offset:16384\n"
      "ds_read_b128 v[124:127], v242 offset:17408\n"
      "ds_read_b128 v[128:131], v242 offset:18432\n"
      "ds_read_b128 v[132:135], v242 offset:19456\n"
      "s_setprio 1\n"
      "v_or3_b32 v208, v152, v156, v208\n"
      "v_or3_b32 v209, v160, v164, v209\n"
      "v_fma_mix_f32 v152, v152, v200, v204 op_sel_hi:[1,0,0]\n"
      "v_fma_mix_f32 v156, v156, v200, v204 op_sel_hi:[1,0,0]\n"
      "v_fma_mix_f32 v160, v160, v200, v204 op_sel_hi:[1,0,0]\n"
      "v_fma_mix_f32 v164, v164, v200, v204 op_sel_hi:[1,0,0]\n"
      "v_cvt_pk_bf16_f32 v186, v152, v156\n"
      "v_cvt_pk_bf16_f32 v187, v160, v164\n"
      "ds_write_b128 v244, v[184:187] offset:0\n"
      "v_or3_b32 v208, v153, v157, v208\n"
      "v_or3_b32 v209, v161, v165, v209\n"
      "v_fma_mix_f32 v153, v153, v201, v205 op_sel_hi:[1,0,0]\n"
      "v_fma_mix_f32 v157, v157, v201, v205 op_sel_hi:[1,0,0]\n"
      "v_fma_mix_f32 v161, v161, v201, v205 op_sel_hi:[1,0,0]\n"
      "v_fma_mix_f32 v165, v165, v201, v205 op_sel_hi:[1,0,0]\n"
      "v_cvt_pk_bf16_f32 v190, v153, v157\n"
      "v_cvt_pk_bf16_f32 v191, v161, v165\n"
      "ds_write_b128 v244, v[188:191] offset:1024\n"
      "v_or3_b32 v208, v154, v158, v208\n"
      "v_or3_b32 v209, v162, v166, v209\n"
      "v_fma_mix_f32 v154, v154, v202, v206 op_sel_hi:[1,0,0]\n"
      "v_fma_mix_f32 v158, v158, v202, v206 op_sel_hi:[1,0,0]\n"
      "v_fma_mix_f32 v162, v162, v202, v206 op_sel_hi:[1,0,0]\n"
      "v_fma_mix_f32 v166, v166, v202, v206 op_sel_hi:[1,0,0]\n"
      "v_cvt_pk_bf16_f32 v194, v154, v158\n"
      "v_cvt_pk_bf16_f32 v195, v162, v166\n"
      "ds_write_b128 v245, v[192:195] offset:0\n"
      "v_or3_b32 v208, v155, v159, v208\n"
      "v_or3_b32 v209, v163, v167, v209\n"
      "v_fma_mix_f32 v155, v155, v203, v207 op_sel_hi:[1,0,0]\n"
      "v_fma_mix_f32 v159, v159, v203, v207 op_sel_hi:[1,0,0]\n"
      "v_fma_mix_f32 v163, v163, v203, v207 op_sel_hi:[1,0,0]\n"
      "v_fma_mix_f32 v167, v167, v203, v207 op_sel_hi:[1,0,0]\n"
      "v_cvt_pk_bf16_f32 v198, v155, v159\n"
      "v_cvt_pk_bf16_f32 v199, v163, v167\n"
      "ds_write_b128 v245, v[196:199] offset:1024\n"
      "v_or_b32 v208, v208, v209\n"
      "v_cmp_lt_u32 vcc, 0x7f, v208\n"
      "s_or_b64 s[66:67], s[66:67], vcc\n"
      "s_or_b32 s66, s66, s67\n"
      "v_mov_b32 v201, s45\n"
      "v_mov_b32 v200, s66\n"
      "ds_write_b32 v201, v200\n"
      "s_setprio 0\n"
      "s_add_u32 m0, s43, 36864\n"
      "s_add_u32 s61, s36, 36864\n"
      "buffer_load_dwordx4 v243, s[16:19], s61 offen lds\n"
      "s_add_u32 m0, s43, 38912\n"
      "s_add_u32 s61, s36, 36992\n"
      "buffer_load_dwordx4 v243, s[16:19], s61 offen lds\n"
      "s_add_u32 m0, s43, 40960\n"
      "s_add_u32 s61, s36, 37120\n"
      "buffer_load_dwordx4 v243, s[16:19], s61 offen lds\n"
      "s_waitcnt lgkmcnt(0)\n"
      "s_waitcnt vmcnt(3)\n"
      "ds_read_b128 v[168:171], v240 offset:0\n"
      "ds_read_b128 v[172:175], v241 offset:0\n"
      "ds_read_b128 v[176:179], v240 offset:2048\n"
      "ds_read_b128 v[180:183], v241 offset:2048\n"
      "ds_read_b128 v[184:187], v240 offset:4096\n"
      "ds_read_b128 v[188:191], v241 offset:4096\n"
      "s_barrier\n"
      "ds_read_b128 v[136:139], v242 offset:0\n"
      "ds_read_b128 v[140:143], v242 offset:1024\n"
      "ds_read_b128 v[144:147], v242 offset:2048\n"
      "ds_read_b128 v[148:151], v242 offset:3072\n"
      "s_add_u32 m0, s43, 55296\n"
      "s_add_u32 s61, s36, 55296\n"
      "buffer_load_dwordx4 v243, s[16:19], s61 offen lds\n"
      "s_add_u32 m0, s43, 57344\n"
      "s_add_u32 s61, s36, 55424\n"
      "buffer_load_dwordx4 v243, s[16:19], s61 offen lds\n"
      "s_add_u32 m0, s43, 59392\n"
      "s_add_u32 s61, s36, 55552\n"
      "buffer_load_dwordx4 v243, s[16:19], s61 offen lds\n"
      "s_waitcnt lgkmcnt(0)\n"
      "v_mfma_f32_16x16x32_bf16 v[152:155], v[168:171], v[8:11], 0\n"
      "ds_read_b128 v[192:195], v240 offset:6144\n"
      "v_mfma_f32_16x16x32_bf16 v[156:159], v[168:171], v[12:15], 0\n"
      "v_mfma_f32_16x16x32_bf16 v[152:155], v[172:175], v[16:19], v[152:155]\n"
      "ds_read_b128 v[196:199], v241 offset:6144\n"
      "v_mfma_f32_16x16x32_bf16 v[156:159], v[172:175], v[20:23], v[156:159]\n"
      "v_mfma_f32_16x16x32_bf16 v[152:155], v[176:179], v[24:27], v[152:155]\n"
      "ds_read_b128 v[168:171], v240 offset:8192\n"
      "v_mfma_f32_16x16x32_bf16 v[156:159], v[176:179], v[28:31], v[156:159]\n"
      "v_mfma_f32_16x16x32_bf16 v[152:155], v[180:183], v[32:35], v[152:155]\n"
      "ds_read_b128 v[172:175], v241 offset:8192\n"
      "v_mfma_f32_16x16x32_bf16 v[156:159], v[180:183], v[36:39], v[156:159]\n"
      "v_mfma_f32_16x16x32_bf16 v[152:155], v[184:187], v[40:43], v[152:155]\n"
      "ds_read_b128 v[176:179], v240 offset:10240\n"
      "v_mfma_f32_16x16x32_bf16 v[156:159], v[184:187], v[44:47], v[156:159]\n"
      "v_mfma_f32_16x16x32_bf16 v[152:155], v[188:191], v[48:51], v[152:155]\n"
      "ds_read_b128 v[180:183], v241 offset:10240\n"
      "v_mfma_f32_16x16x32_bf16 v[156:159], v[188:191], v[52:55], v[156:159]\n"
      "s_waitcnt lgkmcnt(5)\n"
      "v_mfma_f32_16x16x32_bf16 v[152:155], v[192:195], v[56:59], v[152:155]\n"
      "ds_read_b128 v[184:187], v240 offset:12288\n"
      "v_mfma_f32_16x16x32_bf16 v[156:159], v[192:195], v[60:63], v[156:159]\n"
      "s_waitcnt lgkmcnt(5)\n"
      "v_mfma_f32_16x16x32_bf16 v[152:155], v[196:199], v[64:67], v[152:155]\n"
      "ds_read_b128 v[188:191], v241 offset:12288\n"
      "v_mfma_f32_16x16x32_bf16 v[156:159], v[196:199], v[68:71], v[156:159]\n"
      "s_waitcnt lgkmcnt(5)\n"
      "v_mfma_f32_16x16x32_bf16 v[152:155], v[168:171], v[72:75], v[152:155]\n"
      "ds_read_b128 v[192:195], v240 offset:14336\n"
      "v_mfma_f32_16x16x32_bf16 v[156:159], v[168:171], v[76:79], v[156:159]\n"
      "s_add_u32 m0, s43, 73728\n"
      "s_add_u32 s61, s36, 73728\n"
      "buffer_load_dwordx4 v243, s[16:19], s61 offen lds\n"
      "s_add_u32 m0, s43, 75776\n"
      "s_add_u32 s61, s36, 73856\n"
      "buffer_load_dwordx4 v243, s[16:19], s61 offen lds\n"
      "s_add_u32 m0, s43, 77824\n"
      "s_add_u32 s61, s36, 73984\n"
      "buffer_load_dwordx4 v243, s[16:19], s61 offen lds\n"
      "s_add_u32 m0, s43, 79872\n"
      "s_add_u32 s61, s36, 74112\n"
      "buffer_load_dwordx4 v243, s[16:19], s61 offen lds\n"
      "s_add_u32 m0, s43, 81920\n"
      "s_add_u32 s61, s36, 74240\n"
      "buffer_load_dwordx4 v243, s[16:19], s61 offen lds\n"
      "s_waitcnt vmcnt(5)\n"
      "s_barrier\n"
      "s_waitcnt lgkmcnt(5)\n"
      "v_mfma_f32_16x16x32_bf16 v[152:155], v[172:175], v[80:83], v[152:155]\n"
      "ds_read_b128 v[196:199], v241 offset:14336\n"
      "v_mfma_f32_16x16x32_bf16 v[156:159], v[172:175], v[84:87], v[156:159]\n"
      "s_waitcnt lgkmcnt(5)\n"
      "v_mfma_f32_16x16x32_bf16 v[152:155], v[176:179], v[88:91], v[152:155]\n"
      "ds_read_b128 v[168:171], v240 offset:16384\n"
      "v_mfma_f32_16x16x32_bf16 v[156:159], v[176:179], v[92:95], v[156:159]\n"
      "s_waitcnt lgkmcnt(5)\n"
      "v_mfma_f32_16x16x32_bf16 v[152:155], v[180:183], v[96:99], v[152:155]\n"
      "ds_read_b128 v[172:175], v241 offset:16384\n"
      "v_mfma_f32_16x16x32_bf16 v[156:159], v[180:183], v[100:103], v[156:159]\n"
      "s_waitcnt lgkmcnt(5)\n"
      "v_mfma_f32_16x16x32_bf16 v[152:155], v[184:187], v[104:107], v[152:155]\n"
      "ds_read_b128 v[176:179], v240 offset:36864\n"
      "v_mfma_f32_16x16x32_bf16 v[156:159], v[184:187], v[108:111], v[156:159]\n"
      "s_waitcnt lgkmcnt(5)\n"
      "v_mfma_f32_16x16x32_bf16 v[152:155], v[188:191], v[112:115], v[152:155]\n"
      "ds_read_b128 v[180:183], v241 offset:36864\n"
      "v_mfma_f32_16x16x32_bf16 v[156:159], v[188:191], v[116:119], v[156:159]\n"
      "s_waitcnt lgkmcnt(5)\n"
      "v_mfma_f32_16x16x32_bf16 v[152:155], v[192:195], v[120:123], v[152:155]\n"
      "ds_read_b128 v[184:187], v240 offset:38912\n"
      "v_mfma_f32_16x16x32_bf16 v[156:159], v[192:195], v[124:127], v[156:159]\n"
      "s_waitcnt lgkmcnt(5)\n"
      "v_mfma_f32_16x16x32_bf16 v[152:155], v[196:199], v[128:131], v[152:155]\n"
      "ds_read_b128 v[188:191], v241 offset:38912\n"
      "v_mfma_f32_16x16x32_bf16 v[156:159], v[196:199], v[132:135], v[156:159]\n"
      "s_waitcnt lgkmcnt(5)\n"
      "v_mfma_f32_16x16x32_bf16 v[152:155], v[168:171], v[136:139], v[152:155]\n"
      "ds_read_b128 v[192:195], v240 offset:40960\n"
      "v_mfma_f32_16x16x32_bf16 v[156:159], v[168:171], v[140:143], v[156:159]\n"
      "s_waitcnt lgkmcnt(5)\n"
      "v_mfma_f32_16x16x32_bf16 v[152:155], v[172:175], v[144:147], v[152:155]\n"
      "ds_read_b128 v[196:199], v241 offset:40960\n"
      "v_mfma_f32_16x16x32_bf16 v[156:159], v[172:175], v[148:151], v[156:159]\n"
      "s_waitcnt lgkmcnt(5)\n"
      "v_mfma_f32_16x16x32_bf16 v[160:163], v[176:179], v[8:11], 0\n"
      "ds_read_b128 v[168:171], v240 offset:43008\n"
      "v_mfma_f32_16x16x32_bf16 v[164:167], v[176:179], v[12:15], 0\n"
      "s_waitcnt lgkmcnt(5)\n"
      "v_mfma_f32_16x16x32_bf16 v[160:163], v[180:183], v[16:19], v[160:163]\n"
      "ds_read_b128 v[172:175], v241 offset:43008\n"
      "v_mfma_f32_16x16x32_bf16 v[164:167], v[180:183], v[20:23], v[164:167]\n"
      "s_waitcnt lgkmcnt(5)\n"
      "v_mfma_f32_16x16x32_bf16 v[160:163], v[184:187], v[24:27], v[160:163]\n"
      "ds_read_b128 v[176:179], v240 offset:45056\n"
      "v_mfma_f32_16x16x32_bf16 v[164:167], v[184:187], v[28:31], v[164:167]\n"
      "s_waitcnt lgkmcnt(5)\n"
      "v_mfma_f32_16x16x32_bf16 v[160:163], v[188:191], v[32:35], v[160:163]\n"
      "ds_read_b128 v[180:183], v241 offset:45056\n"
      "v_mfma_f32_16x16x32_bf16 v[164:167], v[188:191], v[36:39], v[164:167]\n"
      "s_waitcnt lgkmcnt(5)\n"
      "v_mfma_f32_16x16x32_bf16 v[160:163], v[192:195], v[40:43], v[160:163]\n"
      "ds_read_b128 v[184:187], v240 offset:47104\n"
      "v_mfma_f32_16x16x32_bf16 v[164:167], v[192:195], v[44:47], v[164:167]\n"
      "s_waitcnt lgkmcnt(5)\n"
      "v_mfma_f32_16x16x32_bf16 v[160:163], v[196:199], v[48:51], v[160:163]\n"
      "ds_read_b128 v[188:191], v241 offset:47104\n"
      "v_mfma_f32_16x16x32_bf16 v[164:167], v[196:199], v[52:55], v[164:167]\n"
      "s_waitcnt lgkmcnt(5)\n"
      "v_mfma_f32_16x16x32_bf16 v[160:163], v[168:171], v[56:59], v[160:163]\n"
      "ds_read_b128 v[192:195], v240 offset:49152\n"
      "v_mfma_f32_16x16x32_bf16 v[164:167], v[168:171], v[60:63], v[164:167]\n"
      "s_waitcnt lgkmcnt(5)\n"
      "v_mfma_f32_16x16x32_bf16 v[160:163], v[172:175], v[64:67], v[160:163]\n"
      "ds_read_b128 v[196:199], v241 offset:49152\n"
      "v_mfma_f32_16x16x32_bf16 v[164:167], v[172:175], v[68:71], v[164:167]\n"
      "s_waitcnt lgkmcnt(5)\n"
      "v_mfma_f32_16x16x32_bf16 v[160:163], v[176:179], v[72:75], v[160:163]\n"
      "ds_read_b128 v[168:171], v240 offset:51200\n"
      "v_mfma_f32_16x16x32_bf16 v[164:167], v[176:179], v[76:79], v[164:167]\n"
      "s_add_u32 m0, s43, 110592\n"
      "s_add_u32 s61, s36, 110592\n"
      "buffer_load_dwordx4 v243, s[16:19], s61 offen lds\n"
      "s_add_u32 m0, s43, 112640\n"
      "s_add_u32 s61, s36, 110720\n"
      "buffer_load_dwordx4 v243, s[16:19], s61 offen lds\n"
      "s_add_u32 m0, s43, 114688\n"
      "s_add_u32 s61, s36, 110848\n"
      "buffer_load_dwordx4 v243, s[16:19], s61 offen lds\n"
      "s_add_u32 m0, s43, 116736\n"
      "s_add_u32 s61, s36, 110976\n"
      "buffer_load_dwordx4 v243, s[16:19], s61 offen lds\n"
      "s_add_u32 m0, s43, 118784\n"
      "s_add_u32 s61, s36, 111104\n"
      "buffer_load_dwordx4 v243, s[16:19], s61 offen lds\n"
      "v_cvt_pk_bf16_f32 v152, v152, v156\n"
      "v_cvt_pk_bf16_f32 v153, v153, v157\n"
      "v_cvt_pk_bf16_f32 v154, v154, v158\n"
      "v_cvt_pk_bf16_f32 v155, v155, v159\n"
      "s_add_u32 s62, s37, 0\n"
      "buffer_store_dword v152, v247, s[68:71], s62 offen sc1\n"
      "s_add_u32 s62, s37, 9216\n"
      "buffer_store_dword v153, v247, s[68:71], s62 offen sc1\n"
      "s_add_u32 s62, s37, 18432\n"
      "buffer_store_dword v154, v247, s[68:71], s62 offen sc1\n"
      "s_add_u32 s62, s37, 27648\n"
      "buffer_store_dword v155, v247, s[68:71], s62 offen sc1\n"
      "s_waitcnt vmcnt(9)\n"
      "s_barrier\n"
      "s_waitcnt lgkmcnt(5)\n"
      "v_mfma_f32_16x16x32_bf16 v[160:163], v[180:183], v[80:83], v[160:163]\n"
      "ds_read_b128 v[172:175], v241 offset:51200\n"
      "v_mfma_f32_16x16x32_bf16 v[164:167], v[180:183], v[84:87], v[164:167]\n"
      "s_waitcnt lgkmcnt(5)\n"
      "v_mfma_f32_16x16x32_bf16 v[160:163], v[184:187], v[88:91], v[160:163]\n"
      "ds_read_b128 v[176:179], v240 offset:53248\n"
      "v_mfma_f32_16x16x32_bf16 v[164:167], v[184:187], v[92:95], v[164:167]\n"
      "s_waitcnt lgkmcnt(5)\n"
      "v_mfma_f32_16x16x32_bf16 v[160:163], v[188:191], v[96:99], v[160:163]\n"
      "ds_read_b128 v[180:183], v241 offset:53248\n"
      "v_mfma_f32_16x16x32_bf16 v[164:167], v[188:191], v[100:103], v[164:167]\n"
      "s_waitcnt lgkmcnt(5)\n"
      "v_mfma_f32_16x16x32_bf16 v[160:163], v[192:195], v[104:107], v[160:163]\n"
      "ds_read_b128 v[184:187], v214 offset:8192\n"
      "v_mfma_f32_16x16x32_bf16 v[164:167], v[192:195], v[108:111], v[164:167]\n"
      "s_waitcnt lgkmcnt(5)\n"
      "v_mfma_f32_16x16x32_bf16 v[160:163], v[196:199], v[112:115], v[160:163]\n"
      "ds_read_b128 v[188:191], v215 offset:8192\n"
      "v_mfma_f32_16x16x32_bf16 v[164:167], v[196:199], v[116:119], v[164:167]\n"
      "s_waitcnt lgkmcnt(5)\n"
      "v_mfma_f32_16x16x32_bf16 v[160:163], v[168:171], v[120:123], v[160:163]\n"
      "ds_read_b128 v[192:195], v214 offset:10240\n"
      "v_mfma_f32_16x16x32_bf16 v[164:167], v[168:171], v[124:127], v[164:167]\n"
      "s_waitcnt lgkmcnt(5)\n"
      "v_mfma_f32_16x16x32_bf16 v[160:163], v[172:175], v[128:131], v[160:163]\n"
      "ds_read_b128 v[196:199], v215 offset:10240\n"
      "v_mfma_f32_16x16x32_bf16 v[164:167], v[172:175], v[132:135], v[164:167]\n"
      "s_waitcnt lgkmcnt(5)\n"
      "v_mfma_f32_16x16x32_bf16 v[160:163], v[176:179], v[136:139], v[160:163]\n"
      "ds_read_b128 v[168:171], v214 offset:12288\n"
      "v_mfma_f32_16x16x32_bf16 v[164:167], v[176:179], v[140:143], v[164:167]\n"
      "s_waitcnt lgkmcnt(5)\n"
      "v_mfma_f32_16x16x32_bf16 v[160:163], v[180:183], v[144:147], v[160:163]\n"
      "ds_read_b128 v[172:175], v215 offset:12288\n"
      "v_mfma_f32_16x16x32_bf16 v[164:167], v[180:183], v[148:151], v[164:167]\n"
      "s_waitcnt lgkmcnt(5)\n"
      "v_mfma_f32_16x16x32_bf16 v[152:155], v[184:187], v[8:11], 0\n"
      "ds_read_b128 v[176:179], v214 offset:14336\n"
      "v_mfma_f32_16x16x32_bf16 v[156:159], v[184:187], v[12:15], 0\n"
      "s_waitcnt lgkmcnt(5)\n"
      "v_mfma_f32_16x16x32_bf16 v[152:155], v[188:191], v[16:19], v[152:155]\n"
      "ds_read_b128 v[180:183], v215 offset:14336\n"
      "v_mfma_f32_16x16x32_bf16 v[156:159], v[188:191], v[20:23], v[156:159]\n"
      "s_waitcnt lgkmcnt(5)\n"
      "v_mfma_f32_16x16x32_bf16 v[152:155], v[192:195], v[24:27], v[152:155]\n"
      "ds_read_b128 v[184:187], v214 offset:16384\n"
      "v_mfma_f32_16x16x32_bf16 v[156:159], v[192:195], v[28:31], v[156:159]\n"
      "s_waitcnt lgkmcnt(5)\n"
      "v_mfma_f32_16x16x32_bf16 v[152:155], v[196:199], v[32:35], v[152:155]\n"
      "ds_read_b128 v[188:191], v215 offset:16384\n"
      "v_mfma_f32_16x16x32_bf16 v[156:159], v[196:199], v[36:39], v[156:159]\n"
      "s_waitcnt lgkmcnt(5)\n"
      "v_mfma_f32_16x16x32_bf16 v[152:155], v[168:171], v[40:43], v[152:155]\n"
      "ds_read_b128 v[192:195], v214 offset:18432\n"
      "v_mfma_f32_16x16x32_bf16 v[156:159], v[168:171], v[44:47], v[156:159]\n"
      "s_waitcnt lgkmcnt(5)\n"
      "v_mfma_f32_16x16x32_bf16 v[152:155], v[172:175], v[48:51], v[152:155]\n"
      "ds_read_b128 v[196:199], v215 offset:18432\n"
      "v_mfma_f32_16x16x32_bf16 v[156:159], v[172:175], v[52:55], v[156:159]\n"
      "s_waitcnt lgkmcnt(5)\n"
      "v_mfma_f32_16x16x32_bf16 v[152:155], v[176:179], v[56:59], v[152:155]\n"
      "ds_read_b128 v[168:171], v214 offset:20480\n"
      "v_mfma_f32_16x16x32_bf16 v[156:159], v[176:179], v[60:63], v[156:159]\n"
      "s_waitcnt lgkmcnt(5)\n"
      "v_mfma_f32_16x16x32_bf16 v[152:155], v[180:183], v[64:67], v[152:155]\n"
      "ds_read_b128 v[172:175], v215 offset:20480\n"
      "v_mfma_f32_16x16x32_bf16 v[156:159], v[180:183], v[68:71], v[156:159]\n"
      "s_waitcnt lgkmcnt(5)\n"
      "v_mfma_f32_16x16x32_bf16 v[152:155], v[184:187], v[72:75], v[152:155]\n"
      "ds_read_b128 v[176:179], v214 offset:22528\n"
      "v_mfma_f32_16x16x32_bf16 v[156:159], v[184:187], v[76:79], v[156:159]\n"
      "s_add_u32 m0, s43, 0\n"
      "s_add_u32 s61, s36, 147456\n"
      "buffer_load_dwordx4 v243, s[16:19], s61 offen lds\n"
      "s_add_u32 m0, s43, 2048\n"
      "s_add_u32 s61, s36, 147584\n"
      "buffer_load_dwordx4 v243, s[16:19], s61 offen lds\n"
      "s_add_u32 m0, s43, 4096\n"
      "s_add_u32 s61, s36, 147712\n"
      "buffer_load_dwordx4 v243, s[16:19], s61 offen lds\n"
      "s_add_u32 m0, s43, 6144\n"
      "s_add_u32 s61, s36, 147840\n"
      "buffer_load_dwordx4 v243, s[16:19], s61 offen lds\n"
      "s_add_u32 m0, s43, 8192\n"
      "s_add_u32 s61, s36, 147968\n"
      "buffer_load_dwordx4 v243, s[16:19], s61 offen lds\n"
      "v_cvt_pk_bf16_f32 v160, v160, v164\n"
      "v_cvt_pk_bf16_f32 v161, v161, v165\n"
      "v_cvt_pk_bf16_f32 v162, v162, v166\n"
      "v_cvt_pk_bf16_f32 v163, v163, v167\n"
      "s_add_u32 s62, s37, 294912\n"
      "buffer_store_dword v160, v247, s[68:71], s62 offen sc1\n"
      "s_add_u32 s62, s37, 304128\n"
      "buffer_store_dword v161, v247, s[68:71], s62 offen sc1\n"
      "s_add_u32 s62, s37, 313344\n"
      "buffer_store_dword v162, v247, s[68:71], s62 offen sc1\n"
      "s_add_u32 s62, s37, 322560\n"
      "buffer_store_dword v163, v247, s[68:71], s62 offen sc1\n"
      "s_waitcnt vmcnt(13)\n"
      "s_barrier\n"
      "s_waitcnt lgkmcnt(5)\n"
      "v_mfma_f32_16x16x32_bf16 v[152:155], v[188:191], v[80:83], v[152:155]\n"
      "ds_read_b128 v[180:183], v215 offset:22528\n"
      "v_mfma_f32_16x16x32_bf16 v[156:159], v[188:191], v[84:87], v[156:159]\n"
      "s_waitcnt lgkmcnt(5)\n"
      "v_mfma_f32_16x16x32_bf16 v[152:155], v[192:195], v[88:91], v[152:155]\n"
      "ds_read_b128 v[184:187], v214 offset:24576\n"
      "v_mfma_f32_16x16x32_bf16 v[156:159], v[192:195], v[92:95], v[156:159]\n"
      "s_waitcnt lgkmcnt(5)\n"
      "v_mfma_f32_16x16x32_bf16 v[152:155], v[196:199], v[96:99], v[152:155]\n"
      "ds_read_b128 v[188:191], v215 offset:24576\n"
      "v_mfma_f32_16x16x32_bf16 v[156:159], v[196:199], v[100:103], v[156:159]\n"
      "s_waitcnt lgkmcnt(5)\n"
      "v_mfma_f32_16x16x32_bf16 v[152:155], v[168:171], v[104:107], v[152:155]\n"
      "ds_read_b128 v[192:195], v214 offset:45056\n"
      "v_mfma_f32_16x16x32_bf16 v[156:159], v[168:171], v[108:111], v[156:159]\n"
      "s_waitcnt lgkmcnt(5)\n"
      "v_mfma_f32_16x16x32_bf16 v[152:155], v[172:175], v[112:115], v[152:155]\n"
      "ds_read_b128 v[196:199], v215 offset:45056\n"
      "v_mfma_f32_16x16x32_bf16 v[156:159], v[172:175], v[116:119], v[156:159]\n"
      "s_waitcnt lgkmcnt(5)\n"
      "v_mfma_f32_16x16x32_bf16 v[152:155], v[176:179], v[120:123], v[152:155]\n"
      "ds_read_b128 v[168:171], v214 offset:47104\n"
      "v_mfma_f32_16x16x32_bf16 v[156:159], v[176:179], v[124:127], v[156:159]\n"
      "s_waitcnt lgkmcnt(5)\n"
      "v_mfma_f32_16x16x32_bf16 v[152:155], v[180:183], v[128:131], v[152:155]\n"
      "ds_read_b128 v[172:175], v215 offset:47104\n"
      "v_mfma_f32_16x16x32_bf16 v[156:159], v[180:183], v[132:135], v[156:159]\n"
      "s_waitcnt lgkmcnt(5)\n"
      "v_mfma_f32_16x16x32_bf16 v[152:155], v[184:187], v[136:139], v[152:155]\n"
      "ds_read_b128 v[176:179], v214 offset:49152\n"
      "v_mfma_f32_16x16x32_bf16 v[156:159], v[184:187], v[140:143], v[156:159]\n"
      "s_waitcnt lgkmcnt(5)\n"
      "v_mfma_f32_16x16x32_bf16 v[152:155], v[188:191], v[144:147], v[152:155]\n"
      "ds_read_b128 v[180:183], v215 offset:49152\n"
      "v_mfma_f32_16x16x32_bf16 v[156:159], v[188:191], v[148:151], v[156:159]\n"
      "s_waitcnt lgkmcnt(5)\n"
      "v_mfma_f32_16x16x32_bf16 v[160:163], v[192:195], v[8:11], 0\n"
      "ds_read_b128 v[184:187], v214 offset:51200\n"
      "v_mfma_f32_16x16x32_bf16 v[164:167], v[192:195], v[12:15], 0\n"
      "s_waitcnt lgkmcnt(5)\n"
      "v_mfma_f32_16x16x32_bf16 v[160:163], v[196:199], v[16:19], v[160:163]\n"
      "ds_read_b128 v[188:191], v215 offset:51200\n"
      "v_mfma_f32_16x16x32_bf16 v[164:167], v[196:199], v[20:23], v[164:167]\n"
      "s_waitcnt lgkmcnt(5)\n"
      "v_mfma_f32_16x16x32_bf16 v[160:163], v[168:171], v[24:27], v[160:163]\n"
      "ds_read_b128 v[192:195], v214 offset:53248\n"
      "v_mfma_f32_16x16x32_bf16 v[164:167], v[168:171], v[28:31], v[164:167]\n"
      "s_waitcnt lgkmcnt(5)\n"
      "v_mfma_f32_16x16x32_bf16 v[160:163], v[172:175], v[32:35], v[160:163]\n"
      "ds_read_b128 v[196:199], v215 offset:53248\n"
      "v_mfma_f32_16x16x32_bf16 v[164:167], v[172:175], v[36:39], v[164:167]\n"
      "s_waitcnt lgkmcnt(5)\n"
      "v_mfma_f32_16x16x32_bf16 v[160:163], v[176:179], v[40:43], v[160:163]\n"
      "ds_read_b128 v[168:171], v214 offset:55296\n"
      "v_mfma_f32_16x16x32_bf16 v[164:167], v[176:179], v[44:47], v[164:167]\n"
      "s_waitcnt lgkmcnt(5)\n"
      "v_mfma_f32_16x16x32_bf16 v[160:163], v[180:183], v[48:51], v[160:163]\n"
      "ds_read_b128 v[172:175], v215 offset:55296\n"
      "v_mfma_f32_16x16x32_bf16 v[164:167], v[180:183], v[52:55], v[164:167]\n"
      "s_waitcnt lgkmcnt(5)\n"
      "v_mfma_f32_16x16x32_bf16 v[160:163], v[184:187], v[56:59], v[160:163]\n"
      "ds_read_b128 v[176:179], v214 offset:57344\n"
      "v_mfma_f32_16x16x32_bf16 v[164:167], v[184:187], v[60:63], v[164:167]\n"
      "s_waitcnt lgkmcnt(5)\n"
      "v_mfma_f32_16x16x32_bf16 v[160:163], v[188:191], v[64:67], v[160:163]\n"
      "ds_read_b128 v[180:183], v215 offset:57344\n"
      "v_mfma_f32_16x16x32_bf16 v[164:167], v[188:191], v[68:71], v[164:167]\n"
      "s_waitcnt lgkmcnt(5)\n"
      "v_mfma_f32_16x16x32_bf16 v[160:163], v[192:195], v[72:75], v[160:163]\n"
      "ds_read_b128 v[184:187], v214 offset:59392\n"
      "v_mfma_f32_16x16x32_bf16 v[164:167], v[192:195], v[76:79], v[164:167]\n"
      "s_add_u32 m0, s43, 36864\n"
      "s_add_u32 s61, s36, 184320\n"
      "buffer_load_dwordx4 v243, s[16:19], s61 offen lds\n"
      "s_add_u32 m0, s43, 38912\n"
      "s_add_u32 s61, s36, 184448\n"
      "buffer_load_dwordx4 v243, s[16:19], s61 offen lds\n"
      "s_add_u32 m0, s43, 40960\n"
      "s_add_u32 s61, s36, 184576\n"
      "buffer_load_dwordx4 v243, s[16:19], s61 offen lds\n"
      "s_add_u32 m0, s43, 43008\n"
      "s_add_u32 s61, s36, 184704\n"
      "buffer_load_dwordx4 v243, s[16:19], s61 offen lds\n"
      "s_add_u32 m0, s43, 45056\n"
      "s_add_u32 s61, s36, 184832\n"
      "buffer_load_dwordx4 v243, s[16:19], s61 offen lds\n"
      "v_cvt_pk_bf16_f32 v152, v152, v156\n"
      "v_cvt_pk_bf16_f32 v153, v153, v157\n"
      "v_cvt_pk_bf16_f32 v154, v154, v158\n"
      "v_cvt_pk_bf16_f32 v155, v155, v159\n"
      "s_add_u32 s62, s37, 589824\n"
      "buffer_store_dword v152, v247, s[68:71], s62 offen sc1\n"
      "s_add_u32 s62, s37, 599040\n"
      "buffer_store_dword v153, v247, s[68:71], s62 offen sc1\n"
      "s_add_u32 s62, s37, 608256\n"
      "buffer_store_dword v154, v247, s[68:71], s62 offen sc1\n"
      "s_add_u32 s62, s37, 617472\n"
      "buffer_store_dword v155, v247, s[68:71], s62 offen sc1\n"
      "s_waitcnt vmcnt(13)\n"
      "s_barrier\n"
      "s_waitcnt lgkmcnt(5)\n"
      "v_mfma_f32_16x16x32_bf16 v[160:163], v[196:199], v[80:83], v[160:163]\n"
      "ds_read_b128 v[188:191], v215 offset:59392\n"
      "v_mfma_f32_16x16x32_bf16 v[164:167], v[196:199], v[84:87], v[164:167]\n"
      "s_waitcnt lgkmcnt(5)\n"
      "v_mfma_f32_16x16x32_bf16 v[160:163], v[168:171], v[88:91], v[160:163]\n"
      "ds_read_b128 v[192:195], v214 offset:61440\n"
      "v_mfma_f32_16x16x32_bf16 v[164:167], v[168:171], v[92:95], v[164:167]\n"
      "s_waitcnt lgkmcnt(5)\n"
      "v_mfma_f32_16x16x32_bf16 v[160:163], v[172:175], v[96:99], v[160:163]\n"
      "ds_read_b128 v[196:199], v215 offset:61440\n"
      "v_mfma_f32_16x16x32_bf16 v[164:167], v[172:175], v[100:103], v[164:167]\n"
      "s_waitcnt lgkmcnt(5)\n"
      "v_mfma_f32_16x16x32_bf16 v[160:163], v[176:179], v[104:107], v[160:163]\n"
      "ds_read_b128 v[168:171], v240 offset:0\n"
      "v_mfma_f32_16x16x32_bf16 v[164:167], v[176:179], v[108:111], v[164:167]\n"
      "s_waitcnt lgkmcnt(5)\n"
      "v_mfma_f32_16x16x32_bf16 v[160:163], v[180:183], v[112:115], v[160:163]\n"
      "ds_read_b128 v[172:175], v241 offset:0\n"
      "v_mfma_f32_16x16x32_bf16 v[164:167], v[180:183], v[116:119], v[164:167]\n"
      "s_waitcnt lgkmcnt(5)\n"
      "v_mfma_f32_16x16x32_bf16 v[160:163], v[184:187], v[120:123], v[160:163]\n"
      "ds_read_b128 v[176:179], v240 offset:2048\n"
      "v_mfma_f32_16x16x32_bf16 v[164:167], v[184:187], v[124:127], v[164:167]\n"
      "s_waitcnt lgkmcnt(5)\n"
      "v_mfma_f32_16x16x32_bf16 v[160:163], v[188:191], v[128:131], v[160:163]\n"
      "ds_read_b128 v[180:183], v241 offset:2048\n"
      "v_mfma_f32_16x16x32_bf16 v[164:167], v[188:191], v[132:135], v[164:167]\n"
      "s_waitcnt lgkmcnt(5)\n"
      "v_mfma_f32_16x16x32_bf16 v[160:163], v[192:195], v[136:139], v[160:163]\n"
      "ds_read_b128 v[184:187], v240 offset:4096\n"
      "v_mfma_f32_16x16x32_bf16 v[164:167], v[192:195], v[140:143], v[164:167]\n"
      "s_waitcnt lgkmcnt(5)\n"
      "v_mfma_f32_16x16x32_bf16 v[160:163], v[196:199], v[144:147], v[160:163]\n"
      "ds_read_b128 v[188:191], v241 offset:4096\n"
      "v_mfma_f32_16x16x32_bf16 v[164:167], v[196:199], v[148:151], v[164:167]\n"
      "s_waitcnt lgkmcnt(5)\n"
      "v_mfma_f32_16x16x32_bf16 v[152:155], v[168:171], v[8:11], 0\n"
      "ds_read_b128 v[192:195], v240 offset:6144\n"
      "v_mfma_f32_16x16x32_bf16 v[156:159], v[168:171], v[12:15], 0\n"
      "s_waitcnt lgkmcnt(5)\n"
      "v_mfma_f32_16x16x32_bf16 v[152:155], v[172:175], v[16:19], v[152:155]\n"
      "ds_read_b128 v[196:199], v241 offset:6144\n"
      "v_mfma_f32_16x16x32_bf16 v[156:159], v[172:175], v[20:23], v[156:159]\n"
      "s_waitcnt lgkmcnt(5)\n"
      "v_mfma_f32_16x16x32_bf16 v[152:155], v[176:179], v[24:27], v[152:155]\n"
      "ds_read_b128 v[168:171], v240 offset:8192\n"
      "v_mfma_f32_16x16x32_bf16 v[156:159], v[176:179], v[28:31], v[156:159]\n"
      "s_waitcnt lgkmcnt(5)\n"
      "v_mfma_f32_16x16x32_bf16 v[152:155], v[180:183], v[32:35], v[152:155]\n"
      "ds_read_b128 v[172:175], v241 offset:8192\n"
      "v_mfma_f32_16x16x32_bf16 v[156:159], v[180:183], v[36:39], v[156:159]\n"
      "s_waitcnt lgkmcnt(5)\n"
      "v_mfma_f32_16x16x32_bf16 v[152:155], v[184:187], v[40:43], v[152:155]\n"
      "ds_read_b128 v[176:179], v240 offset:10240\n"
      "v_mfma_f32_16x16x32_bf16 v[156:159], v[184:187], v[44:47], v[156:159]\n"
      "s_waitcnt lgkmcnt(5)\n"
      "v_mfma_f32_16x16x32_bf16 v[152:155], v[188:191], v[48:51], v[152:155]\n"
      "ds_read_b128 v[180:183], v241 offset:10240\n"
      "v_mfma_f32_16x16x32_bf16 v[156:159], v[188:191], v[52:55], v[156:159]\n"
      "s_waitcnt lgkmcnt(5)\n"
      "v_mfma_f32_16x16x32_bf16 v[152:155], v[192:195], v[56:59], v[152:155]\n"
      "ds_read_b128 v[184:187], v240 offset:12288\n"
      "v_mfma_f32_16x16x32_bf16 v[156:159], v[192:195], v[60:63], v[156:159]\n"
      "s_waitcnt lgkmcnt(5)\n"
      "v_mfma_f32_16x16x32_bf16 v[152:155], v[196:199], v[64:67], v[152:155]\n"
      "ds_read_b128 v[188:191], v241 offset:12288\n"
      "v_mfma_f32_16x16x32_bf16 v[156:159], v[196:199], v[68:71], v[156:159]\n"
      "s_waitcnt lgkmcnt(5)\n"
      "v_mfma_f32_16x16x32_bf16 v[152:155], v[168:171], v[72:75], v[152:155]\n"
      "ds_read_b128 v[192:195], v240 offset:14336\n"
      "v_mfma_f32_16x16x32_bf16 v[156:159], v[168:171], v[76:79], v[156:159]\n"
      "s_add_u32 m0, s43, 73728\n"
      "s_add_u32 s61, s36, 221184\n"
      "buffer_load_dwordx4 v243, s[16:19], s61 offen lds\n"
      "s_add_u32 m0, s43, 75776\n"
      "s_add_u32 s61, s36, 221312\n"
      "buffer_load_dwordx4 v243, s[16:19], s61 offen lds\n"
      "s_add_u32 m0, s43, 77824\n"
      "s_add_u32 s61, s36, 221440\n"
      "buffer_load_dwordx4 v243, s[16:19], s61 offen lds\n"
      "s_add_u32 m0, s43, 79872\n"
      "s_add_u32 s61, s36, 221568\n"
      "buffer_load_dwordx4 v243, s[16:19], s61 offen lds\n"
      "s_add_u32 m0, s43, 81920\n"
      "s_add_u32 s61, s36, 221696\n"
      "buffer_load_dwordx4 v243, s[16:19], s61 offen lds\n"
      "v_cvt_pk_bf16_f32 v160, v160, v164\n"
      "v_cvt_pk_bf16_f32 v161, v161, v165\n"
      "v_cvt_pk_bf16_f32 v162, v162, v166\n"
      "v_cvt_pk_bf16_f32 v163, v163, v167\n"
      "s_add_u32 s62, s37, 884736\n"
      "buffer_store_dword v160, v247, s[68:71], s62 offen sc1\n"
      "s_add_u32 s62, s37, 893952\n"
      "buffer_store_dword v161, v247, s[68:71], s62 offen sc1\n"
      "s_add_u32 s62, s37, 903168\n"
      "buffer_store_dword v162, v247, s[68:71], s62 offen sc1\n"
      "s_add_u32 s62, s37, 912384\n"
      "buffer_store_dword v163, v247, s[68:71], s62 offen sc1\n"
      "s_waitcnt vmcnt(13)\n"
      "s_barrier\n"
      "s_waitcnt lgkmcnt(5)\n"
      "v_mfma_f32_16x16x32_bf16 v[152:155], v[172:175], v[80:83], v[152:155]\n"
      "ds_read_b128 v[196:199], v241 offset:14336\n"
      "v_mfma_f32_16x16x32_bf16 v[156:159], v[172:175], v[84:87], v[156:159]\n"
      "s_waitcnt lgkmcnt(5)\n"
      "v_mfma_f32_16x16x32_bf16 v[152:155], v[176:179], v[88:91], v[152:155]\n"
      "ds_read_b128 v[168:171], v240 offset:16384\n"
      "v_mfma_f32_16x16x32_bf16 v[156:159], v[176:179], v[92:95], v[156:159]\n"
      "s_waitcnt lgkmcnt(5)\n"
      "v_mfma_f32_16x16x32_bf16 v[152:155], v[180:183], v[96:99], v[152:155]\n"
      "ds_read_b128 v[172:175], v241 offset:16384\n"
      "v_mfma_f32_16x16x32_bf16 v[156:159], v[180:183], v[100:103], v[156:159]\n"
      "s_waitcnt lgkmcnt(5)\n"
      "v_mfma_f32_16x16x32_bf16 v[152:155], v[184:187], v[104:107], v[152:155]\n"
      "ds_read_b128 v[176:179], v240 offset:36864\n"
      "v_mfma_f32_16x16x32_bf16 v[156:159], v[184:187], v[108:111], v[156:159]\n"
      "s_waitcnt lgkmcnt(5)\n"
      "v_mfma_f32_16x16x32_bf16 v[152:155], v[188:191], v[112:115], v[152:155]\n"
      "ds_read_b128 v[180:183], v241 offset:36864\n"
      "v_mfma_f32_16x16x32_bf16 v[156:159], v[188:191], v[116:119], v[156:159]\n"
      "s_waitcnt lgkmcnt(5)\n"
      "v_mfma_f32_16x16x32_bf16 v[152:155], v[192:195], v[120:123], v[152:155]\n"
      "ds_read_b128 v[184:187], v240 offset:38912\n"
      "v_mfma_f32_16x16x32_bf16 v[156:159], v[192:195], v[124:127], v[156:159]\n"
      "s_waitcnt lgkmcnt(5)\n"
      "v_mfma_f32_16x16x32_bf16 v[152:155], v[196:199], v[128:131], v[152:155]\n"
      "ds_read_b128 v[188:191], v241 offset:38912\n"
      "v_mfma_f32_16x16x32_bf16 v[156:159], v[196:199], v[132:135], v[156:159]\n"
      "s_waitcnt lgkmcnt(5)\n"
      "v_mfma_f32_16x16x32_bf16 v[152:155], v[168:171], v[136:139], v[152:155]\n"
      "ds_read_b128 v[192:195], v240 offset:40960\n"
      "v_mfma_f32_16x16x32_bf16 v[156:159], v[168:171], v[140:143], v[156:159]\n"
      "s_waitcnt lgkmcnt(5)\n"
      "v_mfma_f32_16x16x32_bf16 v[152:155], v[172:175], v[144:147], v[152:155]\n"
      "ds_read_b128 v[196:199], v241 offset:40960\n"
      "v_mfma_f32_16x16x32_bf16 v[156:159], v[172:175], v[148:151], v[156:159]\n"
      "s_waitcnt lgkmcnt(5)\n"
      "v_mfma_f32_16x16x32_bf16 v[160:163], v[176:179], v[8:11], 0\n"
      "ds_read_b128 v[168:171], v240 offset:43008\n"
      "v_mfma_f32_16x16x32_bf16 v[164:167], v[176:179], v[12:15], 0\n"
      "s_waitcnt lgkmcnt(5)\n"
      "v_mfma_f32_16x16x32_bf16 v[160:163], v[180:183], v[16:19], v[160:163]\n"
      "ds_read_b128 v[172:175], v241 offset:43008\n"
      "v_mfma_f32_16x16x32_bf16 v[164:167], v[180:183], v[20:23], v[164:167]\n"
      "s_waitcnt lgkmcnt(5)\n"
      "v_mfma_f32_16x16x32_bf16 v[160:163], v[184:187], v[24:27], v[160:163]\n"
      "ds_read_b128 v[176:179], v240 offset:45056\n"
      "v_mfma_f32_16x16x32_bf16 v[164:167], v[184:187], v[28:31], v[164:167]\n"
      "s_waitcnt lgkmcnt(5)\n"
      "v_mfma_f32_16x16x32_bf16 v[160:163], v[188:191], v[32:35], v[160:163]\n"
      "ds_read_b128 v[180:183], v241 offset:45056\n"
      "v_mfma_f32_16x16x32_bf16 v[164:167], v[188:191], v[36:39], v[164:167]\n"
      "s_waitcnt lgkmcnt(5)\n"
      "v_mfma_f32_16x16x32_bf16 v[160:163], v[192:195], v[40:43], v[160:163]\n"
      "ds_read_b128 v[184:187], v240 offset:47104\n"
      "v_mfma_f32_16x16x32_bf16 v[164:167], v[192:195], v[44:47], v[164:167]\n"
      "s_waitcnt lgkmcnt(5)\n"
      "v_mfma_f32_16x16x32_bf16 v[160:163], v[196:199], v[48:51], v[160:163]\n"
      "ds_read_b128 v[188:191], v241 offset:47104\n"
      "v_mfma_f32_16x16x32_bf16 v[164:167], v[196:199], v[52:55], v[164:167]\n"
      "s_waitcnt lgkmcnt(5)\n"
      "v_mfma_f32_16x16x32_bf16 v[160:163], v[168:171], v[56:59], v[160:163]\n"
      "ds_read_b128 v[192:195], v240 offset:49152\n"
      "v_mfma_f32_16x16x32_bf16 v[164:167], v[168:171], v[60:63], v[164:167]\n"
      "s_waitcnt lgkmcnt(5)\n"
      "v_mfma_f32_16x16x32_bf16 v[160:163], v[172:175], v[64:67], v[160:163]\n"
      "ds_read_b128 v[196:199], v241 offset:49152\n"
      "v_mfma_f32_16x16x32_bf16 v[164:167], v[172:175], v[68:71], v[164:167]\n"
      "s_waitcnt lgkmcnt(5)\n"
      "v_mfma_f32_16x16x32_bf16 v[160:163], v[176:179], v[72:75], v[160:163]\n"
      "ds_read_b128 v[168:171], v240 offset:51200\n"
      "v_mfma_f32_16x16x32_bf16 v[164:167], v[176:179], v[76:79], v[164:167]\n"
      "s_add_u32 m0, s43, 110592\n"
      "s_add_u32 s61, s36, 258048\n"
      "buffer_load_dwordx4 v243, s[16:19], s61 offen lds\n"
      "s_add_u32 m0, s43, 112640\n"
      "s_add_u32 s61, s36, 258176\n"
      "buffer_load_dwordx4 v243, s[16:19], s61 offen lds\n"
      "s_add_u32 m0, s43, 114688\n"
      "s_add_u32 s61, s36, 258304\n"
      "buffer_load_dwordx4 v243, s[16:19], s61 offen lds\n"
      "s_add_u32 m0, s43, 116736\n"
      "s_add_u32 s61, s36, 258432\n"
      "buffer_load_dwordx4 v243, s[16:19], s61 offen lds\n"
      "s_add_u32 m0, s43, 118784\n"
      "s_add_u32 s61, s36, 258560\n"
      "buffer_load_dwordx4 v243, s[16:19], s61 offen lds\n"
      "v_cvt_pk_bf16_f32 v152, v152, v156\n"
      "v_cvt_pk_bf16_f32 v153, v153, v157\n"
      "v_cvt_pk_bf16_f32 v154, v154, v158\n"
      "v_cvt_pk_bf16_f32 v155, v155, v159\n"
      "s_add_u32 s62, s37, 1179648\n"
      "buffer_store_dword v152, v247, s[68:71], s62 offen sc1\n"
      "s_add_u32 s62, s37, 1188864\n"
      "buffer_store_dword v153, v247, s[68:71], s62 offen sc1\n"
      "s_add_u32 s62, s37, 1198080\n"
      "buffer_store_dword v154, v247, s[68:71], s62 offen sc1\n"
      "s_add_u32 s62, s37, 1207296\n"
      "buffer_store_dword v155, v247, s[68:71], s62 offen sc1\n"
      "s_waitcnt vmcnt(13)\n"
      "s_barrier\n"
      "s_waitcnt lgkmcnt(5)\n"
      "v_mfma_f32_16x16x32_bf16 v[160:163], v[180:183], v[80:83], v[160:163]\n"
      "ds_read_b128 v[172:175], v241 offset:51200\n"
      "v_mfma_f32_16x16x32_bf16 v[164:167], v[180:183], v[84:87], v[164:167]\n"
      "s_waitcnt lgkmcnt(5)\n"
      "v_mfma_f32_16x16x32_bf16 v[160:163], v[184:187], v[88:91], v[160:163]\n"
      "ds_read_b128 v[176:179], v240 offset:53248\n"
      "v_mfma_f32_16x16x32_bf16 v[164:167], v[184:187], v[92:95], v[164:167]\n"
      "s_waitcnt lgkmcnt(5)\n"
      "v_mfma_f32_16x16x32_bf16 v[160:163], v[188:191], v[96:99], v[160:163]\n"
      "ds_read_b128 v[180:183], v241 offset:53248\n"
      "v_mfma_f32_16x16x32_bf16 v[164:167], v[188:191], v[100:103], v[164:167]\n"
      "s_waitcnt lgkmcnt(5)\n"
      "v_mfma_f32_16x16x32_bf16 v[160:163], v[192:195], v[104:107], v[160:163]\n"
      "ds_read_b128 v[184:187], v214 offset:8192\n"
      "v_mfma_f32_16x16x32_bf16 v[164:167], v[192:195], v[108:111], v[164:167]\n"
      "s_waitcnt lgkmcnt(5)\n"
      "v_mfma_f32_16x16x32_bf16 v[160:163], v[196:199], v[112:115], v[160:163]\n"
      "ds_read_b128 v[188:191], v215 offset:8192\n"
      "v_mfma_f32_16x16x32_bf16 v[164:167], v[196:199], v[116:119], v[164:167]\n"
      "s_waitcnt lgkmcnt(5)\n"
      "v_mfma_f32_16x16x32_bf16 v[160:163], v[168:171], v[120:123], v[160:163]\n"
      "ds_read_b128 v[192:195], v214 offset:10240\n"
      "v_mfma_f32_16x16x32_bf16 v[164:167], v[168:171], v[124:127], v[164:167]\n"
      "s_waitcnt lgkmcnt(5)\n"
      "v_mfma_f32_16x16x32_bf16 v[160:163], v[172:175], v[128:131], v[160:163]\n"
      "ds_read_b128 v[196:199], v215 offset:10240\n"
      "v_mfma_f32_16x16x32_bf16 v[164:167], v[172:175], v[132:135], v[164:167]\n"
      "s_waitcnt lgkmcnt(5)\n"
      "v_mfma_f32_16x16x32_bf16 v[160:163], v[176:179], v[136:139], v[160:163]\n"
      "ds_read_b128 v[168:171], v214 offset:12288\n"
      "v_mfma_f32_16x16x32_bf16 v[164:167], v[176:179], v[140:143], v[164:167]\n"
      "s_waitcnt lgkmcnt(5)\n"
      "v_mfma_f32_16x16x32_bf16 v[160:163], v[180:183], v[144:147], v[160:163]\n"
      "ds_read_b128 v[172:175], v215 offset:12288\n"
      "v_mfma_f32_16x16x32_bf16 v[164:167], v[180:183], v[148:151], v[164:167]\n"
      "s_waitcnt lgkmcnt(5)\n"
      "v_mfma_f32_16x16x32_bf16 v[152:155], v[184:187], v[8:11], 0\n"
      "ds_read_b128 v[176:179], v214 offset:14336\n"
      "v_mfma_f32_16x16x32_bf16 v[156:159], v[184:187], v[12:15], 0\n"
      "s_waitcnt lgkmcnt(5)\n"
      "v_mfma_f32_16x16x32_bf16 v[152:155], v[188:191], v[16:19], v[152:155]\n"
      "ds_read_b128 v[180:183], v215 offset:14336\n"
      "v_mfma_f32_16x16x32_bf16 v[156:159], v[188:191], v[20:23], v[156:159]\n"
      "s_waitcnt lgkmcnt(5)\n"
      "v_mfma_f32_16x16x32_bf16 v[152:155], v[192:195], v[24:27], v[152:155]\n"
      "ds_read_b128 v[184:187], v214 offset:16384\n"
      "v_mfma_f32_16x16x32_bf16 v[156:159], v[192:195], v[28:31], v[156:159]\n"
      "s_waitcnt lgkmcnt(5)\n"
      "v_mfma_f32_16x16x32_bf16 v[152:155], v[196:199], v[32:35], v[152:155]\n"
      "ds_read_b128 v[188:191], v215 offset:16384\n"
      "v_mfma_f32_16x16x32_bf16 v[156:159], v[196:199], v[36:39], v[156:159]\n"
      "s_waitcnt lgkmcnt(5)\n"
      "v_mfma_f32_16x16x32_bf16 v[152:155], v[168:171], v[40:43], v[152:155]\n"
      "ds_read_b128 v[192:195], v214 offset:18432\n"
      "v_mfma_f32_16x16x32_bf16 v[156:159], v[168:171], v[44:47], v[156:159]\n"
      "s_waitcnt lgkmcnt(5)\n"
      "v_mfma_f32_16x16x32_bf16 v[152:155], v[172:175], v[48:51], v[152:155]\n"
      "ds_read_b128 v[196:199], v215 offset:18432\n"
      "v_mfma_f32_16x16x32_bf16 v[156:159], v[172:175], v[52:55], v[156:159]\n"
      "s_waitcnt lgkmcnt(5)\n"
      "v_mfma_f32_16x16x32_bf16 v[152:155], v[176:179], v[56:59], v[152:155]\n"
      "ds_read_b128 v[168:171], v214 offset:20480\n"
      "v_mfma_f32_16x16x32_bf16 v[156:159], v[176:179], v[60:63], v[156:159]\n"
      "s_waitcnt lgkmcnt(5)\n"
      "v_mfma_f32_16x16x32_bf16 v[152:155], v[180:183], v[64:67], v[152:155]\n"
      "ds_read_b128 v[172:175], v215 offset:20480\n"
      "v_mfma_f32_16x16x32_bf16 v[156:159], v[180:183], v[68:71], v[156:159]\n"
      "s_waitcnt lgkmcnt(5)\n"
      "v_mfma_f32_16x16x32_bf16 v[152:155], v[184:187], v[72:75], v[152:155]\n"
      "ds_read_b128 v[176:179], v214 offset:22528\n"
      "v_mfma_f32_16x16x32_bf16 v[156:159], v[184:187], v[76:79], v[156:159]\n"
      "s_add_u32 m0, s43, 0\n"
      "s_add_u32 s61, s36, 294912\n"
      "buffer_load_dwordx4 v243, s[16:19], s61 offen lds\n"
      "s_add_u32 m0, s43, 2048\n"
      "s_add_u32 s61, s36, 295040\n"
      "buffer_load_dwordx4 v243, s[16:19], s61 offen lds\n"
      "s_add_u32 m0, s43, 4096\n"
      "s_add_u32 s61, s36, 295168\n"
      "buffer_load_dwordx4 v243, s[16:19], s61 offen lds\n"
      "s_add_u32 m0, s43, 6144\n"
      "s_add_u32 s61, s36, 295296\n"
      "buffer_load_dwordx4 v243, s[16:19], s61 offen lds\n"
      "s_add_u32 m0, s43, 8192\n"
      "s_add_u32 s61, s36, 295424\n"
      "buffer_load_dwordx4 v243, s[16:19], s61 offen lds\n"
      "v_cvt_pk_bf16_f32 v160, v160, v164\n"
      "v_cvt_pk_bf16_f32 v161, v161, v165\n"
      "v_cvt_pk_bf16_f32 v162, v162, v166\n"
      "v_cvt_pk_bf16_f32 v163, v163, v167\n"
      "s_add_u32 s62, s37, 1474560\n"
      "buffer_store_dword v160, v247, s[68:71], s62 offen sc1\n"
      "s_add_u32 s62, s37, 1483776\n"
      "buffer_store_dword v161, v247, s[68:71], s62 offen sc1\n"
      "s_add_u32 s62, s37, 1492992\n"
      "buffer_store_dword v162, v247, s[68:71], s62 offen sc1\n"
      "s_add_u32 s62, s37, 1502208\n"
      "buffer_store_dword v163, v247, s[68:71], s62 offen sc1\n"
      "s_waitcnt vmcnt(13)\n"
      "s_barrier\n"
      "s_waitcnt lgkmcnt(5)\n"
      "v_mfma_f32_16x16x32_bf16 v[152:155], v[188:191], v[80:83], v[152:155]\n"
      "ds_read_b128 v[180:183], v215 offset:22528\n"
      "v_mfma_f32_16x16x32_bf16 v[156:159], v[188:191], v[84:87], v[156:159]\n"
      "s_waitcnt lgkmcnt(5)\n"
      "v_mfma_f32_16x16x32_bf16 v[152:155], v[192:195], v[88:91], v[152:155]\n"
      "ds_read_b128 v[184:187], v214 offset:24576\n"
      "v_mfma_f32_16x16x32_bf16 v[156:159], v[192:195], v[92:95], v[156:159]\n"
      "s_waitcnt lgkmcnt(5)\n"
      "v_mfma_f32_16x16x32_bf16 v[152:155], v[196:199], v[96:99], v[152:155]\n"
      "ds_read_b128 v[188:191], v215 offset:24576\n"
      "v_mfma_f32_16x16x32_bf16 v[156:159], v[196:199], v[100:103], v[156:159]\n"
      "s_waitcnt lgkmcnt(5)\n"
      "v_mfma_f32_16x16x32_bf16 v[152:155], v[168:171], v[104:107], v[152:155]\n"
      "ds_read_b128 v[192:195], v214 offset:45056\n"
      "v_mfma_f32_16x16x32_bf16 v[156:159], v[168:171], v[108:111], v[156:159]\n"
      "s_waitcnt lgkmcnt(5)\n"
      "v_mfma_f32_16x16x32_bf16 v[152:155], v[172:175], v[112:115], v[152:155]\n"
      "ds_read_b128 v[196:199], v215 offset:45056\n"
      "v_mfma_f32_16x16x32_bf16 v[156:159], v[172:175], v[116:119], v[156:159]\n"
      "s_waitcnt lgkmcnt(5)\n"
      "v_mfma_f32_16x16x32_bf16 v[152:155], v[176:179], v[120:123], v[152:155]\n"
      "ds_read_b128 v[168:171], v214 offset:47104\n"
      "v_mfma_f32_16x16x32_bf16 v[156:159], v[176:179], v[124:127], v[156:159]\n"
      "s_waitcnt lgkmcnt(5)\n"
      "v_mfma_f32_16x16x32_bf16 v[152:155], v[180:183], v[128:131], v[152:155]\n"
      "ds_read_b128 v[172:175], v215 offset:47104\n"
      "v_mfma_f32_16x16x32_bf16 v[156:159], v[180:183], v[132:135], v[156:159]\n"
      "s_waitcnt lgkmcnt(5)\n"
      "v_mfma_f32_16x16x32_bf16 v[152:155], v[184:187], v[136:139], v[152:155]\n"
      "ds_read_b128 v[176:179], v214 offset:49152\n"
      "v_mfma_f32_16x16x32_bf16 v[156:159], v[184:187], v[140:143], v[156:159]\n"
      "s_waitcnt lgkmcnt(5)\n"
      "v_mfma_f32_16x16x32_bf16 v[152:155], v[188:191], v[144:147], v[152:155]\n"
      "ds_read_b128 v[180:183], v215 offset:49152\n"
      "v_mfma_f32_16x16x32_bf16 v[156:159], v[188:191], v[148:151], v[156:159]\n"
      "s_waitcnt lgkmcnt(5)\n"
      "v_mfma_f32_16x16x32_bf16 v[160:163], v[192:195], v[8:11], 0\n"
      "ds_read_b128 v[184:187], v214 offset:51200\n"
      "v_mfma_f32_16x16x32_bf16 v[164:167], v[192:195], v[12:15], 0\n"
      "s_waitcnt lgkmcnt(5)\n"
      "v_mfma_f32_16x16x32_bf16 v[160:163], v[196:199], v[16:19], v[160:163]\n"
      "ds_read_b128 v[188:191], v215 offset:51200\n"
      "v_mfma_f32_16x16x32_bf16 v[164:167], v[196:199], v[20:23], v[164:167]\n"
      "s_waitcnt lgkmcnt(5)\n"
      "v_mfma_f32_16x16x32_bf16 v[160:163], v[168:171], v[24:27], v[160:163]\n"
      "ds_read_b128 v[192:195], v214 offset:53248\n"
      "v_mfma_f32_16x16x32_bf16 v[164:167], v[168:171], v[28:31], v[164:167]\n"
      "s_waitcnt lgkmcnt(5)\n"
      "v_mfma_f32_16x16x32_bf16 v[160:163], v[172:175], v[32:35], v[160:163]\n"
      "ds_read_b128 v[196:199], v215 offset:53248\n"
      "v_mfma_f32_16x16x32_bf16 v[164:167], v[172:175], v[36:39], v[164:167]\n"
      "s_waitcnt lgkmcnt(5)\n"
      "v_mfma_f32_16x16x32_bf16 v[160:163], v[176:179], v[40:43], v[160:163]\n"
      "ds_read_b128 v[168:171], v214 offset:55296\n"
      "v_mfma_f32_16x16x32_bf16 v[164:167], v[176:179], v[44:47], v[164:167]\n"
      "s_waitcnt lgkmcnt(5)\n"
      "v_mfma_f32_16x16x32_bf16 v[160:163], v[180:183], v[48:51], v[160:163]\n"
      "ds_read_b128 v[172:175], v215 offset:55296\n"
      "v_mfma_f32_16x16x32_bf16 v[164:167], v[180:183], v[52:55], v[164:167]\n"
      "s_waitcnt lgkmcnt(5)\n"
      "v_mfma_f32_16x16x32_bf16 v[160:163], v[184:187], v[56:59], v[160:163]\n"
      "ds_read_b128 v[176:179], v214 offset:57344\n"
      "v_mfma_f32_16x16x32_bf16 v[164:167], v[184:187], v[60:63], v[164:167]\n"
      "s_waitcnt lgkmcnt(5)\n"
      "v_mfma_f32_16x16x32_bf16 v[160:163], v[188:191], v[64:67], v[160:163]\n"
      "ds_read_b128 v[180:183], v215 offset:57344\n"
      "v_mfma_f32_16x16x32_bf16 v[164:167], v[188:191], v[68:71], v[164:167]\n"
      "s_waitcnt lgkmcnt(5)\n"
      "v_mfma_f32_16x16x32_bf16 v[160:163], v[192:195], v[72:75], v[160:163]\n"
      "ds_read_b128 v[184:187], v214 offset:59392\n"
      "v_mfma_f32_16x16x32_bf16 v[164:167], v[192:195], v[76:79], v[164:167]\n"
      "s_cmp_lt_u32 s38, 19\n"
      "s_cbranch_scc1 Lskipd00_%=\n"
      "s_add_u32 m0, s43, 36864\n"
      "s_add_u32 s61, s36, 331776\n"
      "buffer_load_dwordx4 v243, s[16:19], s61 offen lds\n"
      "s_add_u32 m0, s43, 38912\n"
      "s_add_u32 s61, s36, 331904\n"
      "buffer_load_dwordx4 v243, s[16:19], s61 offen lds\n"
      "s_add_u32 m0, s43, 40960\n"
      "s_add_u32 s61, s36, 332032\n"
      "buffer_load_dwordx4 v243, s[16:19], s61 offen lds\n"
      "s_add_u32 m0, s43, 43008\n"
      "s_add_u32 s61, s36, 332160\n"
      "buffer_load_dwordx4 v243, s[16:19], s61 offen lds\n"
      "s_add_u32 m0, s43, 45056\n"
      "s_add_u32 s61, s36, 332288\n"
      "buffer_load_dwordx4 v243, s[16:19], s61 offen lds\n"
      "Lskipd00_%=:\n"
      "v_cvt_pk_bf16_f32 v152, v152, v156\n"
      "v_cvt_pk_bf16_f32 v153, v153, v157\n"
      "v_cvt_pk_bf16_f32 v154, v154, v158\n"
      "v_cvt_pk_bf16_f32 v155, v155, v159\n"
      "s_add_u32 s62, s37, 1769472\n"
      "buffer_store_dword v152, v247, s[68:71], s62 offen sc1\n"
      "s_add_u32 s62, s37, 1778688\n"
      "buffer_store_dword v153, v247, s[68:71], s62 offen sc1\n"
      "s_add_u32 s62, s37, 1787904\n"
      "buffer_store_dword v154, v247, s[68:71], s62 offen sc1\n"
      "s_add_u32 s62, s37, 1797120\n"
      "buffer_store_dword v155, v247, s[68:71], s62 offen sc1\n"
      "s_waitcnt vmcnt(8)\n"
      "s_barrier\n"
      "s_waitcnt lgkmcnt(5)\n"
      "v_mfma_f32_16x16x32_bf16 v[160:163], v[196:199], v[80:83], v[160:163]\n"
      "ds_read_b128 v[188:191], v215 offset:59392\n"
      "v_mfma_f32_16x16x32_bf16 v[164:167], v[196:199], v[84:87], v[164:167]\n"
      "s_waitcnt lgkmcnt(5)\n"
      "v_mfma_f32_16x16x32_bf16 v[160:163], v[168:171], v[88:91], v[160:163]\n"
      "ds_read_b128 v[192:195], v214 offset:61440\n"
      "v_mfma_f32_16x16x32_bf16 v[164:167], v[168:171], v[92:95], v[164:167]\n"
      "s_waitcnt lgkmcnt(5)\n"
      "v_mfma_f32_16x16x32_bf16 v[160:163], v[172:175], v[96:99], v[160:163]\n"
      "ds_read_b128 v[196:199], v215 offset:61440\n"
      "v_mfma_f32_16x16x32_bf16 v[164:167], v[172:175], v[100:103], v[164:167]\n"
      "s_waitcnt lgkmcnt(5)\n"
      "v_mfma_f32_16x16x32_bf16 v[160:163], v[176:179], v[104:107], v[160:163]\n"
      "ds_read_b128 v[168:171], v240 offset:0\n"
      "v_mfma_f32_16x16x32_bf16 v[164:167], v[176:179], v[108:111], v[164:167]\n"
      "s_waitcnt lgkmcnt(5)\n"
      "v_mfma_f32_16x16x32_bf16 v[160:163], v[180:183], v[112:115], v[160:163]\n"
      "ds_read_b128 v[172:175], v241 offset:0\n"
      "v_mfma_f32_16x16x32_bf16 v[164:167], v[180:183], v[116:119], v[164:167]\n"
      "s_waitcnt lgkmcnt(5)\n"
      "v_mfma_f32_16x16x32_bf16 v[160:163], v[184:187], v[120:123], v[160:163]\n"
      "ds_read_b128 v[176:179], v240 offset:2048\n"
      "v_mfma_f32_16x16x32_bf16 v[164:167], v[184:187], v[124:127], v[164:167]\n"
      "s_waitcnt lgkmcnt(5)\n"
      "v_mfma_f32_16x16x32_bf16 v[160:163], v[188:191], v[128:131], v[160:163]\n"
      "ds_read_b128 v[180:183], v241 offset:2048\n"
      "v_mfma_f32_16x16x32_bf16 v[164:167], v[188:191], v[132:135], v[164:167]\n"
      "s_waitcnt lgkmcnt(5)\n"
      "v_mfma_f32_16x16x32_bf16 v[160:163], v[192:195], v[136:139], v[160:163]\n"
      "ds_read_b128 v[184:187], v240 offset:4096\n"
      "v_mfma_f32_16x16x32_bf16 v[164:167], v[192:195], v[140:143], v[164:167]\n"
      "s_waitcnt lgkmcnt(5)\n"
      "v_mfma_f32_16x16x32_bf16 v[160:163], v[196:199], v[144:147], v[160:163]\n"
      "ds_read_b128 v[188:191], v241 offset:4096\n"
      "v_mfma_f32_16x16x32_bf16 v[164:167], v[196:199], v[148:151], v[164:167]\n"
      "s_waitcnt lgkmcnt(5)\n"
      "v_mfma_f32_16x16x32_bf16 v[152:155], v[168:171], v[8:11], 0\n"
      "ds_read_b128 v[192:195], v240 offset:6144\n"
      "v_mfma_f32_16x16x32_bf16 v[156:159], v[168:171], v[12:15], 0\n"
      "s_waitcnt lgkmcnt(5)\n"
      "v_mfma_f32_16x16x32_bf16 v[152:155], v[172:175], v[16:19], v[152:155]\n"
      "ds_read_b128 v[196:199], v241 offset:6144\n"
      "v_mfma_f32_16x16x32_bf16 v[156:159], v[172:175], v[20:23], v[156:159]\n"
      "s_waitcnt lgkmcnt(5)\n"
      "v_mfma_f32_16x16x32_bf16 v[152:155], v[176:179], v[24:27], v[152:155]\n"
      "ds_read_b128 v[168:171], v240 offset:8192\n"
      "v_mfma_f32_16x16x32_bf16 v[156:159], v[176:179], v[28:31], v[156:159]\n"
      "s_waitcnt lgkmcnt(5)\n"
      "v_mfma_f32_16x16x32_bf16 v[152:155], v[180:183], v[32:35], v[152:155]\n"
      "ds_read_b128 v[172:175], v241 offset:8192\n"
      "v_mfma_f32_16x16x32_bf16 v[156:159], v[180:183], v[36:39], v[156:159]\n"
      "s_waitcnt lgkmcnt(5)\n"
      "v_mfma_f32_16x16x32_bf16 v[152:155], v[184:187], v[40:43], v[152:155]\n"
      "ds_read_b128 v[176:179], v240 offset:10240\n"
      "v_mfma_f32_16x16x32_bf16 v[156:159], v[184:187], v[44:47], v[156:159]\n"
      "s_waitcnt lgkmcnt(5)\n"
      "v_mfma_f32_16x16x32_bf16 v[152:155], v[188:191], v[48:51], v[152:155]\n"
      "ds_read_b128 v[180:183], v241 offset:10240\n"
      "v_mfma_f32_16x16x32_bf16 v[156:159], v[188:191], v[52:55], v[156:159]\n"
      "s_waitcnt lgkmcnt(5)\n"
      "v_mfma_f32_16x16x32_bf16 v[152:155], v[192:195], v[56:59], v[152:155]\n"
      "ds_read_b128 v[184:187], v240 offset:12288\n"
      "v_mfma_f32_16x16x32_bf16 v[156:159], v[192:195], v[60:63], v[156:159]\n"
      "s_waitcnt lgkmcnt(5)\n"
      "v_mfma_f32_16x16x32_bf16 v[152:155], v[196:199], v[64:67], v[152:155]\n"
      "ds_read_b128 v[188:191], v241 offset:12288\n"
      "v_mfma_f32_16x16x32_bf16 v[156:159], v[196:199], v[68:71], v[156:159]\n"
      "s_waitcnt lgkmcnt(5)\n"
      "v_mfma_f32_16x16x32_bf16 v[152:155], v[168:171], v[72:75], v[152:155]\n"
      "ds_read_b128 v[192:195], v240 offset:14336\n"
      "v_mfma_f32_16x16x32_bf16 v[156:159], v[168:171], v[76:79], v[156:159]\n"
      "v_cvt_pk_bf16_f32 v160, v160, v164\n"
      "v_cvt_pk_bf16_f32 v161, v161, v165\n"
      "v_cvt_pk_bf16_f32 v162, v162, v166\n"
      "v_cvt_pk_bf16_f32 v163, v163, v167\n"
      "s_add_u32 s62, s37, 2064384\n"
      "buffer_store_dword v160, v247, s[68:71], s62 offen sc1\n"
      "s_add_u32 s62, s37, 2073600\n"
      "buffer_store_dword v161, v247, s[68:71], s62 offen sc1\n"
      "s_add_u32 s62, s37, 2082816\n"
      "buffer_store_dword v162, v247, s[68:71], s62 offen sc1\n"
      "s_add_u32 s62, s37, 2092032\n"
      "buffer_store_dword v163, v247, s[68:71], s62 offen sc1\n"
      "s_waitcnt vmcnt(8)\n"
      "s_barrier\n"
      "s_waitcnt lgkmcnt(5)\n"
      "v_mfma_f32_16x16x32_bf16 v[152:155], v[172:175], v[80:83], v[152:155]\n"
      "ds_read_b128 v[196:199], v241 offset:14336\n"
      "v_mfma_f32_16x16x32_bf16 v[156:159], v[172:175], v[84:87], v[156:159]\n"
      "s_waitcnt lgkmcnt(5)\n"
      "v_mfma_f32_16x16x32_bf16 v[152:155], v[176:179], v[88:91], v[152:155]\n"
      "ds_read_b128 v[168:171], v240 offset:16384\n"
      "v_mfma_f32_16x16x32_bf16 v[156:159], v[176:179], v[92:95], v[156:159]\n"
      "s_waitcnt lgkmcnt(5)\n"
      "v_mfma_f32_16x16x32_bf16 v[152:155], v[180:183], v[96:99], v[152:155]\n"
      "ds_read_b128 v[172:175], v241 offset:16384\n"
      "v_mfma_f32_16x16x32_bf16 v[156:159], v[180:183], v[100:103], v[156:159]\n"
      "s_waitcnt lgkmcnt(5)\n"
      "v_mfma_f32_16x16x32_bf16 v[152:155], v[184:187], v[104:107], v[152:155]\n"
      "ds_read_b128 v[176:179], v240 offset:36864\n"
      "v_mfma_f32_16x16x32_bf16 v[156:159], v[184:187], v[108:111], v[156:159]\n"
      "s_waitcnt lgkmcnt(5)\n"
      "v_mfma_f32_16x16x32_bf16 v[152:155], v[188:191], v[112:115], v[152:155]\n"
      "ds_read_b128 v[180:183], v241 offset:36864\n"
      "v_mfma_f32_16x16x32_bf16 v[156:159], v[188:191], v[116:119], v[156:159]\n"
      "s_waitcnt lgkmcnt(5)\n"
      "v_mfma_f32_16x16x32_bf16 v[152:155], v[192:195], v[120:123], v[152:155]\n"
      "ds_read_b128 v[184:187], v240 offset:38912\n"
      "v_mfma_f32_16x16x32_bf16 v[156:159], v[192:195], v[124:127], v[156:159]\n"
      "s_waitcnt lgkmcnt(5)\n"
      "v_mfma_f32_16x16x32_bf16 v[152:155], v[196:199], v[128:131], v[152:155]\n"
      "ds_read_b128 v[188:191], v241 offset:38912\n"
      "v_mfma_f32_16x16x32_bf16 v[156:159], v[196:199], v[132:135], v[156:159]\n"
      "s_waitcnt lgkmcnt(5)\n"
      "v_mfma_f32_16x16x32_bf16 v[152:155], v[168:171], v[136:139], v[152:155]\n"
      "ds_read_b128 v[192:195], v240 offset:40960\n"
      "v_mfma_f32_16x16x32_bf16 v[156:159], v[168:171], v[140:143], v[156:159]\n"
      "s_waitcnt lgkmcnt(5)\n"
      "v_mfma_f32_16x16x32_bf16 v[152:155], v[172:175], v[144:147], v[152:155]\n"
      "ds_read_b128 v[196:199], v241 offset:40960\n"
      "v_mfma_f32_16x16x32_bf16 v[156:159], v[172:175], v[148:151], v[156:159]\n"
      "s_nop 7\n"
      "v_cvt_pk_bf16_f32 v152, v152, v156\n"
      "v_cvt_pk_bf16_f32 v153, v153, v157\n"
      "v_cvt_pk_bf16_f32 v154, v154, v158\n"
      "v_cvt_pk_bf16_f32 v155, v155, v159\n"
      "s_add_u32 s62, s37, 2359296\n"
      "buffer_store_dword v152, v247, s[68:71], s62 offen sc1\n"
      "s_add_u32 s62, s37, 2368512\n"
      "buffer_store_dword v153, v247, s[68:71], s62 offen sc1\n"
      "s_add_u32 s62, s37, 2377728\n"
      "buffer_store_dword v154, v247, s[68:71], s62 offen sc1\n"
      "s_add_u32 s62, s37, 2386944\n"
      "buffer_store_dword v155, v247, s[68:71], s62 offen sc1\n"
      "s_cmp_lt_u32 s38, 19\n"
      "s_cbranch_scc1 Lnox00_%=\n"
      "s_waitcnt lgkmcnt(5)\n"
      "v_mfma_f32_16x16x32_bf16 v[160:163], v[176:179], v[8:11], 0\n"
      "ds_read_b128 v[168:171], v240 offset:43008\n"
      "v_mfma_f32_16x16x32_bf16 v[164:167], v[176:179], v[12:15], 0\n"
      "s_waitcnt lgkmcnt(5)\n"
      "v_mfma_f32_16x16x32_bf16 v[160:163], v[180:183], v[16:19], v[160:163]\n"
      "ds_read_b128 v[172:175], v241 offset:43008\n"
      "v_mfma_f32_16x16x32_bf16 v[164:167], v[180:183], v[20:23], v[164:167]\n"
      "s_waitcnt lgkmcnt(5)\n"
      "v_mfma_f32_16x16x32_bf16 v[160:163], v[184:187], v[24:27], v[160:163]\n"
      "ds_read_b128 v[176:179], v240 offset:45056\n"
      "v_mfma_f32_16x16x32_bf16 v[164:167], v[184:187], v[28:31], v[164:167]\n"
      "s_waitcnt lgkmcnt(5)\n"
      "v_mfma_f32_16x16x32_bf16 v[160:163], v[188:191], v[32:35], v[160:163]\n"
      "ds_read_b128 v[180:183], v241 offset:45056\n"
      "v_mfma_f32_16x16x32_bf16 v[164:167], v[188:191], v[36:39], v[164:167]\n"
      "s_waitcnt lgkmcnt(5)\n"
      "v_mfma_f32_16x16x32_bf16 v[160:163], v[192:195], v[40:43], v[160:163]\n"
      "ds_read_b128 v[184:187], v240 offset:47104\n"
      "v_mfma_f32_16x16x32_bf16 v[164:167], v[192:195], v[44:47], v[164:167]\n"
      "s_waitcnt lgkmcnt(5)\n"
      "v_mfma_f32_16x16x32_bf16 v[160:163], v[196:199], v[48:51], v[160:163]\n"
      "ds_read_b128 v[188:191], v241 offset:47104\n"
      "v_mfma_f32_16x16x32_bf16 v[164:167], v[196:199], v[52:55], v[164:167]\n"
      "s_waitcnt lgkmcnt(5)\n"
      "v_mfma_f32_16x16x32_bf16 v[160:163], v[168:171], v[56:59], v[160:163]\n"
      "ds_read_b128 v[192:195], v240 offset:49152\n"
      "v_mfma_f32_16x16x32_bf16 v[164:167], v[168:171], v[60:63], v[164:167]\n"
      "s_waitcnt lgkmcnt(5)\n"
      "v_mfma_f32_16x16x32_bf16 v[160:163], v[172:175], v[64:67], v[160:163]\n"
      "ds_read_b128 v[196:199], v241 offset:49152\n"
      "v_mfma_f32_16x16x32_bf16 v[164:167], v[172:175], v[68:71], v[164:167]\n"
      "s_waitcnt lgkmcnt(5)\n"
      "v_mfma_f32_16x16x32_bf16 v[160:163], v[176:179], v[72:75], v[160:163]\n"
      "ds_read_b128 v[168:171], v240 offset:51200\n"
      "v_mfma_f32_16x16x32_bf16 v[164:167], v[176:179], v[76:79], v[164:167]\n"
      "s_waitcnt lgkmcnt(5)\n"
      "v_mfma_f32_16x16x32_bf16 v[160:163], v[180:183], v[80:83], v[160:163]\n"
      "ds_read_b128 v[172:175], v241 offset:51200\n"
      "v_mfma_f32_16x16x32_bf16 v[164:167], v[180:183], v[84:87], v[164:167]\n"
      "s_waitcnt lgkmcnt(5)\n"
      "v_mfma_f32_16x16x32_bf16 v[160:163], v[184:187], v[88:91], v[160:163]\n"
      "ds_read_b128 v[176:179], v240 offset:53248\n"
      "v_mfma_f32_16x16x32_bf16 v[164:167], v[184:187], v[92:95], v[164:167]\n"
      "s_waitcnt lgkmcnt(5)\n"
      "v_mfma_f32_16x16x32_bf16 v[160:163], v[188:191], v[96:99], v[160:163]\n"
      "ds_read_b128 v[180:183], v241 offset:53248\n"
      "v_mfma_f32_16x16x32_bf16 v[164:167], v[188:191], v[100:103], v[164:167]\n"
      "s_waitcnt lgkmcnt(5)\n"
      "v_mfma_f32_16x16x32_bf16 v[160:163], v[192:195], v[104:107], v[160:163]\n"
      "v_mfma_f32_16x16x32_bf16 v[164:167], v[192:195], v[108:111], v[164:167]\n"
      "s_waitcnt lgkmcnt(4)\n"
      "v_mfma_f32_16x16x32_bf16 v[160:163], v[196:199], v[112:115], v[160:163]\n"
      "v_mfma_f32_16x16x32_bf16 v[164:167], v[196:199], v[116:119], v[164:167]\n"
      "s_waitcnt lgkmcnt(3)\n"
      "v_mfma_f32_16x16x32_bf16 v[160:163], v[168:171], v[120:123], v[160:163]\n"
      "v_mfma_f32_16x16x32_bf16 v[164:167], v[168:171], v[124:127], v[164:167]\n"
      "s_waitcnt lgkmcnt(2)\n"
      "v_mfma_f32_16x16x32_bf16 v[160:163], v[172:175], v[128:131], v[160:163]\n"
      "v_mfma_f32_16x16x32_bf16 v[164:167], v[172:175], v[132:135], v[164:167]\n"
      "s_waitcnt lgkmcnt(1)\n"
      "v_mfma_f32_16x16x32_bf16 v[160:163], v[176:179], v[136:139], v[160:163]\n"
      "v_mfma_f32_16x16x32_bf16 v[164:167], v[176:179], v[140:143], v[164:167]\n"
      "s_waitcnt lgkmcnt(0)\n"
      "v_mfma_f32_16x16x32_bf16 v[160:163], v[180:183], v[144:147], v[160:163]\n"
      "v_mfma_f32_16x16x32_bf16 v[164:167], v[180:183], v[148:151], v[164:167]\n"
      "s_nop 7\n"
      "v_cvt_pk_bf16_f32 v160, v160, v164\n"
      "v_cvt_pk_bf16_f32 v161, v161, v165\n"
      "v_cvt_pk_bf16_f32 v162, v162, v166\n"
      "v_cvt_pk_bf16_f32 v163, v163, v167\n"
      "s_add_u32 s62, s37, 2654208\n"
      "buffer_store_dword v160, v247, s[68:71], s62 offen sc1\n"
      "s_add_u32 s62, s37, 2663424\n"
      "buffer_store_dword v161, v247, s[68:71], s62 offen sc1\n"
      "s_add_u32 s62, s37, 2672640\n"
      "buffer_store_dword v162, v247, s[68:71], s62 offen sc1\n"
      "s_add_u32 s62, s37, 2681856\n"
      "buffer_store_dword v163, v247, s[68:71], s62 offen sc1\n"
      "Lnox00_%=:\n"
      "s_waitcnt lgkmcnt(0)\n"
      "v_mov_b32 v8, 153600\n"
      "ds_read_b128 v[12:15], v8\n"
      "ds_read_b128 v[16:19], v8 offset:16\n"
      "s_waitcnt lgkmcnt(0)\n"
      "v_or3_b32 v12, v12, v13, v14\n"
      "v_or3_b32 v12, v12, v15, v16\n"
      "v_or3_b32 v12, v12, v17, v18\n"
      "v_or_b32 %[bad], v12, v19\n"
      "s_branch Lend_%=\n"
      "Lp1_%=:\n"
      "s_lshl_b32 s60, s39, 6\n"
      "v_add_u32 v4, s60, v1\n"
      "v_mul_u32_u24 v4, 7, v4\n"
      "v_add_u32 v4, s41, v4\n"
      "v_min_u32 v4, 2303, v4\n"
      "v_lshrrev_b32 v5, 2, v4\n"
      "v_and_b32 v4, 3, v4\n"
      "v_mul_u32_u24 v5, 18432, v5\n"
      "v_lshl_add_u32 v4, v4, 7, v5\n"
      "s_lshl_b32 s60, s40, 2\n"
      "s_mov_b32 m0, 153632\n"
      "s_nop 0\n"
      "buffer_load_dword v4, s[20:23], s60 offen lds\n"
      "v_lshrrev_b32 v4, 1, v2\n"
      "v_xor_b32 v5, v3, v4\n"
      "v_lshlrev_b32 v5, 4, v5\n"
      "v_lshl_add_u32 v240, v2, 7, v5\n"
      "v_xor_b32 v241, 64, v240\n"
      "v_add_u32 v214, 0x10000, v240\n"
      "v_add_u32 v216, 0x20000, v240\n"
      "v_add_u32 v215, 0x10000, v241\n"
      "v_add_u32 v217, 0x20000, v241\n"
      "v_lshrrev_b32 v4, 3, v1\n"
      "v_and_b32 v5, 1, v4\n"
      "v_xor_b32 v5, v1, v5\n"
      "s_lshl_b32 s60, s49, 12\n"
      "s_add_u32 s60, s60, 110592\n"
      "v_lshlrev_b32 v5, 4, v5\n"
      "v_add_u32 v242, s60, v5\n"
      "s_lshl_b32 s60, s48, 3\n"
      "v_add_u32 v4, s60, v4\n"
      "v_lshrrev_b32 v5, 1, v4\n"
      "v_and_b32 v6, 7, v1\n"
      "v_xor_b32 v5, v5, v6\n"
      "v_and_b32 v5, 7, v5\n"
      "v_mul_u32_u24 v4, 1152, v4\n"
      "v_lshl_add_u32 v243, v5, 4, v4\n"
      "s_lshl_b32 s43, s48, 10\n"
      "v_lshrrev_b32 v4, 3, v2\n"
      "v_lshlrev_b32 v4, 12, v4\n"
      "v_lshl_add_u32 v4, v3, 8, v4\n"
      "v_and_b32 v5, 7, v2\n"
      "v_lshl_add_u32 v4, v5, 5, v4\n"
      "s_lshl_b32 s60, s48, 13\n"
      "s_add_u32 s60, s60, 112640\n"
      "v_add_u32 v4, s60, v4\n"
      "v_lshrrev_b32 v5, 2, v2\n"
      "v_and_b32 v5, 1, v5\n"
      "v_lshl_add_u32 v244, v5, 4, v4\n"
      "v_xor_b32 v245, 16, v244\n"
      "v_lshlrev_b32 v4, 3, v2\n"
      "v_add_u32 v246, s44, v4\n"
      "s_lshl_b32 s60, s49, 5\n"
      "s_add_u32 s60, s60, s40\n"
      "s_lshl_b32 s60, s60, 1\n"
      "v_mul_u32_u24 v4, 36864, v3\n"
      "v_lshl_add_u32 v4, v2, 2, v4\n"
      "v_add_u32 v247, s60, v4\n"
      "s_lshl_b32 s60, s39, 2\n"
      "s_add_u32 s45, s60, 153600\n"
      "v_mov_b32 v208, 0\n"
      "v_mov_b32 v209, 0\n"
      "s_waitcnt vmcnt(9)\n"
      "v_lshlrev_b32 v168, 7, v3\n"
      "v_add_u32 v168, v246, v168\n"
      "ds_read_b64 v[172:173], v168\n"
      "ds_read_b64 v[174:175], v168 offset:640\n"
      "ds_read_b64 v[176:177], v246 offset:512\n"
      "ds_read_b64 v[178:179], v246 offset:1152\n"
      "v_mov_b32 v180, 0\n"
      "v_mov_b32 v181, 0\n"
      "v_mov_b32 v182, 0\n"
      "s_waitcnt lgkmcnt(0)\n"
      "v_lshlrev_b32 v168, 16, v174\n"
      "v_and_b32 v169, 0xffff0000, v174\n"
      "v_max3_f32 v180, |v168|, |v169|, v180\n"
      "v_rndne_f32 v170, v168\n"
      "v_rndne_f32 v171, v169\n"
      "v_sub_f32 v168, v168, v170\n"
      "v_sub_f32 v169, v169, v171\n"
      "v_max3_f32 v181, |v168|, |v169|, v181\n"
      "v_lshlrev_b32 v168, 16, v172\n"
      "v_and_b32 v169, 0xffff0000, v172\n"
      "v_max3_f32 v182, |v168|, |v169|, v182\n"
      "v_lshlrev_b32 v168, 16, v175\n"
      "v_and_b32 v169, 0xffff0000, v175\n"
      "v_max3_f32 v180, |v168|, |v169|, v180\n"
      "v_rndne_f32 v170, v168\n"
      "v_rndne_f32 v171, v169\n"
      "v_sub_f32 v168, v168, v170\n"
      "v_sub_f32 v169, v169, v171\n"
      "v_max3_f32 v181, |v168|, |v169|, v181\n"
      "v_lshlrev_b32 v168, 16, v173\n"
      "v_and_b32 v169, 0xffff0000, v173\n"
      "v_max3_f32 v182, |v168|, |v169|, v182\n"
      "v_lshlrev_b32 v168, 16, v178\n"
      "v_and_b32 v169, 0xffff0000, v178\n"
      "v_max3_f32 v180, |v168|, |v169|, v180\n"
      "v_rndne_f32 v170, v168\n"
      "v_rndne_f32 v171, v169\n"
      "v_sub_f32 v168, v168, v170\n"
      "v_sub_f32 v169, v169, v171\n"
      "v_max3_f32 v181, |v168|, |v169|, v181\n"
      "v_lshlrev_b32 v168, 16, v176\n"
      "v_and_b32 v169, 0xffff0000, v176\n"
      "v_max3_f32 v182, |v168|, |v169|, v182\n"
      "v_lshlrev_b32 v168, 16, v179\n"
      "v_and_b32 v169, 0xffff0000, v179\n"
      "v_max3_f32 v180, |v168|, |v169|, v180\n"
      "v_rndne_f32 v170, v168\n"
      "v_rndne_f32 v171, v169\n"
      "v_sub_f32 v168, v168, v170\n"
      "v_sub_f32 v169, v169, v171\n"
      "v_max3_f32 v181, |v168|, |v169|, v181\n"
      "v_lshlrev_b32 v168, 16, v177\n"
      "v_and_b32 v169, 0xffff0000, v177\n"
      "v_max3_f32 v182, |v168|, |v169|, v182\n"
      "v_cmp_lt_f32 vcc, 0, v181\n"
      "s_mov_b64 s[66:67], vcc\n"
      "v_cmp_lt_f32 vcc, 0x43000000, v180\n"
      "s_or_b64 s[66:67], s[66:67], vcc\n"
      "v_cmp_le_f32 vcc, 0x71800000, v182\n"
      "s_or_b64 s[66:67], s[66:67], vcc\n"
      "ds_read_b64 v[184:185], v246 offset:0\n"
      "ds_read_b64 v[186:187], v246 offset:640\n"
      "s_mov_b32 s63, 2359296\n"
      "buffer_load_dwordx4 v[40:43], v210, s[20:23], s63 offen\n"
      "s_waitcnt lgkmcnt(0)\n"
      "v_lshlrev_b32 v200, 16, v184\n"
      "v_and_b32 v201, 0xffff0000, v184\n"
      "v_lshlrev_b32 v204, 16, v186\n"
      "v_and_b32 v205, 0xffff0000, v186\n"
      "v_lshlrev_b32 v202, 16, v185\n"
      "v_and_b32 v203, 0xffff0000, v185\n"
      "v_lshlrev_b32 v206, 16, v187\n"
      "v_and_b32 v207, 0xffff0000, v187\n"
      "buffer_load_dwordx4 v[44:47], v211, s[20:23], s63 offen\n"
      "v_mul_f32_e64 v204, -v204, v200\n"
      "v_mul_f32_e64 v205, -v205, v201\n"
      "v_mul_f32 v200, 0x4b800000, v200\n"
      "v_mul_f32 v201, 0x4b800000, v201\n"
      "v_mul_f32_e64 v206, -v206, v202\n"
      "v_mul_f32_e64 v207, -v207, v203\n"
      "v_mul_f32 v202, 0x4b800000, v202\n"
      "v_mul_f32 v203, 0x4b800000, v203\n"
      "s_waitcnt vmcnt(9)\n"
      "v_or3_b32 v208, v120, v121, v208\n"
      "v_or3_b32 v208, v122, v123, v208\n"
      "v_or3_b32 v209, v124, v125, v209\n"
      "v_or3_b32 v209, v126, v127, v209\n"
      "buffer_load_dwordx4 v[48:51], v212, s[20:23], s63 offen\n"
      "v_fma_mix_f32 v120, v120, v200, v204 op_sel_hi:[1,0,0]\n"
      "v_fma_mix_f32 v121, v121, v201, v205 op_sel_hi:[1,0,0]\n"
      "v_fma_mix_f32 v122, v122, v202, v206 op_sel_hi:[1,0,0]\n"
      "v_fma_mix_f32 v123, v123, v203, v207 op_sel_hi:[1,0,0]\n"
      "v_fma_mix_f32 v124, v124, v200, v204 op_sel_hi:[1,0,0]\n"
      "v_fma_mix_f32 v125, v125, v201, v205 op_sel_hi:[1,0,0]\n"
      "v_fma_mix_f32 v126, v126, v202, v206 op_sel_hi:[1,0,0]\n"
      "v_fma_mix_f32 v127, v127, v203, v207 op_sel_hi:[1,0,0]\n"
      "v_cvt_pk_bf16_f32 v184, v120, v124\n"
      "v_cvt_pk_bf16_f32 v188, v121, v125\n"
      "v_cvt_pk_bf16_f32 v192, v122, v126\n"
      "v_cvt_pk_bf16_f32 v196, v123, v127\n"
      "buffer_load_dwordx4 v[52:55], v213, s[20:23], s63 offen\n"
      "s_waitcnt vmcnt(9)\n"
      "v_or3_b32 v208, v128, v129, v208\n"
      "v_or3_b32 v208, v130, v131, v208\n"
      "v_or3_b32 v209, v132, v133, v209\n"
      "v_or3_b32 v209, v134, v135, v209\n"
      "v_fma_mix_f32 v128, v128, v200, v204 op_sel_hi:[1,0,0]\n"
      "v_fma_mix_f32 v129, v129, v201, v205 op_sel_hi:[1,0,0]\n"
      "v_fma_mix_f32 v130, v130, v202, v206 op_sel_hi:[1,0,0]\n"
      "v_fma_mix_f32 v131, v131, v203, v207 op_sel_hi:[1,0,0]\n"
      "v_fma_mix_f32 v132, v132, v200, v204 op_sel_hi:[1,0,0]\n"
      "v_fma_mix_f32 v133, v133, v201, v205 op_sel_hi:[1,0,0]\n"
      "v_fma_mix_f32 v134, v134, v202, v206 op_sel_hi:[1,0,0]\n"
      "v_fma_mix_f32 v135, v135, v203, v207 op_sel_hi:[1,0,0]\n"
      "s_mov_b32 s63, 2433024\n"
      "buffer_load_dwordx4 v[56:59], v210, s[20:23], s63 offen\n"
      "v_cvt_pk_bf16_f32 v185, v128, v132\n"
      "v_cvt_pk_bf16_f32 v189, v129, v133\n"
      "v_cvt_pk_bf16_f32 v193, v130, v134\n"
      "v_cvt_pk_bf16_f32 v197, v131, v135\n"
      "s_waitcnt vmcnt(6)\n"
      "buffer_load_dwordx4 v[60:63], v211, s[20:23], s63 offen\n"
      "v_or3_b32 v208, v136, v140, v208\n"
      "v_or3_b32 v209, v144, v148, v209\n"
      "v_fma_mix_f32 v136, v136, v200, v204 op_sel_hi:[1,0,0]\n"
      "v_fma_mix_f32 v140, v140, v200, v204 op_sel_hi:[1,0,0]\n"
      "v_fma_mix_f32 v144, v144, v200, v204 op_sel_hi:[1,0,0]\n"
      "v_fma_mix_f32 v148, v148, v200, v204 op_sel_hi:[1,0,0]\n"
      "v_cvt_pk_bf16_f32 v186, v136, v140\n"
      "v_cvt_pk_bf16_f32 v187, v144, v148\n"
      "ds_write_b128 v244, v[184:187] offset:0\n"
      "v_or3_b32 v208, v137, v141, v208\n"
      "v_or3_b32 v209, v145, v149, v209\n"
      "v_fma_mix_f32 v137, v137, v201, v205 op_sel_hi:[1,0,0]\n"
      "v_fma_mix_f32 v141, v141, v201, v205 op_sel_hi:[1,0,0]\n"
      "buffer_load_dwordx4 v[64:67], v212, s[20:23], s63 offen\n"
      "v_fma_mix_f32 v145, v145, v201, v205 op_sel_hi:[1,0,0]\n"
      "v_fma_mix_f32 v149, v149, v201, v205 op_sel_hi:[1,0,0]\n"
      "v_cvt_pk_bf16_f32 v190, v137, v141\n"
      "v_cvt_pk_bf16_f32 v191, v145, v149\n"
      "ds_write_b128 v244, v[188:191] offset:1024\n"
      "v_or3_b32 v208, v138, v142, v208\n"
      "v_or3_b32 v209, v146, v150, v209\n"
      "v_fma_mix_f32 v138, v138, v202, v206 op_sel_hi:[1,0,0]\n"
      "v_fma_mix_f32 v142, v142, v202, v206 op_sel_hi:[1,0,0]\n"
      "v_fma_mix_f32 v146, v146, v202, v206 op_sel_hi:[1,0,0]\n"
      "v_fma_mix_f32 v150, v150, v202, v206 op_sel_hi:[1,0,0]\n"
      "v_cvt_pk_bf16_f32 v194, v138, v142\n"
      "v_cvt_pk_bf16_f32 v195, v146, v150\n"
      "ds_write_b128 v245, v[192:195] offset:0\n"
      "buffer_load_dwordx4 v[68:71], v213, s[20:23], s63 offen\n"
      "v_or3_b32 v208, v139, v143, v208\n"
      "v_or3_b32 v209, v147, v151, v209\n"
      "v_fma_mix_f32 v139, v139, v203, v207 op_sel_hi:[1,0,0]\n"
      "v_fma_mix_f32 v143, v143, v203, v207 op_sel_hi:[1,0,0]\n"
      "v_fma_mix_f32 v147, v147, v203, v207 op_sel_hi:[1,0,0]\n"
      "v_fma_mix_f32 v151, v151, v203, v207 op_sel_hi:[1,0,0]\n"
      "v_cvt_pk_bf16_f32 v198, v139, v143\n"
      "v_cvt_pk_bf16_f32 v199, v147, v151\n"
      "ds_write_b128 v245, v[196:199] offset:1024\n"
      "s_waitcnt lgkmcnt(0)\n"
      "s_barrier\n"
      "ds_read_b128 v[8:11], v242 offset:0\n"
      "ds_read_b128 v[12:15], v242 offset:1024\n"
      "ds_read_b128 v[16:19], v242 offset:2048\n"
      "ds_read_b128 v[20:23], v242 offset:3072\n"
      "s_mov_b32 s63, 4718592\n"
      "buffer_load_dwordx4 v[72:75], v210, s[20:23], s63 offen\n"
      "buffer_load_dwordx4 v[76:79], v211, s[20:23], s63 offen\n"
      "buffer_load_dwordx4 v[80:83], v212, s[20:23], s63 offen\n"
      "buffer_load_dwordx4 v[84:87], v213, s[20:23], s63 offen\n"
      "ds_read_b64 v[184:185], v246 offset:128\n"
      "ds_read_b64 v[186:187], v246 offset:768\n"
      "s_waitcnt lgkmcnt(0)\n"
      "v_lshlrev_b32 v200, 16, v184\n"
      "v_and_b32 v201, 0xffff0000, v184\n"
      "v_lshlrev_b32 v204, 16, v186\n"
      "v_and_b32 v205, 0xffff0000, v186\n"
      "v_lshlrev_b32 v202, 16, v185\n"
      "v_and_b32 v203, 0xffff0000, v185\n"
      "v_lshlrev_b32 v206, 16, v187\n"
      "v_and_b32 v207, 0xffff0000, v187\n"
      "v_mul_f32_e64 v204, -v204, v200\n"
      "v_mul_f32_e64 v205, -v205, v201\n"
      "v_mul_f32 v200, 0x4b800000, v200\n"
      "v_mul_f32 v201, 0x4b800000, v201\n"
      "v_mul_f32_e64 v206, -v206, v202\n"
      "v_mul_f32_e64 v207, -v207, v203\n"
      "v_mul_f32 v202, 0x4b800000, v202\n"
      "v_mul_f32 v203, 0x4b800000, v203\n"
      "s_waitcnt vmcnt(10)\n"
      "v_or3_b32 v208, v40, v41, v208\n"
      "v_or3_b32 v208, v42, v43, v208\n"
      "v_or3_b32 v209, v44, v45, v209\n"
      "v_or3_b32 v209, v46, v47, v209\n"
      "v_fma_mix_f32 v40, v40, v200, v204 op_sel_hi:[1,0,0]\n"
      "v_fma_mix_f32 v41, v41, v201, v205 op_sel_hi:[1,0,0]\n"
      "v_fma_mix_f32 v42, v42, v202, v206 op_sel_hi:[1,0,0]\n"
      "v_fma_mix_f32 v43, v43, v203, v207 op_sel_hi:[1,0,0]\n"
      "v_fma_mix_f32 v44, v44, v200, v204 op_sel_hi:[1,0,0]\n"
      "v_fma_mix_f32 v45, v45, v201, v205 op_sel_hi:[1,0,0]\n"
      "v_fma_mix_f32 v46, v46, v202, v206 op_sel_hi:[1,0,0]\n"
      "v_fma_mix_f32 v47, v47, v203, v207 op_sel_hi:[1,0,0]\n"
      "v_cvt_pk_bf16_f32 v184, v40, v44\n"
      "v_cvt_pk_bf16_f32 v188, v41, v45\n"
      "v_cvt_pk_bf16_f32 v192, v42, v46\n"
      "v_cvt_pk_bf16_f32 v196, v43, v47\n"
      "s_waitcnt vmcnt(8)\n"
      "v_or3_b32 v208, v48, v49, v208\n"
      "v_or3_b32 v208, v50, v51, v208\n"
      "v_or3_b32 v209, v52, v53, v209\n"
      "v_or3_b32 v209, v54, v55, v209\n"
      "v_fma_mix_f32 v48, v48, v200, v204 op_sel_hi:[1,0,0]\n"
      "v_fma_mix_f32 v49, v49, v201, v205 op_sel_hi:[1,0,0]\n"
      "v_fma_mix_f32 v50, v50, v202, v206 op_sel_hi:[1,0,0]\n"
      "v_fma_mix_f32 v51, v51, v203, v207 op_sel_hi:[1,0,0]\n"
      "v_fma_mix_f32 v52, v52, v200, v204 op_sel_hi:[1,0,0]\n"
      "v_fma_mix_f32 v53, v53, v201, v205 op_sel_hi:[1,0,0]\n"
      "v_fma_mix_f32 v54, v54, v202, v206 op_sel_hi:[1,0,0]\n"
      "v_fma_mix_f32 v55, v55, v203, v207 op_sel_hi:[1,0,0]\n"
      "v_cvt_pk_bf16_f32 v185, v48, v52\n"
      "v_cvt_pk_bf16_f32 v189, v49, v53\n"
      "v_cvt_pk_bf16_f32 v193, v50, v54\n"
      "v_cvt_pk_bf16_f32 v197, v51, v55\n"
      "s_barrier\n"
      "ds_read_b128 v[24:27], v242 offset:16384\n"
      "ds_read_b128 v[28:31], v242 offset:17408\n"
      "ds_read_b128 v[32:35], v242 offset:18432\n"
      "ds_read_b128 v[36:39], v242 offset:19456\n"
      "s_setprio 1\n"
      "s_waitcnt vmcnt(4)\n"
      "v_or3_b32 v208, v56, v60, v208\n"
      "v_or3_b32 v209, v64, v68, v209\n"
      "v_fma_mix_f32 v56, v56, v200, v204 op_sel_hi:[1,0,0]\n"
      "v_fma_mix_f32 v60, v60, v200, v204 op_sel_hi:[1,0,0]\n"
      "v_fma_mix_f32 v64, v64, v200, v204 op_sel_hi:[1,0,0]\n"
      "v_fma_mix_f32 v68, v68, v200, v204 op_sel_hi:[1,0,0]\n"
      "v_cvt_pk_bf16_f32 v186, v56, v60\n"
      "v_cvt_pk_bf16_f32 v187, v64, v68\n"
      "ds_write_b128 v244, v[184:187] offset:0\n"
      "v_or3_b32 v208, v57, v61, v208\n"
      "v_or3_b32 v209, v65, v69, v209\n"
      "v_fma_mix_f32 v57, v57, v201, v205 op_sel_hi:[1,0,0]\n"
      "v_fma_mix_f32 v61, v61, v201, v205 op_sel_hi:[1,0,0]\n"
      "v_fma_mix_f32 v65, v65, v201, v205 op_sel_hi:[1,0,0]\n"
      "v_fma_mix_f32 v69, v69, v201, v205 op_sel_hi:[1,0,0]\n"
      "v_cvt_pk_bf16_f32 v190, v57, v61\n"
      "v_cvt_pk_bf16_f32 v191, v65, v69\n"
      "ds_write_b128 v244, v[188:191] offset:1024\n"
      "v_or3_b32 v208, v58, v62, v208\n"
      "v_or3_b32 v209, v66, v70, v209\n"
      "v_fma_mix_f32 v58, v58, v202, v206 op_sel_hi:[1,0,0]\n"
      "v_fma_mix_f32 v62, v62, v202, v206 op_sel_hi:[1,0,0]\n"
      "v_fma_mix_f32 v66, v66, v202, v206 op_sel_hi:[1,0,0]\n"
      "v_fma_mix_f32 v70, v70, v202, v206 op_sel_hi:[1,0,0]\n"
      "v_cvt_pk_bf16_f32 v194, v58, v62\n"
      "v_cvt_pk_bf16_f32 v195, v66, v70\n"
      "ds_write_b128 v245, v[192:195] offset:0\n"
      "v_or3_b32 v208, v59, v63, v208\n"
      "v_or3_b32 v209, v67, v71, v209\n"
      "v_fma_mix_f32 v59, v59, v203, v207 op_sel_hi:[1,0,0]\n"
      "v_fma_mix_f32 v63, v63, v203, v207 op_sel_hi:[1,0,0]\n"
      "v_fma_mix_f32 v67, v67, v203, v207 op_sel_hi:[1,0,0]\n"
      "v_fma_mix_f32 v71, v71, v203, v207 op_sel_hi:[1,0,0]\n"
      "v_cvt_pk_bf16_f32 v198, v59, v63\n"
      "v_cvt_pk_bf16_f32 v199, v67, v71\n"
      "ds_write_b128 v245, v[196:199] offset:1024\n"
      "s_setprio 0\n"
      "s_mov_b32 s63, 4792320\n"
      "buffer_load_dwordx4 v[88:91], v210, s[20:23], s63 offen\n"
      "buffer_load_dwordx4 v[92:95], v211, s[20:23], s63 offen\n"
      "buffer_load_dwordx4 v[96:99], v212, s[20:23], s63 offen\n"
      "buffer_load_dwordx4 v[100:103], v213, s[20:23], s63 offen\n"
      "s_waitcnt lgkmcnt(0)\n"
      "s_barrier\n"
      "ds_read_b128 v[40:43], v242 offset:0\n"
      "ds_read_b128 v[44:47], v242 offset:1024\n"
      "ds_read_b128 v[48:51], v242 offset:2048\n"
      "ds_read_b128 v[52:55], v242 offset:3072\n"
      "s_mov_b32 s63, 7077888\n"
      "buffer_load_dwordx4 v[104:107], v210, s[20:23], s63 offen\n"
      "buffer_load_dwordx4 v[108:111], v211, s[20:23], s63 offen\n"
      "buffer_load_dwordx4 v[112:115], v212, s[20:23], s63 offen\n"
      "buffer_load_dwordx4 v[116:119], v213, s[20:23], s63 offen\n"
      "ds_read_b64 v[184:185], v246 offset:256\n"
      "ds_read_b64 v[186:187], v246 offset:896\n"
      "s_waitcnt lgkmcnt(0)\n"
      "v_lshlrev_b32 v200, 16, v184\n"
      "v_and_b32 v201, 0xffff0000, v184\n"
      "v_lshlrev_b32 v204, 16, v186\n"
      "v_and_b32 v205, 0xffff0000, v186\n"
      "v_lshlrev_b32 v202, 16, v185\n"
      "v_and_b32 v203, 0xffff0000, v185\n"
      "v_lshlrev_b32 v206, 16, v187\n"
      "v_and_b32 v207, 0xffff0000, v187\n"
      "v_mul_f32_e64 v204, -v204, v200\n"
      "v_mul_f32_e64 v205, -v205, v201\n"
      "v_mul_f32 v200, 0x4b800000, v200\n"
      "v_mul_f32 v201, 0x4b800000, v201\n"
      "v_mul_f32_e64 v206, -v206, v202\n"
      "v_mul_f32_e64 v207, -v207, v203\n"
      "v_mul_f32 v202, 0x4b800000, v202\n"
      "v_mul_f32 v203, 0x4b800000, v203\n"
      "s_waitcnt vmcnt(10)\n"
      "v_or3_b32 v208, v72, v73, v208\n"
      "v_or3_b32 v208, v74, v75, v208\n"
      "v_or3_b32 v209, v76, v77, v209\n"
      "v_or3_b32 v209, v78, v79, v209\n"
      "v_fma_mix_f32 v72, v72, v200, v204 op_sel_hi:[1,0,0]\n"
      "v_fma_mix_f32 v73, v73, v201, v205 op_sel_hi:[1,0,0]\n"
      "v_fma_mix_f32 v74, v74, v202, v206 op_sel_hi:[1,0,0]\n"
      "v_fma_mix_f32 v75, v75, v203, v207 op_sel_hi:[1,0,0]\n"
      "v_fma_mix_f32 v76, v76, v200, v204 op_sel_hi:[1,0,0]\n"
      "v_fma_mix_f32 v77, v77, v201, v205 op_sel_hi:[1,0,0]\n"
      "v_fma_mix_f32 v78, v78, v202, v206 op_sel_hi:[1,0,0]\n"
      "v_fma_mix_f32 v79, v79, v203, v207 op_sel_hi:[1,0,0]\n"
      "v_cvt_pk_bf16_f32 v184, v72, v76\n"
      "v_cvt_pk_bf16_f32 v188, v73, v77\n"
      "v_cvt_pk_bf16_f32 v192, v74, v78\n"
      "v_cvt_pk_bf16_f32 v196, v75, v79\n"
      "s_waitcnt vmcnt(8)\n"
      "v_or3_b32 v208, v80, v81, v208\n"
      "v_or3_b32 v208, v82, v83, v208\n"
      "v_or3_b32 v209, v84, v85, v209\n"
      "v_or3_b32 v209, v86, v87, v209\n"
      "v_fma_mix_f32 v80, v80, v200, v204 op_sel_hi:[1,0,0]\n"
      "v_fma_mix_f32 v81, v81, v201, v205 op_sel_hi:[1,0,0]\n"
      "v_fma_mix_f32 v82, v82, v202, v206 op_sel_hi:[1,0,0]\n"
      "v_fma_mix_f32 v83, v83, v203, v207 op_sel_hi:[1,0,0]\n"
      "v_fma_mix_f32 v84, v84, v200, v204 op_sel_hi:[1,0,0]\n"
      "v_fma_mix_f32 v85, v85, v201, v205 op_sel_hi:[1,0,0]\n"
      "v_fma_mix_f32 v86, v86, v202, v206 op_sel_hi:[1,0,0]\n"
      "v_fma_mix_f32 v87, v87, v203, v207 op_sel_hi:[1,0,0]\n"
      "v_cvt_pk_bf16_f32 v185, v80, v84\n"
      "v_cvt_pk_bf16_f32 v189, v81, v85\n"
      "v_cvt_pk_bf16_f32 v193, v82, v86\n"
      "v_cvt_pk_bf16_f32 v197, v83, v87\n"
      "s_barrier\n"
      "ds_read_b128 v[56:59], v242 offset:16384\n"
      "ds_read_b128 v[60:63], v242 offset:17408\n"
      "ds_read_b128 v[64:67], v242 offset:18432\n"
      "ds_read_b128 v[68:71], v242 offset:19456\n"
      "s_setprio 1\n"
      "s_waitcnt vmcnt(4)\n"
      "v_or3_b32 v208, v88, v92, v208\n"
      "v_or3_b32 v209, v96, v100, v209\n"
      "v_fma_mix_f32 v88, v88, v200, v204 op_sel_hi:[1,0,0]\n"
      "v_fma_mix_f32 v92, v92, v200, v204 op_sel_hi:[1,0,0]\n"
      "v_fma_mix_f32 v96, v96, v200, v204 op_sel_hi:[1,0,0]\n"
      "v_fma_mix_f32 v100, v100, v200, v204 op_sel_hi:[1,0,0]\n"
      "v_cvt_pk_bf16_f32 v186, v88, v92\n"
      "v_cvt_pk_bf16_f32 v187, v96, v100\n"
      "ds_write_b128 v244, v[184:187] offset:0\n"
      "v_or3_b32 v208, v89, v93, v208\n"
      "v_or3_b32 v209, v97, v101, v209\n"
      "v_fma_mix_f32 v89, v89, v201, v205 op_sel_hi:[1,0,0]\n"
      "v_fma_mix_f32 v93, v93, v201, v205 op_sel_hi:[1,0,0]\n"
      "v_fma_mix_f32 v97, v97, v201, v205 op_sel_hi:[1,0,0]\n"
      "v_fma_mix_f32 v101, v101, v201, v205 op_sel_hi:[1,0,0]\n"
      "v_cvt_pk_bf16_f32 v190, v89, v93\n"
      "v_cvt_pk_bf16_f32 v191, v97, v101\n"
      "ds_write_b128 v244, v[188:191] offset:1024\n"
      "v_or3_b32 v208, v90, v94, v208\n"
      "v_or3_b32 v209, v98, v102, v209\n"
      "v_fma_mix_f32 v90, v90, v202, v206 op_sel_hi:[1,0,0]\n"
      "v_fma_mix_f32 v94, v94, v202, v206 op_sel_hi:[1,0,0]\n"
      "v_fma_mix_f32 v98, v98, v202, v206 op_sel_hi:[1,0,0]\n"
      "v_fma_mix_f32 v102, v102, v202, v206 op_sel_hi:[1,0,0]\n"
      "v_cvt_pk_bf16_f32 v194, v90, v94\n"
      "v_cvt_pk_bf16_f32 v195, v98, v102\n"
      "ds_write_b128 v245, v[192:195] offset:0\n"
      "v_or3_b32 v208, v91, v95, v208\n"
      "v_or3_b32 v209, v99, v103, v209\n"
      "v_fma_mix_f32 v91, v91, v203, v207 op_sel_hi:[1,0,0]\n"
      "v_fma_mix_f32 v95, v95, v203, v207 op_sel_hi:[1,0,0]\n"
      "v_fma_mix_f32 v99, v99, v203, v207 op_sel_hi:[1,0,0]\n"
      "v_fma_mix_f32 v103, v103, v203, v207 op_sel_hi:[1,0,0]\n"
      "v_cvt_pk_bf16_f32 v198, v91, v95\n"
      "v_cvt_pk_bf16_f32 v199, v99, v103\n"
      "ds_write_b128 v245, v[196:199] offset:1024\n"
      "s_setprio 0\n"
      "s_mov_b32 s63, 7151616\n"
      "buffer_load_dwordx4 v[120:123], v210, s[20:23], s63 offen\n"
      "buffer_load_dwordx4 v[124:127], v211, s[20:23], s63 offen\n"
      "buffer_load_dwordx4 v[128:131], v212, s[20:23], s63 offen\n"
      "buffer_load_dwordx4 v[132:135], v213, s[20:23], s63 offen\n"
      "s_waitcnt lgkmcnt(0)\n"
      "s_barrier\n"
      "ds_read_b128 v[72:75], v242 offset:0\n"
      "ds_read_b128 v[76:79], v242 offset:1024\n"
      "ds_read_b128 v[80:83], v242 offset:2048\n"
      "ds_read_b128 v[84:87], v242 offset:3072\n"
      "s_mov_b32 s63, 9437184\n"
      "buffer_load_dwordx4 v[136:139], v210, s[20:23], s63 offen\n"
      "buffer_load_dwordx4 v[140:143], v211, s[20:23], s63 offen\n"
      "buffer_load_dwordx4 v[144:147], v212, s[20:23], s63 offen\n"
      "buffer_load_dwordx4 v[148:151], v213, s[20:23], s63 offen\n"
      "ds_read_b64 v[184:185], v246 offset:384\n"
      "ds_read_b64 v[186:187], v246 offset:1024\n"
      "s_waitcnt lgkmcnt(0)\n"
      "v_lshlrev_b32 v200, 16, v184\n"
      "v_and_b32 v201, 0xffff0000, v184\n"
      "v_lshlrev_b32 v204, 16, v186\n"
      "v_and_b32 v205, 0xffff0000, v186\n"
      "v_lshlrev_b32 v202, 16, v185\n"
      "v_and_b32 v203, 0xffff0000, v185\n"
      "v_lshlrev_b32 v206, 16, v187\n"
      "v_and_b32 v207, 0xffff0000, v187\n"
      "v_mul_f32_e64 v204, -v204, v200\n"
      "v_mul_f32_e64 v205, -v205, v201\n"
      "v_mul_f32 v200, 0x4b800000, v200\n"
      "v_mul_f32 v201, 0x4b800000, v201\n"
      "v_mul_f32_e64 v206, -v206, v202\n"
      "v_mul_f32_e64 v207, -v207, v203\n"
      "v_mul_f32 v202, 0x4b800000, v202\n"
      "v_mul_f32 v203, 0x4b800000, v203\n"
      "s_waitcnt vmcnt(10)\n"
      "v_or3_b32 v208, v104, v105, v208\n"
      "v_or3_b32 v208, v106, v107, v208\n"
      "v_or3_b32 v209, v108, v109, v209\n"
      "v_or3_b32 v209, v110, v111, v209\n"
      "v_fma_mix_f32 v104, v104, v200, v204 op_sel_hi:[1,0,0]\n"
      "v_fma_mix_f32 v105, v105, v201, v205 op_sel_hi:[1,0,0]\n"
      "v_fma_mix_f32 v106, v106, v202, v206 op_sel_hi:[1,0,0]\n"
      "v_fma_mix_f32 v107, v107, v203, v207 op_sel_hi:[1,0,0]\n"
      "v_fma_mix_f32 v108, v108, v200, v204 op_sel_hi:[1,0,0]\n"
      "v_fma_mix_f32 v109, v109, v201, v205 op_sel_hi:[1,0,0]\n"
      "v_fma_mix_f32 v110, v110, v202, v206 op_sel_hi:[1,0,0]\n"
      "v_fma_mix_f32 v111, v111, v203, v207 op_sel_hi:[1,0,0]\n"
      "v_cvt_pk_bf16_f32 v184, v104, v108\n"
      "v_cvt_pk_bf16_f32 v188, v105, v109\n"
      "v_cvt_pk_bf16_f32 v192, v106, v110\n"
      "v_cvt_pk_bf16_f32 v196, v107, v111\n"
      "s_waitcnt vmcnt(8)\n"
      "v_or3_b32 v208, v112, v113, v208\n"
      "v_or3_b32 v208, v114, v115, v208\n"
      "v_or3_b32 v209, v116, v117, v209\n"
      "v_or3_b32 v209, v118, v119, v209\n"
      "v_fma_mix_f32 v112, v112, v200, v204 op_sel_hi:[1,0,0]\n"
      "v_fma_mix_f32 v113, v113, v201, v205 op_sel_hi:[1,0,0]\n"
      "v_fma_mix_f32 v114, v114, v202, v206 op_sel_hi:[1,0,0]\n"
      "v_fma_mix_f32 v115, v115, v203, v207 op_sel_hi:[1,0,0]\n"
      "v_fma_mix_f32 v116, v116, v200, v204 op_sel_hi:[1,0,0]\n"
      "v_fma_mix_f32 v117, v117, v201, v205 op_sel_hi:[1,0,0]\n"
      "v_fma_mix_f32 v118, v118, v202, v206 op_sel_hi:[1,0,0]\n"
      "v_fma_mix_f32 v119, v119, v203, v207 op_sel_hi:[1,0,0]\n"
      "v_cvt_pk_bf16_f32 v185, v112, v116\n"
      "v_cvt_pk_bf16_f32 v189, v113, v117\n"
      "v_cvt_pk_bf16_f32 v193, v114, v118\n"
      "v_cvt_pk_bf16_f32 v197, v115, v119\n"
      "s_barrier\n"
      "ds_read_b128 v[88:91], v242 offset:16384\n"
      "ds_read_b128 v[92:95], v242 offset:17408\n"
      "ds_read_b128 v[96:99], v242 offset:18432\n"
      "ds_read_b128 v[100:103], v242 offset:19456\n"
      "s_setprio 1\n"
      "s_waitcnt vmcnt(4)\n"
      "v_or3_b32 v208, v120, v124, v208\n"
      "v_or3_b32 v209, v128, v132, v209\n"
      "v_fma_mix_f32 v120, v120, v200, v204 op_sel_hi:[1,0,0]\n"
      "v_fma_mix_f32 v124, v124, v200, v204 op_sel_hi:[1,0,0]\n"
      "v_fma_mix_f32 v128, v128, v200, v204 op_sel_hi:[1,0,0]\n"
      "v_fma_mix_f32 v132, v132, v200, v204 op_sel_hi:[1,0,0]\n"
      "v_cvt_pk_bf16_f32 v186, v120, v124\n"
      "v_cvt_pk_bf16_f32 v187, v128, v132\n"
      "ds_write_b128 v244, v[184:187] offset:0\n"
      "v_or3_b32 v208, v121, v125, v208\n"
      "v_or3_b32 v209, v129, v133, v209\n"
      "v_fma_mix_f32 v121, v121, v201, v205 op_sel_hi:[1,0,0]\n"
      "v_fma_mix_f32 v125, v125, v201, v205 op_sel_hi:[1,0,0]\n"
      "v_fma_mix_f32 v129, v129, v201, v205 op_sel_hi:[1,0,0]\n"
      "v_fma_mix_f32 v133, v133, v201, v205 op_sel_hi:[1,0,0]\n"
      "v_cvt_pk_bf16_f32 v190, v121, v125\n"
      "v_cvt_pk_bf16_f32 v191, v129, v133\n"
      "ds_write_b128 v244, v[188:191] offset:1024\n"
      "v_or3_b32 v208, v122, v126, v208\n"
      "v_or3_b32 v209, v130, v134, v209\n"
      "v_fma_mix_f32 v122, v122, v202, v206 op_sel_hi:[1,0,0]\n"
      "v_fma_mix_f32 v126, v126, v202, v206 op_sel_hi:[1,0,0]\n"
      "v_fma_mix_f32 v130, v130, v202, v206 op_sel_hi:[1,0,0]\n"
      "v_fma_mix_f32 v134, v134, v202, v206 op_sel_hi:[1,0,0]\n"
      "v_cvt_pk_bf16_f32 v194, v122, v126\n"
      "v_cvt_pk_bf16_f32 v195, v130, v134\n"
      "ds_write_b128 v245, v[192:195] offset:0\n"
      "v_or3_b32 v208, v123, v127, v208\n"
      "v_or3_b32 v209, v131, v135, v209\n"
      "v_fma_mix_f32 v123, v123, v203, v207 op_sel_hi:[1,0,0]\n"
      "v_fma_mix_f32 v127, v127, v203, v207 op_sel_hi:[1,0,0]\n"
      "v_fma_mix_f32 v131, v131, v203, v207 op_sel_hi:[1,0,0]\n"
      "v_fma_mix_f32 v135, v135, v203, v207 op_sel_hi:[1,0,0]\n"
      "v_cvt_pk_bf16_f32 v198, v123, v127\n"
      "v_cvt_pk_bf16_f32 v199, v131, v135\n"
      "ds_write_b128 v245, v[196:199] offset:1024\n"
      "s_setprio 0\n"
      "s_mov_b32 s63, 9510912\n"
      "buffer_load_dwordx4 v[152:155], v210, s[20:23], s63 offen\n"
      "buffer_load_dwordx4 v[156:159], v211, s[20:23], s63 offen\n"
      "buffer_load_dwordx4 v[160:163], v212, s[20:23], s63 offen\n"
      "buffer_load_dwordx4 v[164:167], v213, s[20:23], s63 offen\n"
      "s_add_u32 m0, s43, 6144\n"
      "s_add_u32 s61, s36, 384\n"
      "buffer_load_dwordx4 v243, s[16:19], s61 offen lds\n"
      "s_add_u32 m0, s43, 8192\n"
      "s_add_u32 s61, s36, 512\n"
      "buffer_load_dwordx4 v243, s[16:19], s61 offen lds\n"
      "s_waitcnt lgkmcnt(0)\n"
      "s_barrier\n"
      "ds_read_b128 v[104:107], v242 offset:0\n"
      "ds_read_b128 v[108:111], v242 offset:1024\n"
      "ds_read_b128 v[112:115], v242 offset:2048\n"
      "ds_read_b128 v[116:119], v242 offset:3072\n"
      "s_add_u32 m0, s43, 24576\n"
      "s_add_u32 s61, s36, 18816\n"
      "buffer_load_dwordx4 v243, s[16:19], s61 offen lds\n"
      "s_add_u32 m0, s43, 26624\n"
      "s_add_u32 s61, s36, 18944\n"
      "buffer_load_dwordx4 v243, s[16:19], s61 offen lds\n"
      "ds_read_b64 v[184:185], v246 offset:512\n"
      "ds_read_b64 v[186:187], v246 offset:1152\n"
      "s_waitcnt lgkmcnt(0)\n"
      "v_lshlrev_b32 v200, 16, v184\n"
      "v_and_b32 v201, 0xffff0000, v184\n"
      "v_lshlrev_b32 v204, 16, v186\n"
      "v_and_b32 v205, 0xffff0000, v186\n"
      "v_lshlrev_b32 v202, 16, v185\n"
      "v_and_b32 v203, 0xffff0000, v185\n"
      "v_lshlrev_b32 v206, 16, v187\n"
      "v_and_b32 v207, 0xffff0000, v187\n"
      "v_mul_f32_e64 v204, -v204, v200\n"
      "v_mul_f32_e64 v205, -v205, v201\n"
      "v_mul_f32 v200, 0x4b800000, v200\n"
      "v_mul_f32 v201, 0x4b800000, v201\n"
      "v_mul_f32_e64 v206, -v206, v202\n"
      "v_mul_f32_e64 v207, -v207, v203\n"
      "v_mul_f32 v202, 0x4b800000, v202\n"
      "v_mul_f32 v203, 0x4b800000, v203\n"
      "s_waitcnt vmcnt(10)\n"
      "v_or3_b32 v208, v136, v137, v208\n"
      "v_or3_b32 v208, v138, v139, v208\n"
      "v_or3_b32 v209, v140, v141, v209\n"
      "v_or3_b32 v209, v142, v143, v209\n"
      "v_fma_mix_f32 v136, v136, v200, v204 op_sel_hi:[1,0,0]\n"
      "v_fma_mix_f32 v137, v137, v201, v205 op_sel_hi:[1,0,0]\n"
      "v_fma_mix_f32 v138, v138, v202, v206 op_sel_hi:[1,0,0]\n"
      "v_fma_mix_f32 v139, v139, v203, v207 op_sel_hi:[1,0,0]\n"
      "v_fma_mix_f32 v140, v140, v200, v204 op_sel_hi:[1,0,0]\n"
      "v_fma_mix_f32 v141, v141, v201, v205 op_sel_hi:[1,0,0]\n"
      "v_fma_mix_f32 v142, v142, v202, v206 op_sel_hi:[1,0,0]\n"
      "v_fma_mix_f32 v143, v143, v203, v207 op_sel_hi:[1,0,0]\n"
      "v_cvt_pk_bf16_f32 v184, v136, v140\n"
      "v_cvt_pk_bf16_f32 v188, v137, v141\n"
      "v_cvt_pk_bf16_f32 v192, v138, v142\n"
      "v_cvt_pk_bf16_f32 v196, v139, v143\n"
      "s_waitcnt vmcnt(8)\n"
      "v_or3_b32 v208, v144, v145, v208\n"
      "v_or3_b32 v208, v146, v147, v208\n"
      "v_or3_b32 v209, v148, v149, v209\n"
      "v_or3_b32 v209, v150, v151, v209\n"
      "v_fma_mix_f32 v144, v144, v200, v204 op_sel_hi:[1,0,0]\n"
      "v_fma_mix_f32 v145, v145, v201, v205 op_sel_hi:[1,0,0]\n"
      "v_fma_mix_f32 v146, v146, v202, v206 op_sel_hi:[1,0,0]\n"
      "v_fma_mix_f32 v147, v147, v203, v207 op_sel_hi:[1,0,0]\n"
      "v_fma_mix_f32 v148, v148, v200, v204 op_sel_hi:[1,0,0]\n"
      "v_fma_mix_f32 v149, v149, v201, v205 op_sel_hi:[1,0,0]\n"
      "v_fma_mix_f32 v150, v150, v202, v206 op_sel_hi:[1,0,0]\n"
      "v_fma_mix_f32 v151, v151, v203, v207 op_sel_hi:[1,0,0]\n"
      "v_cvt_pk_bf16_f32 v185, v144, v148\n"
      "v_cvt_pk_bf16_f32 v189, v145, v149\n"
      "v_cvt_pk_bf16_f32 v193, v146, v150\n"
      "v_cvt_pk_bf16_f32 v197, v147, v151\n"
      "s_waitcnt vmcnt(2)\n"
      "s_barrier\n"
      "ds_read_b128 v[120:123], v242 offset:16384\n"
      "ds_read_b128 v[124:127], v242 offset:17408\n"
      "ds_read_b128 v[128:131], v242 offset:18432\n"
      "ds_read_b128 v[132:135], v242 offset:19456\n"
      "s_setprio 1\n"
      "v_or3_b32 v208, v152, v156, v208\n"
      "v_or3_b32 v209, v160, v164, v209\n"
      "v_fma_mix_f32 v152, v152, v200, v204 op_sel_hi:[1,0,0]\n"
      "v_fma_mix_f32 v156, v156, v200, v204 op_sel_hi:[1,0,0]\n"
      "v_fma_mix_f32 v160, v160, v200, v204 op_sel_hi:[1,0,0]\n"
      "v_fma_mix_f32 v164, v164, v200, v204 op_sel_hi:[1,0,0]\n"
      "v_cvt_pk_bf16_f32 v186, v152, v156\n"
      "v_cvt_pk_bf16_f32 v187, v160, v164\n"
      "ds_write_b128 v244, v[184:187] offset:0\n"
      "v_or3_b32 v208, v153, v157, v208\n"
      "v_or3_b32 v209, v161, v165, v209\n"
      "v_fma_mix_f32 v153, v153, v201, v205 op_sel_hi:[1,0,0]\n"
      "v_fma_mix_f32 v157, v157, v201, v205 op_sel_hi:[1,0,0]\n"
      "v_fma_mix_f32 v161, v161, v201, v205 op_sel_hi:[1,0,0]\n"
      "v_fma_mix_f32 v165, v165, v201, v205 op_sel_hi:[1,0,0]\n"
      "v_cvt_pk_bf16_f32 v190, v153, v157\n"
      "v_cvt_pk_bf16_f32 v191, v161, v165\n"
      "ds_write_b128 v244, v[188:191] offset:1024\n"
      "v_or3_b32 v208, v154, v158, v208\n"
      "v_or3_b32 v209, v162, v166, v209\n"
      "v_fma_mix_f32 v154, v154, v202, v206 op_sel_hi:[1,0,0]\n"
      "v_fma_mix_f32 v158, v158, v202, v206 op_sel_hi:[1,0,0]\n"
      "v_fma_mix_f32 v162, v162, v202, v206 op_sel_hi:[1,0,0]\n"
      "v_fma_mix_f32 v166, v166, v202, v206 op_sel_hi:[1,0,0]\n"
      "v_cvt_pk_bf16_f32 v194, v154, v158\n"
      "v_cvt_pk_bf16_f32 v195, v162, v166\n"
      "ds_write_b128 v245, v[192:195] offset:0\n"
      "v_or3_b32 v208, v155, v159, v208\n"
      "v_or3_b32 v209, v163, v167, v209\n"
      "v_fma_mix_f32 v155, v155, v203, v207 op_sel_hi:[1,0,0]\n"
      "v_fma_mix_f32 v159, v159, v203, v207 op_sel_hi:[1,0,0]\n"
      "v_fma_mix_f32 v163, v163, v203, v207 op_sel_hi:[1,0,0]\n"
      "v_fma_mix_f32 v167, v167, v203, v207 op_sel_hi:[1,0,0]\n"
      "v_cvt_pk_bf16_f32 v198, v155, v159\n"
      "v_cvt_pk_bf16_f32 v199, v163, v167\n"
      "ds_write_b128 v245, v[196:199] offset:1024\n"
      "v_or_b32 v208, v208, v209\n"
      "v_cmp_lt_u32 vcc, 0x7f, v208\n"
      "s_or_b64 s[66:67], s[66:67], vcc\n"
      "s_or_b32 s66, s66, s67\n"
      "v_mov_b32 v201, s45\n"
      "v_mov_b32 v200, s66\n"
      "ds_write_b32 v201, v200\n"
      "s_setprio 0\n"
      "s_add_u32 m0, s43, 43008\n"
      "s_add_u32 s61, s36, 37248\n"
      "buffer_load_dwordx4 v243, s[16:19], s61 offen lds\n"
      "s_add_u32 m0, s43, 45056\n"
      "s_add_u32 s61, s36, 37376\n"
      "buffer_load_dwordx4 v243, s[16:19], s61 offen lds\n"
      "s_waitcnt lgkmcnt(0)\n"
      "s_waitcnt vmcnt(2)\n"
      "ds_read_b128 v[168:171], v240 offset:0\n"
      "ds_read_b128 v[172:175], v241 offset:0\n"
      "ds_read_b128 v[176:179], v240 offset:2048\n"
      "ds_read_b128 v[180:183], v241 offset:2048\n"
      "ds_read_b128 v[184:187], v240 offset:4096\n"
      "ds_read_b128 v[188:191], v241 offset:4096\n"
      "s_barrier\n"
      "ds_read_b128 v[136:139], v242 offset:0\n"
      "ds_read_b128 v[140:143], v242 offset:1024\n"
      "ds_read_b128 v[144:147], v242 offset:2048\n"
      "ds_read_b128 v[148:151], v242 offset:3072\n"
      "s_add_u32 m0, s43, 61440\n"
      "s_add_u32 s61, s36, 55680\n"
      "buffer_load_dwordx4 v243, s[16:19], s61 offen lds\n"
      "s_add_u32 m0, s43, 63488\n"
      "s_add_u32 s61, s36, 55808\n"
      "buffer_load_dwordx4 v243, s[16:19], s61 offen lds\n"
      "s_waitcnt lgkmcnt(0)\n"
      "v_mfma_f32_16x16x32_bf16 v[152:155], v[168:171], v[8:11], 0\n"
      "ds_read_b128 v[192:195], v240 offset:6144\n"
      "v_mfma_f32_16x16x32_bf16 v[156:159], v[168:171], v[12:15], 0\n"
      "v_mfma_f32_16x16x32_bf16 v[152:155], v[172:175], v[16:19], v[152:155]\n"
      "ds_read_b128 v[196:199], v241 offset:6144\n"
      "v_mfma_f32_16x16x32_bf16 v[156:159], v[172:175], v[20:23], v[156:159]\n"
      "v_mfma_f32_16x16x32_bf16 v[152:155], v[176:179], v[24:27], v[152:155]\n"
      "ds_read_b128 v[168:171], v240 offset:8192\n"
      "v_mfma_f32_16x16x32_bf16 v[156:159], v[176:179], v[28:31], v[156:159]\n"
      "v_mfma_f32_16x16x32_bf16 v[152:155], v[180:183], v[32:35], v[152:155]\n"
      "ds_read_b128 v[172:175], v241 offset:8192\n"
      "v_mfma_f32_16x16x32_bf16 v[156:159], v[180:183], v[36:39], v[156:159]\n"
      "v_mfma_f32_16x16x32_bf16 v[152:155], v[184:187], v[40:43], v[152:155]\n"
      "ds_read_b128 v[176:179], v240 offset:10240\n"
      "v_mfma_f32_16x16x32_bf16 v[156:159], v[184:187], v[44:47], v[156:159]\n"
      "v_mfma_f32_16x16x32_bf16 v[152:155], v[188:191], v[48:51], v[152:155]\n"
      "ds_read_b128 v[180:183], v241 offset:10240\n"
      "v_mfma_f32_16x16x32_bf16 v[156:159], v[188:191], v[52:55], v[156:159]\n"
      "s_waitcnt lgkmcnt(5)\n"
      "v_mfma_f32_16x16x32_bf16 v[152:155], v[192:195], v[56:59], v[152:155]\n"
      "ds_read_b128 v[184:187], v240 offset:12288\n"
      "v_mfma_f32_16x16x32_bf16 v[156:159], v[192:195], v[60:63], v[156:159]\n"
      "s_waitcnt lgkmcnt(5)\n"
      "v_mfma_f32_16x16x32_bf16 v[152:155], v[196:199], v[64:67], v[152:155]\n"
      "ds_read_b128 v[188:191], v241 offset:12288\n"
      "v_mfma_f32_16x16x32_bf16 v[156:159], v[196:199], v[68:71], v[156:159]\n"
      "s_waitcnt lgkmcnt(5)\n"
      "v_mfma_f32_16x16x32_bf16 v[152:155], v[168:171], v[72:75], v[152:155]\n"
      "ds_read_b128 v[192:195], v240 offset:14336\n"
      "v_mfma_f32_16x16x32_bf16 v[156:159], v[168:171], v[76:79], v[156:159]\n"
      "s_add_u32 m0, s43, 83968\n"
      "s_add_u32 s61, s36, 74368\n"
      "buffer_load_dwordx4 v243, s[16:19], s61 offen lds\n"
      "s_add_u32 m0, s43, 86016\n"
      "s_add_u32 s61, s36, 74496\n"
      "buffer_load_dwordx4 v243, s[16:19], s61 offen lds\n"
      "s_add_u32 m0, s43, 88064\n"
      "s_add_u32 s61, s36, 74624\n"
      "buffer_load_dwordx4 v243, s[16:19], s61 offen lds\n"
      "s_add_u32 m0, s43, 90112\n"
      "s_add_u32 s61, s36, 74752\n"
      "buffer_load_dwordx4 v243, s[16:19], s61 offen lds\n"
      "s_waitcnt vmcnt(4)\n"
      "s_barrier\n"
      "s_waitcnt lgkmcnt(5)\n"
      "v_mfma_f32_16x16x32_bf16 v[152:155], v[172:175], v[80:83], v[152:155]\n"
      "ds_read_b128 v[196:199], v241 offset:14336\n"
      "v_mfma_f32_16x16x32_bf16 v[156:159], v[172:175], v[84:87], v[156:159]\n"
      "s_waitcnt lgkmcnt(5)\n"
      "v_mfma_f32_16x16x32_bf16 v[152:155], v[176:179], v[88:91], v[152:155]\n"
      "ds_read_b128 v[168:171], v240 offset:16384\n"
      "v_mfma_f32_16x16x32_bf16 v[156:159], v[176:179], v[92:95], v[156:159]\n"
      "s_waitcnt lgkmcnt(5)\n"
      "v_mfma_f32_16x16x32_bf16 v[152:155], v[180:183], v[96:99], v[152:155]\n"
      "ds_read_b128 v[172:175], v241 offset:16384\n"
      "v_mfma_f32_16x16x32_bf16 v[156:159], v[180:183], v[100:103], v[156:159]\n"
      "s_waitcnt lgkmcnt(5)\n"
      "v_mfma_f32_16x16x32_bf16 v[152:155], v[184:187], v[104:107], v[152:155]\n"
      "ds_read_b128 v[176:179], v240 offset:36864\n"
      "v_mfma_f32_16x16x32_bf16 v[156:159], v[184:187], v[108:111], v[156:159]\n"
      "s_waitcnt lgkmcnt(5)\n"
      "v_mfma_f32_16x16x32_bf16 v[152:155], v[188:191], v[112:115], v[152:155]\n"
      "ds_read_b128 v[180:183], v241 offset:36864\n"
      "v_mfma_f32_16x16x32_bf16 v[156:159], v[188:191], v[116:119], v[156:159]\n"
      "s_waitcnt lgkmcnt(5)\n"
      "v_mfma_f32_16x16x32_bf16 v[152:155], v[192:195], v[120:123], v[152:155]\n"
      "ds_read_b128 v[184:187], v240 offset:38912\n"
      "v_mfma_f32_16x16x32_bf16 v[156:159], v[192:195], v[124:127], v[156:159]\n"
      "s_waitcnt lgkmcnt(5)\n"
      "v_mfma_f32_16x16x32_bf16 v[152:155], v[196:199], v[128:131], v[152:155]\n"
      "ds_read_b128 v[188:191], v241 offset:38912\n"
      "v_mfma_f32_16x16x32_bf16 v[156:159], v[196:199], v[132:135], v[156:159]\n"
      "s_waitcnt lgkmcnt(5)\n"
      "v_mfma_f32_16x16x32_bf16 v[152:155], v[168:171], v[136:139], v[152:155]\n"
      "ds_read_b128 v[192:195], v240 offset:40960\n"
      "v_mfma_f32_16x16x32_bf16 v[156:159], v[168:171], v[140:143], v[156:159]\n"
      "s_waitcnt lgkmcnt(5)\n"
      "v_mfma_f32_16x16x32_bf16 v[152:155], v[172:175], v[144:147], v[152:155]\n"
      "ds_read_b128 v[196:199], v241 offset:40960\n"
      "v_mfma_f32_16x16x32_bf16 v[156:159], v[172:175], v[148:151], v[156:159]\n"
      "s_waitcnt lgkmcnt(5)\n"
      "v_mfma_f32_16x16x32_bf16 v[160:163], v[176:179], v[8:11], 0\n"
      "ds_read_b128 v[168:171], v240 offset:43008\n"
      "v_mfma_f32_16x16x32_bf16 v[164:167], v[176:179], v[12:15], 0\n"
      "s_waitcnt lgkmcnt(5)\n"
      "v_mfma_f32_16x16x32_bf16 v[160:163], v[180:183], v[16:19], v[160:163]\n"
      "ds_read_b128 v[172:175], v241 offset:43008\n"
      "v_mfma_f32_16x16x32_bf16 v[164:167], v[180:183], v[20:23], v[164:167]\n"
      "s_waitcnt lgkmcnt(5)\n"
      "v_mfma_f32_16x16x32_bf16 v[160:163], v[184:187], v[24:27], v[160:163]\n"
      "ds_read_b128 v[176:179], v240 offset:45056\n"
      "v_mfma_f32_16x16x32_bf16 v[164:167], v[184:187], v[28:31], v[164:167]\n"
      "s_waitcnt lgkmcnt(5)\n"
      "v_mfma_f32_16x16x32_bf16 v[160:163], v[188:191], v[32:35], v[160:163]\n"
      "ds_read_b128 v[180:183], v241 offset:45056\n"
      "v_mfma_f32_16x16x32_bf16 v[164:167], v[188:191], v[36:39], v[164:167]\n"
      "s_waitcnt lgkmcnt(5)\n"
      "v_mfma_f32_16x16x32_bf16 v[160:163], v[192:195], v[40:43], v[160:163]\n"
      "ds_read_b128 v[184:187], v240 offset:47104\n"
      "v_mfma_f32_16x16x32_bf16 v[164:167], v[192:195], v[44:47], v[164:167]\n"
      "s_waitcnt lgkmcnt(5)\n"
      "v_mfma_f32_16x16x32_bf16 v[160:163], v[196:199], v[48:51], v[160:163]\n"
      "ds_read_b128 v[188:191], v241 offset:47104\n"
      "v_mfma_f32_16x16x32_bf16 v[164:167], v[196:199], v[52:55], v[164:167]\n"
      "s_waitcnt lgkmcnt(5)\n"
      "v_mfma_f32_16x16x32_bf16 v[160:163], v[168:171], v[56:59], v[160:163]\n"
      "ds_read_b128 v[192:195], v240 offset:49152\n"
      "v_mfma_f32_16x16x32_bf16 v[164:167], v[168:171], v[60:63], v[164:167]\n"
      "s_waitcnt lgkmcnt(5)\n"
      "v_mfma_f32_16x16x32_bf16 v[160:163], v[172:175], v[64:67], v[160:163]\n"
      "ds_read_b128 v[196:199], v241 offset:49152\n"
      "v_mfma_f32_16x16x32_bf16 v[164:167], v[172:175], v[68:71], v[164:167]\n"
      "s_waitcnt lgkmcnt(5)\n"
      "v_mfma_f32_16x16x32_bf16 v[160:163], v[176:179], v[72:75], v[160:163]\n"
      "ds_read_b128 v[168:171], v240 offset:51200\n"
      "v_mfma_f32_16x16x32_bf16 v[164:167], v[176:179], v[76:79], v[164:167]\n"
      "s_add_u32 m0, s43, 120832\n"
      "s_add_u32 s61, s36, 111232\n"
      "buffer_load_dwordx4 v243, s[16:19], s61 offen lds\n"
      "s_add_u32 m0, s43, 122880\n"
      "s_add_u32 s61, s36, 111360\n"
      "buffer_load_dwordx4 v243, s[16:19], s61 offen lds\n"
      "s_add_u32 m0, s43, 124928\n"
      "s_add_u32 s61, s36, 111488\n"
      "buffer_load_dwordx4 v243, s[16:19], s61 offen lds\n"
      "s_add_u32 m0, s43, 126976\n"
      "s_add_u32 s61, s36, 111616\n"
      "buffer_load_dwordx4 v243, s[16:19], s61 offen lds\n"
      "v_cvt_pk_bf16_f32 v152, v152, v156\n"
      "v_cvt_pk_bf16_f32 v153, v153, v157\n"
      "v_cvt_pk_bf16_f32 v154, v154, v158\n"
      "v_cvt_pk_bf16_f32 v155, v155, v159\n"
      "s_add_u32 s62, s37, 0\n"
      "buffer_store_dword v152, v247, s[68:71], s62 offen sc1\n"
      "s_add_u32 s62, s37, 9216\n"
      "buffer_store_dword v153, v247, s[68:71], s62 offen sc1\n"
      "s_add_u32 s62, s37, 18432\n"
      "buffer_store_dword v154, v247, s[68:71], s62 offen sc1\n"
      "s_add_u32 s62, s37, 27648\n"
      "buffer_store_dword v155, v247, s[68:71], s62 offen sc1\n"
      "s_waitcnt vmcnt(8)\n"
      "s_barrier\n"
      "s_waitcnt lgkmcnt(5)\n"
      "v_mfma_f32_16x16x32_bf16 v[160:163], v[180:183], v[80:83], v[160:163]\n"
      "ds_read_b128 v[172:175], v241 offset:51200\n"
      "v_mfma_f32_16x16x32_bf16 v[164:167], v[180:183], v[84:87], v[164:167]\n"
      "s_waitcnt lgkmcnt(5)\n"
      "v_mfma_f32_16x16x32_bf16 v[160:163], v[184:187], v[88:91], v[160:163]\n"
      "ds_read_b128 v[176:179], v240 offset:53248\n"
      "v_mfma_f32_16x16x32_bf16 v[164:167], v[184:187], v[92:95], v[164:167]\n"
      "s_waitcnt lgkmcnt(5)\n"
      "v_mfma_f32_16x16x32_bf16 v[160:163], v[188:191], v[96:99], v[160:163]\n"
      "ds_read_b128 v[180:183], v241 offset:53248\n"
      "v_mfma_f32_16x16x32_bf16 v[164:167], v[188:191], v[100:103], v[164:167]\n"
      "s_waitcnt lgkmcnt(5)\n"
      "v_mfma_f32_16x16x32_bf16 v[160:163], v[192:195], v[104:107], v[160:163]\n"
      "ds_read_b128 v[184:187], v214 offset:8192\n"
      "v_mfma_f32_16x16x32_bf16 v[164:167], v[192:195], v[108:111], v[164:167]\n"
      "s_waitcnt lgkmcnt(5)\n"
      "v_mfma_f32_16x16x32_bf16 v[160:163], v[196:199], v[112:115], v[160:163]\n"
      "ds_read_b128 v[188:191], v215 offset:8192\n"
      "v_mfma_f32_16x16x32_bf16 v[164:167], v[196:199], v[116:119], v[164:167]\n"
      "s_waitcnt lgkmcnt(5)\n"
      "v_mfma_f32_16x16x32_bf16 v[160:163], v[168:171], v[120:123], v[160:163]\n"
      "ds_read_b128 v[192:195], v214 offset:10240\n"
      "v_mfma_f32_16x16x32_bf16 v[164:167], v[168:171], v[124:127], v[164:167]\n"
      "s_waitcnt lgkmcnt(5)\n"
      "v_mfma_f32_16x16x32_bf16 v[160:163], v[172:175], v[128:131], v[160:163]\n"
      "ds_read_b128 v[196:199], v215 offset:10240\n"
      "v_mfma_f32_16x16x32_bf16 v[164:167], v[172:175], v[132:135], v[164:167]\n"
      "s_waitcnt lgkmcnt(5)\n"
      "v_mfma_f32_16x16x32_bf16 v[160:163], v[176:179], v[136:139], v[160:163]\n"
      "ds_read_b128 v[168:171], v214 offset:12288\n"
      "v_mfma_f32_16x16x32_bf16 v[164:167], v[176:179], v[140:143], v[164:167]\n"
      "s_waitcnt lgkmcnt(5)\n"
      "v_mfma_f32_16x16x32_bf16 v[160:163], v[180:183], v[144:147], v[160:163]\n"
      "ds_read_b128 v[172:175], v215 offset:12288\n"
      "v_mfma_f32_16x16x32_bf16 v[164:167], v[180:183], v[148:151], v[164:167]\n"
      "s_waitcnt lgkmcnt(5)\n"
      "v_mfma_f32_16x16x32_bf16 v[152:155], v[184:187], v[8:11], 0\n"
      "ds_read_b128 v[176:179], v214 offset:14336\n"
      "v_mfma_f32_16x16x32_bf16 v[156:159], v[184:187], v[12:15], 0\n"
      "s_waitcnt lgkmcnt(5)\n"
      "v_mfma_f32_16x16x32_bf16 v[152:155], v[188:191], v[16:19], v[152:155]\n"
      "ds_read_b128 v[180:183], v215 offset:14336\n"
      "v_mfma_f32_16x16x32_bf16 v[156:159], v[188:191], v[20:23], v[156:159]\n"
      "s_waitcnt lgkmcnt(5)\n"
      "v_mfma_f32_16x16x32_bf16 v[152:155], v[192:195], v[24:27], v[152:155]\n"
      "ds_read_b128 v[184:187], v214 offset:16384\n"
      "v_mfma_f32_16x16x32_bf16 v[156:159], v[192:195], v[28:31], v[156:159]\n"
      "s_waitcnt lgkmcnt(5)\n"
      "v_mfma_f32_16x16x32_bf16 v[152:155], v[196:199], v[32:35], v[152:155]\n"
      "ds_read_b128 v[188:191], v215 offset:16384\n"
      "v_mfma_f32_16x16x32_bf16 v[156:159], v[196:199], v[36:39], v[156:159]\n"
      "s_waitcnt lgkmcnt(5)\n"
      "v_mfma_f32_16x16x32_bf16 v[152:155], v[168:171], v[40:43], v[152:155]\n"
      "ds_read_b128 v[192:195], v214 offset:18432\n"
      "v_mfma_f32_16x16x32_bf16 v[156:159], v[168:171], v[44:47], v[156:159]\n"
      "s_waitcnt lgkmcnt(5)\n"
      "v_mfma_f32_16x16x32_bf16 v[152:155], v[172:175], v[48:51], v[152:155]\n"
      "ds_read_b128 v[196:199], v215 offset:18432\n"
      "v_mfma_f32_16x16x32_bf16 v[156:159], v[172:175], v[52:55], v[156:159]\n"
      "s_waitcnt lgkmcnt(5)\n"
      "v_mfma_f32_16x16x32_bf16 v[152:155], v[176:179], v[56:59], v[152:155]\n"
      "ds_read_b128 v[168:171], v214 offset:20480\n"
      "v_mfma_f32_16x16x32_bf16 v[156:159], v[176:179], v[60:63], v[156:159]\n"
      "s_waitcnt lgkmcnt(5)\n"
      "v_mfma_f32_16x16x32_bf16 v[152:155], v[180:183], v[64:67], v[152:155]\n"
      "ds_read_b128 v[172:175], v215 offset:20480\n"
      "v_mfma_f32_16x16x32_bf16 v[156:159], v[180:183], v[68:71], v[156:159]\n"
      "s_waitcnt lgkmcnt(5)\n"
      "v_mfma_f32_16x16x32_bf16 v[152:155], v[184:187], v[72:75], v[152:155]\n"
      "ds_read_b128 v[176:179], v214 offset:22528\n"
      "v_mfma_f32_16x16x32_bf16 v[156:159], v[184:187], v[76:79], v[156:159]\n"
      "s_add_u32 m0, s43, 10240\n"
      "s_add_u32 s61, s36, 148096\n"
      "buffer_load_dwordx4 v243, s[16:19], s61 offen lds\n"
      "s_add_u32 m0, s43, 12288\n"
      "s_add_u32 s61, s36, 148224\n"
      "buffer_load_dwordx4 v243, s[16:19], s61 offen lds\n"
      "s_add_u32 m0, s43, 14336\n"
      "s_add_u32 s61, s36, 148352\n"
      "buffer_load_dwordx4 v243, s[16:19], s61 offen lds\n"
      "s_add_u32 m0, s43, 16384\n"
      "s_add_u32 s61, s36, 148480\n"
      "buffer_load_dwordx4 v243, s[16:19], s61 offen lds\n"
      "v_cvt_pk_bf16_f32 v160, v160, v164\n"
      "v_cvt_pk_bf16_f32 v161, v161, v165\n"
      "v_cvt_pk_bf16_f32 v162, v162, v166\n"
      "v_cvt_pk_bf16_f32 v163, v163, v167\n"
      "s_add_u32 s62, s37, 294912\n"
      "buffer_store_dword v160, v247, s[68:71], s62 offen sc1\n"
      "s_add_u32 s62, s37, 304128\n"
      "buffer_store_dword v161, v247, s[68:71], s62 offen sc1\n"
      "s_add_u32 s62, s37, 313344\n"
      "buffer_store_dword v162, v247, s[68:71], s62 offen sc1\n"
      "s_add_u32 s62, s37, 322560\n"
      "buffer_store_dword v163, v247, s[68:71], s62 offen sc1\n"
      "s_waitcnt vmcnt(12)\n"
      "s_barrier\n"
      "s_waitcnt lgkmcnt(5)\n"
      "v_mfma_f32_16x16x32_bf16 v[152:155], v[188:191], v[80:83], v[152:155]\n"
      "ds_read_b128 v[180:183], v215 offset:22528\n"
      "v_mfma_f32_16x16x32_bf16 v[156:159], v[188:191], v[84:87], v[156:159]\n"
      "s_waitcnt lgkmcnt(5)\n"
      "v_mfma_f32_16x16x32_bf16 v[152:155], v[192:195], v[88:91], v[152:155]\n"
      "ds_read_b128 v[184:187], v214 offset:24576\n"
      "v_mfma_f32_16x16x32_bf16 v[156:159], v[192:195], v[92:95], v[156:159]\n"
      "s_waitcnt lgkmcnt(5)\n"
      "v_mfma_f32_16x16x32_bf16 v[152:155], v[196:199], v[96:99], v[152:155]\n"
      "ds_read_b128 v[188:191], v215 offset:24576\n"
      "v_mfma_f32_16x16x32_bf16 v[156:159], v[196:199], v[100:103], v[156:159]\n"
      "s_waitcnt lgkmcnt(5)\n"
      "v_mfma_f32_16x16x32_bf16 v[152:155], v[168:171], v[104:107], v[152:155]\n"
      "ds_read_b128 v[192:195], v214 offset:45056\n"
      "v_mfma_f32_16x16x32_bf16 v[156:159], v[168:171], v[108:111], v[156:159]\n"
      "s_waitcnt lgkmcnt(5)\n"
      "v_mfma_f32_16x16x32_bf16 v[152:155], v[172:175], v[112:115], v[152:155]\n"
      "ds_read_b128 v[196:199], v215 offset:45056\n"
      "v_mfma_f32_16x16x32_bf16 v[156:159], v[172:175], v[116:119], v[156:159]\n"
      "s_waitcnt lgkmcnt(5)\n"
      "v_mfma_f32_16x16x32_bf16 v[152:155], v[176:179], v[120:123], v[152:155]\n"
      "ds_read_b128 v[168:171], v214 offset:47104\n"
      "v_mfma_f32_16x16x32_bf16 v[156:159], v[176:179], v[124:127], v[156:159]\n"
      "s_waitcnt lgkmcnt(5)\n"
      "v_mfma_f32_16x16x32_bf16 v[152:155], v[180:183], v[128:131], v[152:155]\n"
      "ds_read_b128 v[172:175], v215 offset:47104\n"
      "v_mfma_f32_16x16x32_bf16 v[156:159], v[180:183], v[132:135], v[156:159]\n"
      "s_waitcnt lgkmcnt(5)\n"
      "v_mfma_f32_16x16x32_bf16 v[152:155], v[184:187], v[136:139], v[152:155]\n"
      "ds_read_b128 v[176:179], v214 offset:49152\n"
      "v_mfma_f32_16x16x32_bf16 v[156:159], v[184:187], v[140:143], v[156:159]\n"
      "s_waitcnt lgkmcnt(5)\n"
      "v_mfma_f32_16x16x32_bf16 v[152:155], v[188:191], v[144:147], v[152:155]\n"
      "ds_read_b128 v[180:183], v215 offset:49152\n"
      "v_mfma_f32_16x16x32_bf16 v[156:159], v[188:191], v[148:151], v[156:159]\n"
      "s_waitcnt lgkmcnt(5)\n"
      "v_mfma_f32_16x16x32_bf16 v[160:163], v[192:195], v[8:11], 0\n"
      "ds_read_b128 v[184:187], v214 offset:51200\n"
      "v_mfma_f32_16x16x32_bf16 v[164:167], v[192:195], v[12:15], 0\n"
      "s_waitcnt lgkmcnt(5)\n"
      "v_mfma_f32_16x16x32_bf16 v[160:163], v[196:199], v[16:19], v[160:163]\n"
      "ds_read_b128 v[188:191], v215 offset:51200\n"
      "v_mfma_f32_16x16x32_bf16 v[164:167], v[196:199], v[20:23], v[164:167]\n"
      "s_waitcnt lgkmcnt(5)\n"
      "v_mfma_f32_16x16x32_bf16 v[160:163], v[168:171], v[24:27], v[160:163]\n"
      "ds_read_b128 v[192:195], v214 offset:53248\n"
      "v_mfma_f32_16x16x32_bf16 v[164:167], v[168:171], v[28:31], v[164:167]\n"
      "s_waitcnt lgkmcnt(5)\n"
      "v_mfma_f32_16x16x32_bf16 v[160:163], v[172:175], v[32:35], v[160:163]\n"
      "ds_read_b128 v[196:199], v215 offset:53248\n"
      "v_mfma_f32_16x16x32_bf16 v[164:167], v[172:175], v[36:39], v[164:167]\n"
      "s_waitcnt lgkmcnt(5)\n"
      "v_mfma_f32_16x16x32_bf16 v[160:163], v[176:179], v[40:43], v[160:163]\n"
      "ds_read_b128 v[168:171], v214 offset:55296\n"
      "v_mfma_f32_16x16x32_bf16 v[164:167], v[176:179], v[44:47], v[164:167]\n"
      "s_waitcnt lgkmcnt(5)\n"
      "v_mfma_f32_16x16x32_bf16 v[160:163], v[180:183], v[48:51], v[160:163]\n"
      "ds_read_b128 v[172:175], v215 offset:55296\n"
      "v_mfma_f32_16x16x32_bf16 v[164:167], v[180:183], v[52:55], v[164:167]\n"
      "s_waitcnt lgkmcnt(5)\n"
      "v_mfma_f32_16x16x32_bf16 v[160:163], v[184:187], v[56:59], v[160:163]\n"
      "ds_read_b128 v[176:179], v214 offset:57344\n"
      "v_mfma_f32_16x16x32_bf16 v[164:167], v[184:187], v[60:63], v[164:167]\n"
      "s_waitcnt lgkmcnt(5)\n"
      "v_mfma_f32_16x16x32_bf16 v[160:163], v[188:191], v[64:67], v[160:163]\n"
      "ds_read_b128 v[180:183], v215 offset:57344\n"
      "v_mfma_f32_16x16x32_bf16 v[164:167], v[188:191], v[68:71], v[164:167]\n"
      "s_waitcnt lgkmcnt(5)\n"
      "v_mfma_f32_16x16x32_bf16 v[160:163], v[192:195], v[72:75], v[160:163]\n"
      "ds_read_b128 v[184:187], v214 offset:59392\n"
      "v_mfma_f32_16x16x32_bf16 v[164:167], v[192:195], v[76:79], v[164:167]\n"
      "s_add_u32 m0, s43, 47104\n"
      "s_add_u32 s61, s36, 184960\n"
      "buffer_load_dwordx4 v243, s[16:19], s61 offen lds\n"
      "s_add_u32 m0, s43, 49152\n"
      "s_add_u32 s61, s36, 185088\n"
      "buffer_load_dwordx4 v243, s[16:19], s61 offen lds\n"
      "s_add_u32 m0, s43, 51200\n"
      "s_add_u32 s61, s36, 185216\n"
      "buffer_load_dwordx4 v243, s[16:19], s61 offen lds\n"
      "s_add_u32 m0, s43, 53248\n"
      "s_add_u32 s61, s36, 185344\n"
      "buffer_load_dwordx4 v243, s[16:19], s61 offen lds\n"
      "v_cvt_pk_bf16_f32 v152, v152, v156\n"
      "v_cvt_pk_bf16_f32 v153, v153, v157\n"
      "v_cvt_pk_bf16_f32 v154, v154, v158\n"
      "v_cvt_pk_bf16_f32 v155, v155, v159\n"
      "s_add_u32 s62, s37, 589824\n"
      "buffer_store_dword v152, v247, s[68:71], s62 offen sc1\n"
      "s_add_u32 s62, s37, 599040\n"
      "buffer_store_dword v153, v247, s[68:71], s62 offen sc1\n"
      "s_add_u32 s62, s37, 608256\n"
      "buffer_store_dword v154, v247, s[68:71], s62 offen sc1\n"
      "s_add_u32 s62, s37, 617472\n"
      "buffer_store_dword v155, v247, s[68:71], s62 offen sc1\n"
      "s_waitcnt vmcnt(12)\n"
      "s_barrier\n"
      "s_waitcnt lgkmcnt(5)\n"
      "v_mfma_f32_16x16x32_bf16 v[160:163], v[196:199], v[80:83], v[160:163]\n"
      "ds_read_b128 v[188:191], v215 offset:59392\n"
      "v_mfma_f32_16x16x32_bf16 v[164:167], v[196:199], v[84:87], v[164:167]\n"
      "s_waitcnt lgkmcnt(5)\n"
      "v_mfma_f32_16x16x32_bf16 v[160:163], v[168:171], v[88:91], v[160:163]\n"
      "ds_read_b128 v[192:195], v214 offset:61440\n"
      "v_mfma_f32_16x16x32_bf16 v[164:167], v[168:171], v[92:95], v[164:167]\n"
      "s_waitcnt lgkmcnt(5)\n"
      "v_mfma_f32_16x16x32_bf16 v[160:163], v[172:175], v[96:99], v[160:163]\n"
      "ds_read_b128 v[196:199], v215 offset:61440\n"
      "v_mfma_f32_16x16x32_bf16 v[164:167], v[172:175], v[100:103], v[164:167]\n"
      "s_waitcnt lgkmcnt(5)\n"
      "v_mfma_f32_16x16x32_bf16 v[160:163], v[176:179], v[104:107], v[160:163]\n"
      "ds_read_b128 v[168:171], v240 offset:0\n"
      "v_mfma_f32_16x16x32_bf16 v[164:167], v[176:179], v[108:111], v[164:167]\n"
      "s_waitcnt lgkmcnt(5)\n"
      "v_mfma_f32_16x16x32_bf16 v[160:163], v[180:183], v[112:115], v[160:163]\n"
      "ds_read_b128 v[172:175], v241 offset:0\n"
      "v_mfma_f32_16x16x32_bf16 v[164:167], v[180:183], v[116:119], v[164:167]\n"
      "s_waitcnt lgkmcnt(5)\n"
      "v_mfma_f32_16x16x32_bf16 v[160:163], v[184:187], v[120:123], v[160:163]\n"
      "ds_read_b128 v[176:179], v240 offset:2048\n"
      "v_mfma_f32_16x16x32_bf16 v[164:167], v[184:187], v[124:127], v[164:167]\n"
      "s_waitcnt lgkmcnt(5)\n"
      "v_mfma_f32_16x16x32_bf16 v[160:163], v[188:191], v[128:131], v[160:163]\n"
      "ds_read_b128 v[180:183], v241 offset:2048\n"
      "v_mfma_f32_16x16x32_bf16 v[164:167], v[188:191], v[132:135], v[164:167]\n"
      "s_waitcnt lgkmcnt(5)\n"
      "v_mfma_f32_16x16x32_bf16 v[160:163], v[192:195], v[136:139], v[160:163]\n"
      "ds_read_b128 v[184:187], v240 offset:4096\n"
      "v_mfma_f32_16x16x32_bf16 v[164:167], v[192:195], v[140:143], v[164:167]\n"
      "s_waitcnt lgkmcnt(5)\n"
      "v_mfma_f32_16x16x32_bf16 v[160:163], v[196:199], v[144:147], v[160:163]\n"
      "ds_read_b128 v[188:191], v241 offset:4096\n"
      "v_mfma_f32_16x16x32_bf16 v[164:167], v[196:199], v[148:151], v[164:167]\n"
      "s_waitcnt lgkmcnt(5)\n"
      "v_mfma_f32_16x16x32_bf16 v[152:155], v[168:171], v[8:11], 0\n"
      "ds_read_b128 v[192:195], v240 offset:6144\n"
      "v_mfma_f32_16x16x32_bf16 v[156:159], v[168:171], v[12:15], 0\n"
      "s_waitcnt lgkmcnt(5)\n"
      "v_mfma_f32_16x16x32_bf16 v[152:155], v[172:175], v[16:19], v[152:155]\n"
      "ds_read_b128 v[196:199], v241 offset:6144\n"
      "v_mfma_f32_16x16x32_bf16 v[156:159], v[172:175], v[20:23], v[156:159]\n"
      "s_waitcnt lgkmcnt(5)\n"
      "v_mfma_f32_16x16x32_bf16 v[152:155], v[176:179], v[24:27], v[152:155]\n"
      "ds_read_b128 v[168:171], v240 offset:8192\n"
      "v_mfma_f32_16x16x32_bf16 v[156:159], v[176:179], v[28:31], v[156:159]\n"
      "s_waitcnt lgkmcnt(5)\n"
      "v_mfma_f32_16x16x32_bf16 v[152:155], v[180:183], v[32:35], v[152:155]\n"
      "ds_read_b128 v[172:175], v241 offset:8192\n"
      "v_mfma_f32_16x16x32_bf16 v[156:159], v[180:183], v[36:39], v[156:159]\n"
      "s_waitcnt lgkmcnt(5)\n"
      "v_mfma_f32_16x16x32_bf16 v[152:155], v[184:187], v[40:43], v[152:155]\n"
      "ds_read_b128 v[176:179], v240 offset:10240\n"
      "v_mfma_f32_16x16x32_bf16 v[156:159], v[184:187], v[44:47], v[156:159]\n"
      "s_waitcnt lgkmcnt(5)\n"
      "v_mfma_f32_16x16x32_bf16 v[152:155], v[188:191], v[48:51], v[152:155]\n"
      "ds_read_b128 v[180:183], v241 offset:10240\n"
      "v_mfma_f32_16x16x32_bf16 v[156:159], v[188:191], v[52:55], v[156:159]\n"
      "s_waitcnt lgkmcnt(5)\n"
      "v_mfma_f32_16x16x32_bf16 v[152:155], v[192:195], v[56:59], v[152:155]\n"
      "ds_read_b128 v[184:187], v240 offset:12288\n"
      "v_mfma_f32_16x16x32_bf16 v[156:159], v[192:195], v[60:63], v[156:159]\n"
      "s_waitcnt lgkmcnt(5)\n"
      "v_mfma_f32_16x16x32_bf16 v[152:155], v[196:199], v[64:67], v[152:155]\n"
      "ds_read_b128 v[188:191], v241 offset:12288\n"
      "v_mfma_f32_16x16x32_bf16 v[156:159], v[196:199], v[68:71], v[156:159]\n"
      "s_waitcnt lgkmcnt(5)\n"
      "v_mfma_f32_16x16x32_bf16 v[152:155], v[168:171], v[72:75], v[152:155]\n"
      "ds_read_b128 v[192:195], v240 offset:14336\n"
      "v_mfma_f32_16x16x32_bf16 v[156:159], v[168:171], v[76:79], v[156:159]\n"
      "s_add_u32 m0, s43, 83968\n"
      "s_add_u32 s61, s36, 221824\n"
      "buffer_load_dwordx4 v243, s[16:19], s61 offen lds\n"
      "s_add_u32 m0, s43, 86016\n"
      "s_add_u32 s61, s36, 221952\n"
      "buffer_load_dwordx4 v243, s[16:19], s61 offen lds\n"
      "s_add_u32 m0, s43, 88064\n"
      "s_add_u32 s61, s36, 222080\n"
      "buffer_load_dwordx4 v243, s[16:19], s61 offen lds\n"
      "s_add_u32 m0, s43, 90112\n"
      "s_add_u32 s61, s36, 222208\n"
      "buffer_load_dwordx4 v243, s[16:19], s61 offen lds\n"
      "v_cvt_pk_bf16_f32 v160, v160, v164\n"
      "v_cvt_pk_bf16_f32 v161, v161, v165\n"
      "v_cvt_pk_bf16_f32 v162, v162, v166\n"
      "v_cvt_pk_bf16_f32 v163, v163, v167\n"
      "s_add_u32 s62, s37, 884736\n"
      "buffer_store_dword v160, v247, s[68:71], s62 offen sc1\n"
      "s_add_u32 s62, s37, 893952\n"
      "buffer_store_dword v161, v247, s[68:71], s62 offen sc1\n"
      "s_add_u32 s62, s37, 903168\n"
      "buffer_store_dword v162, v247, s[68:71], s62 offen sc1\n"
      "s_add_u32 s62, s37, 912384\n"
      "buffer_store_dword v163, v247, s[68:71], s62 offen sc1\n"
      "s_waitcnt vmcnt(12)\n"
      "s_barrier\n"
      "s_waitcnt lgkmcnt(5)\n"
      "v_mfma_f32_16x16x32_bf16 v[152:155], v[172:175], v[80:83], v[152:155]\n"
      "ds_read_b128 v[196:199], v241 offset:14336\n"
      "v_mfma_f32_16x16x32_bf16 v[156:159], v[172:175], v[84:87], v[156:159]\n"
      "s_waitcnt lgkmcnt(5)\n"
      "v_mfma_f32_16x16x32_bf16 v[152:155], v[176:179], v[88:91], v[152:155]\n"
      "ds_read_b128 v[168:171], v240 offset:16384\n"
      "v_mfma_f32_16x16x32_bf16 v[156:159], v[176:179], v[92:95], v[156:159]\n"
      "s_waitcnt lgkmcnt(5)\n"
      "v_mfma_f32_16x16x32_bf16 v[152:155], v[180:183], v[96:99], v[152:155]\n"
      "ds_read_b128 v[172:175], v241 offset:16384\n"
      "v_mfma_f32_16x16x32_bf16 v[156:159], v[180:183], v[100:103], v[156:159]\n"
      "s_waitcnt lgkmcnt(5)\n"
      "v_mfma_f32_16x16x32_bf16 v[152:155], v[184:187], v[104:107], v[152:155]\n"
      "ds_read_b128 v[176:179], v240 offset:36864\n"
      "v_mfma_f32_16x16x32_bf16 v[156:159], v[184:187], v[108:111], v[156:159]\n"
      "s_waitcnt lgkmcnt(5)\n"
      "v_mfma_f32_16x16x32_bf16 v[152:155], v[188:191], v[112:115], v[152:155]\n"
      "ds_read_b128 v[180:183], v241 offset:36864\n"
      "v_mfma_f32_16x16x32_bf16 v[156:159], v[188:191], v[116:119], v[156:159]\n"
      "s_waitcnt lgkmcnt(5)\n"
      "v_mfma_f32_16x16x32_bf16 v[152:155], v[192:195], v[120:123], v[152:155]\n"
      "ds_read_b128 v[184:187], v240 offset:38912\n"
      "v_mfma_f32_16x16x32_bf16 v[156:159], v[192:195], v[124:127], v[156:159]\n"
      "s_waitcnt lgkmcnt(5)\n"
      "v_mfma_f32_16x16x32_bf16 v[152:155], v[196:199], v[128:131], v[152:155]\n"
      "ds_read_b128 v[188:191], v241 offset:38912\n"
      "v_mfma_f32_16x16x32_bf16 v[156:159], v[196:199], v[132:135], v[156:159]\n"
      "s_waitcnt lgkmcnt(5)\n"
      "v_mfma_f32_16x16x32_bf16 v[152:155], v[168:171], v[136:139], v[152:155]\n"
      "ds_read_b128 v[192:195], v240 offset:40960\n"
      "v_mfma_f32_16x16x32_bf16 v[156:159], v[168:171], v[140:143], v[156:159]\n"
      "s_waitcnt lgkmcnt(5)\n"
      "v_mfma_f32_16x16x32_bf16 v[152:155], v[172:175], v[144:147], v[152:155]\n"
      "ds_read_b128 v[196:199], v241 offset:40960\n"
      "v_mfma_f32_16x16x32_bf16 v[156:159], v[172:175], v[148:151], v[156:159]\n"
      "s_waitcnt lgkmcnt(5)\n"
      "v_mfma_f32_16x16x32_bf16 v[160:163], v[176:179], v[8:11], 0\n"
      "ds_read_b128 v[168:171], v240 offset:43008\n"
      "v_mfma_f32_16x16x32_bf16 v[164:167], v[176:179], v[12:15], 0\n"
      "s_waitcnt lgkmcnt(5)\n"
      "v_mfma_f32_16x16x32_bf16 v[160:163], v[180:183], v[16:19], v[160:163]\n"
      "ds_read_b128 v[172:175], v241 offset:43008\n"
      "v_mfma_f32_16x16x32_bf16 v[164:167], v[180:183], v[20:23], v[164:167]\n"
      "s_waitcnt lgkmcnt(5)\n"
      "v_mfma_f32_16x16x32_bf16 v[160:163], v[184:187], v[24:27], v[160:163]\n"
      "ds_read_b128 v[176:179], v240 offset:45056\n"
      "v_mfma_f32_16x16x32_bf16 v[164:167], v[184:187], v[28:31], v[164:167]\n"
      "s_waitcnt lgkmcnt(5)\n"
      "v_mfma_f32_16x16x32_bf16 v[160:163], v[188:191], v[32:35], v[160:163]\n"
      "ds_read_b128 v[180:183], v241 offset:45056\n"
      "v_mfma_f32_16x16x32_bf16 v[164:167], v[188:191], v[36:39], v[164:167]\n"
      "s_waitcnt lgkmcnt(5)\n"
      "v_mfma_f32_16x16x32_bf16 v[160:163], v[192:195], v[40:43], v[160:163]\n"
      "ds_read_b128 v[184:187], v240 offset:47104\n"
      "v_mfma_f32_16x16x32_bf16 v[164:167], v[192:195], v[44:47], v[164:167]\n"
      "s_waitcnt lgkmcnt(5)\n"
      "v_mfma_f32_16x16x32_bf16 v[160:163], v[196:199], v[48:51], v[160:163]\n"
      "ds_read_b128 v[188:191], v241 offset:47104\n"
      "v_mfma_f32_16x16x32_bf16 v[164:167], v[196:199], v[52:55], v[164:167]\n"
      "s_waitcnt lgkmcnt(5)\n"
      "v_mfma_f32_16x16x32_bf16 v[160:163], v[168:171], v[56:59], v[160:163]\n"
      "ds_read_b128 v[192:195], v240 offset:49152\n"
      "v_mfma_f32_16x16x32_bf16 v[164:167], v[168:171], v[60:63], v[164:167]\n"
      "s_waitcnt lgkmcnt(5)\n"
      "v_mfma_f32_16x16x32_bf16 v[160:163], v[172:175], v[64:67], v[160:163]\n"
      "ds_read_b128 v[196:199], v241 offset:49152\n"
      "v_mfma_f32_16x16x32_bf16 v[164:167], v[172:175], v[68:71], v[164:167]\n"
      "s_waitcnt lgkmcnt(5)\n"
      "v_mfma_f32_16x16x32_bf16 v[160:163], v[176:179], v[72:75], v[160:163]\n"
      "ds_read_b128 v[168:171], v240 offset:51200\n"
      "v_mfma_f32_16x16x32_bf16 v[164:167], v[176:179], v[76:79], v[164:167]\n"
      "s_add_u32 m0, s43, 120832\n"
      "s_add_u32 s61, s36, 258688\n"
      "buffer_load_dwordx4 v243, s[16:19], s61 offen lds\n"
      "s_add_u32 m0, s43, 122880\n"
      "s_add_u32 s61, s36, 258816\n"
      "buffer_load_dwordx4 v243, s[16:19], s61 offen lds\n"
      "s_add_u32 m0, s43, 124928\n"
      "s_add_u32 s61, s36, 258944\n"
      "buffer_load_dwordx4 v243, s[16:19], s61 offen lds\n"
      "s_add_u32 m0, s43, 126976\n"
      "s_add_u32 s61, s36, 259072\n"
      "buffer_load_dwordx4 v243, s[16:19], s61 offen lds\n"
      "v_cvt_pk_bf16_f32 v152, v152, v156\n"
      "v_cvt_pk_bf16_f32 v153, v153, v157\n"
      "v_cvt_pk_bf16_f32 v154, v154, v158\n"
      "v_cvt_pk_bf16_f32 v155, v155, v159\n"
      "s_add_u32 s62, s37, 1179648\n"
      "buffer_store_dword v152, v247, s[68:71], s62 offen sc1\n"
      "s_add_u32 s62, s37, 1188864\n"
      "buffer_store_dword v153, v247, s[68:71], s62 offen sc1\n"
      "s_add_u32 s62, s37, 1198080\n"
      "buffer_store_dword v154, v247, s[68:71], s62 offen sc1\n"
      "s_add_u32 s62, s37, 1207296\n"
      "buffer_store_dword v155, v247, s[68:71], s62 offen sc1\n"
      "s_waitcnt vmcnt(12)\n"
      "s_barrier\n"
      "s_waitcnt lgkmcnt(5)\n"
      "v_mfma_f32_16x16x32_bf16 v[160:163], v[180:183], v[80:83], v[160:163]\n"
      "ds_read_b128 v[172:175], v241 offset:51200\n"
      "v_mfma_f32_16x16x32_bf16 v[164:167], v[180:183], v[84:87], v[164:167]\n"
      "s_waitcnt lgkmcnt(5)\n"
      "v_mfma_f32_16x16x32_bf16 v[160:163], v[184:187], v[88:91], v[160:163]\n"
      "ds_read_b128 v[176:179], v240 offset:53248\n"
      "v_mfma_f32_16x16x32_bf16 v[164:167], v[184:187], v[92:95], v[164:167]\n"
      "s_waitcnt lgkmcnt(5)\n"
      "v_mfma_f32_16x16x32_bf16 v[160:163], v[188:191], v[96:99], v[160:163]\n"
      "ds_read_b128 v[180:183], v241 offset:53248\n"
      "v_mfma_f32_16x16x32_bf16 v[164:167], v[188:191], v[100:103], v[164:167]\n"
      "s_waitcnt lgkmcnt(5)\n"
      "v_mfma_f32_16x16x32_bf16 v[160:163], v[192:195], v[104:107], v[160:163]\n"
      "ds_read_b128 v[184:187], v214 offset:8192\n"
      "v_mfma_f32_16x16x32_bf16 v[164:167], v[192:195], v[108:111], v[164:167]\n"
      "s_waitcnt lgkmcnt(5)\n"
      "v_mfma_f32_16x16x32_bf16 v[160:163], v[196:199], v[112:115], v[160:163]\n"
      "ds_read_b128 v[188:191], v215 offset:8192\n"
      "v_mfma_f32_16x16x32_bf16 v[164:167], v[196:199], v[116:119], v[164:167]\n"
      "s_waitcnt lgkmcnt(5)\n"
      "v_mfma_f32_16x16x32_bf16 v[160:163], v[168:171], v[120:123], v[160:163]\n"
      "ds_read_b128 v[192:195], v214 offset:10240\n"
      "v_mfma_f32_16x16x32_bf16 v[164:167], v[168:171], v[124:127], v[164:167]\n"
      "s_waitcnt lgkmcnt(5)\n"
      "v_mfma_f32_16x16x32_bf16 v[160:163], v[172:175], v[128:131], v[160:163]\n"
      "ds_read_b128 v[196:199], v215 offset:10240\n"
      "v_mfma_f32_16x16x32_bf16 v[164:167], v[172:175], v[132:135], v[164:167]\n"
      "s_waitcnt lgkmcnt(5)\n"
      "v_mfma_f32_16x16x32_bf16 v[160:163], v[176:179], v[136:139], v[160:163]\n"
      "ds_read_b128 v[168:171], v214 offset:12288\n"
      "v_mfma_f32_16x16x32_bf16 v[164:167], v[176:179], v[140:143], v[164:167]\n"
      "s_waitcnt lgkmcnt(5)\n"
      "v_mfma_f32_16x16x32_bf16 v[160:163], v[180:183], v[144:147], v[160:163]\n"
      "ds_read_b128 v[172:175], v215 offset:12288\n"
      "v_mfma_f32_16x16x32_bf16 v[164:167], v[180:183], v[148:151], v[164:167]\n"
      "s_waitcnt lgkmcnt(5)\n"
      "v_mfma_f32_16x16x32_bf16 v[152:155], v[184:187], v[8:11], 0\n"
      "ds_read_b128 v[176:179], v214 offset:14336\n"
      "v_mfma_f32_16x16x32_bf16 v[156:159], v[184:187], v[12:15], 0\n"
      "s_waitcnt lgkmcnt(5)\n"
      "v_mfma_f32_16x16x32_bf16 v[152:155], v[188:191], v[16:19], v[152:155]\n"
      "ds_read_b128 v[180:183], v215 offset:14336\n"
      "v_mfma_f32_16x16x32_bf16 v[156:159], v[188:191], v[20:23], v[156:159]\n"
      "s_waitcnt lgkmcnt(5)\n"
      "v_mfma_f32_16x16x32_bf16 v[152:155], v[192:195], v[24:27], v[152:155]\n"
      "ds_read_b128 v[184:187], v214 offset:16384\n"
      "v_mfma_f32_16x16x32_bf16 v[156:159], v[192:195], v[28:31], v[156:159]\n"
      "s_waitcnt lgkmcnt(5)\n"
      "v_mfma_f32_16x16x32_bf16 v[152:155], v[196:199], v[32:35], v[152:155]\n"
      "ds_read_b128 v[188:191], v215 offset:16384\n"
      "v_mfma_f32_16x16x32_bf16 v[156:159], v[196:199], v[36:39], v[156:159]\n"
      "s_waitcnt lgkmcnt(5)\n"
      "v_mfma_f32_16x16x32_bf16 v[152:155], v[168:171], v[40:43], v[152:155]\n"
      "ds_read_b128 v[192:195], v214 offset:18432\n"
      "v_mfma_f32_16x16x32_bf16 v[156:159], v[168:171], v[44:47], v[156:159]\n"
      "s_waitcnt lgkmcnt(5)\n"
      "v_mfma_f32_16x16x32_bf16 v[152:155], v[172:175], v[48:51], v[152:155]\n"
      "ds_read_b128 v[196:199], v215 offset:18432\n"
      "v_mfma_f32_16x16x32_bf16 v[156:159], v[172:175], v[52:55], v[156:159]\n"
      "s_waitcnt lgkmcnt(5)\n"
      "v_mfma_f32_16x16x32_bf16 v[152:155], v[176:179], v[56:59], v[152:155]\n"
      "ds_read_b128 v[168:171], v214 offset:20480\n"
      "v_mfma_f32_16x16x32_bf16 v[156:159], v[176:179], v[60:63], v[156:159]\n"
      "s_waitcnt lgkmcnt(5)\n"
      "v_mfma_f32_16x16x32_bf16 v[152:155], v[180:183], v[64:67], v[152:155]\n"
      "ds_read_b128 v[172:175], v215 offset:20480\n"
      "v_mfma_f32_16x16x32_bf16 v[156:159], v[180:183], v[68:71], v[156:159]\n"
      "s_waitcnt lgkmcnt(5)\n"
      "v_mfma_f32_16x16x32_bf16 v[152:155], v[184:187], v[72:75], v[152:155]\n"
      "ds_read_b128 v[176:179], v214 offset:22528\n"
      "v_mfma_f32_16x16x32_bf16 v[156:159], v[184:187], v[76:79], v[156:159]\n"
      "s_add_u32 m0, s43, 10240\n"
      "s_add_u32 s61, s36, 295552\n"
      "buffer_load_dwordx4 v243, s[16:19], s61 offen lds\n"
      "s_add_u32 m0, s43, 12288\n"
      "s_add_u32 s61, s36, 295680\n"
      "buffer_load_dwordx4 v243, s[16:19], s61 offen lds\n"
      "s_add_u32 m0, s43, 14336\n"
      "s_add_u32 s61, s36, 295808\n"
      "buffer_load_dwordx4 v243, s[16:19], s61 offen lds\n"
      "s_add_u32 m0, s43, 16384\n"
      "s_add_u32 s61, s36, 295936\n"
      "buffer_load_dwordx4 v243, s[16:19], s61 offen lds\n"
      "v_cvt_pk_bf16_f32 v160, v160, v164\n"
      "v_cvt_pk_bf16_f32 v161, v161, v165\n"
      "v_cvt_pk_bf16_f32 v162, v162, v166\n"
      "v_cvt_pk_bf16_f32 v163, v163, v167\n"
      "s_add_u32 s62, s37, 1474560\n"
      "buffer_store_dword v160, v247, s[68:71], s62 offen sc1\n"
      "s_add_u32 s62, s37, 1483776\n"
      "buffer_store_dword v161, v247, s[68:71], s62 offen sc1\n"
      "s_add_u32 s62, s37, 1492992\n"
      "buffer_store_dword v162, v247, s[68:71], s62 offen sc1\n"
      "s_add_u32 s62, s37, 1502208\n"
      "buffer_store_dword v163, v247, s[68:71], s62 offen sc1\n"
      "s_waitcnt vmcnt(12)\n"
      "s_barrier\n"
      "s_waitcnt lgkmcnt(5)\n"
      "v_mfma_f32_16x16x32_bf16 v[152:155], v[188:191], v[80:83], v[152:155]\n"
      "ds_read_b128 v[180:183], v215 offset:22528\n"
      "v_mfma_f32_16x16x32_bf16 v[156:159], v[188:191], v[84:87], v[156:159]\n"
      "s_waitcnt lgkmcnt(5)\n"
      "v_mfma_f32_16x16x32_bf16 v[152:155], v[192:195], v[88:91], v[152:155]\n"
      "ds_read_b128 v[184:187], v214 offset:24576\n"
      "v_mfma_f32_16x16x32_bf16 v[156:159], v[192:195], v[92:95], v[156:159]\n"
      "s_waitcnt lgkmcnt(5)\n"
      "v_mfma_f32_16x16x32_bf16 v[152:155], v[196:199], v[96:99], v[152:155]\n"
      "ds_read_b128 v[188:191], v215 offset:24576\n"
      "v_mfma_f32_16x16x32_bf16 v[156:159], v[196:199], v[100:103], v[156:159]\n"
      "s_waitcnt lgkmcnt(5)\n"
      "v_mfma_f32_16x16x32_bf16 v[152:155], v[168:171], v[104:107], v[152:155]\n"
      "ds_read_b128 v[192:195], v214 offset:45056\n"
      "v_mfma_f32_16x16x32_bf16 v[156:159], v[168:171], v[108:111], v[156:159]\n"
      "s_waitcnt lgkmcnt(5)\n"
      "v_mfma_f32_16x16x32_bf16 v[152:155], v[172:175], v[112:115], v[152:155]\n"
      "ds_read_b128 v[196:199], v215 offset:45056\n"
      "v_mfma_f32_16x16x32_bf16 v[156:159], v[172:175], v[116:119], v[156:159]\n"
      "s_waitcnt lgkmcnt(5)\n"
      "v_mfma_f32_16x16x32_bf16 v[152:155], v[176:179], v[120:123], v[152:155]\n"
      "ds_read_b128 v[168:171], v214 offset:47104\n"
      "v_mfma_f32_16x16x32_bf16 v[156:159], v[176:179], v[124:127], v[156:159]\n"
      "s_waitcnt lgkmcnt(5)\n"
      "v_mfma_f32_16x16x32_bf16 v[152:155], v[180:183], v[128:131], v[152:155]\n"
      "ds_read_b128 v[172:175], v215 offset:47104\n"
      "v_mfma_f32_16x16x32_bf16 v[156:159], v[180:183], v[132:135], v[156:159]\n"
      "s_waitcnt lgkmcnt(5)\n"
      "v_mfma_f32_16x16x32_bf16 v[152:155], v[184:187], v[136:139], v[152:155]\n"
      "ds_read_b128 v[176:179], v214 offset:49152\n"
      "v_mfma_f32_16x16x32_bf16 v[156:159], v[184:187], v[140:143], v[156:159]\n"
      "s_waitcnt lgkmcnt(5)\n"
      "v_mfma_f32_16x16x32_bf16 v[152:155], v[188:191], v[144:147], v[152:155]\n"
      "ds_read_b128 v[180:183], v215 offset:49152\n"
      "v_mfma_f32_16x16x32_bf16 v[156:159], v[188:191], v[148:151], v[156:159]\n"
      "s_waitcnt lgkmcnt(5)\n"
      "v_mfma_f32_16x16x32_bf16 v[160:163], v[192:195], v[8:11], 0\n"
      "ds_read_b128 v[184:187], v214 offset:51200\n"
      "v_mfma_f32_16x16x32_bf16 v[164:167], v[192:195], v[12:15], 0\n"
      "s_waitcnt lgkmcnt(5)\n"
      "v_mfma_f32_16x16x32_bf16 v[160:163], v[196:199], v[16:19], v[160:163]\n"
      "ds_read_b128 v[188:191], v215 offset:51200\n"
      "v_mfma_f32_16x16x32_bf16 v[164:167], v[196:199], v[20:23], v[164:167]\n"
      "s_waitcnt lgkmcnt(5)\n"
      "v_mfma_f32_16x16x32_bf16 v[160:163], v[168:171], v[24:27], v[160:163]\n"
      "ds_read_b128 v[192:195], v214 offset:53248\n"
      "v_mfma_f32_16x16x32_bf16 v[164:167], v[168:171], v[28:31], v[164:167]\n"
      "s_waitcnt lgkmcnt(5)\n"
      "v_mfma_f32_16x16x32_bf16 v[160:163], v[172:175], v[32:35], v[160:163]\n"
      "ds_read_b128 v[196:199], v215 offset:53248\n"
      "v_mfma_f32_16x16x32_bf16 v[164:167], v[172:175], v[36:39], v[164:167]\n"
      "s_waitcnt lgkmcnt(5)\n"
      "v_mfma_f32_16x16x32_bf16 v[160:163], v[176:179], v[40:43], v[160:163]\n"
      "ds_read_b128 v[168:171], v214 offset:55296\n"
      "v_mfma_f32_16x16x32_bf16 v[164:167], v[176:179], v[44:47], v[164:167]\n"
      "s_waitcnt lgkmcnt(5)\n"
      "v_mfma_f32_16x16x32_bf16 v[160:163], v[180:183], v[48:51], v[160:163]\n"
      "ds_read_b128 v[172:175], v215 offset:55296\n"
      "v_mfma_f32_16x16x32_bf16 v[164:167], v[180:183], v[52:55], v[164:167]\n"
      "s_waitcnt lgkmcnt(5)\n"
      "v_mfma_f32_16x16x32_bf16 v[160:163], v[184:187], v[56:59], v[160:163]\n"
      "ds_read_b128 v[176:179], v214 offset:57344\n"
      "v_mfma_f32_16x16x32_bf16 v[164:167], v[184:187], v[60:63], v[164:167]\n"
      "s_waitcnt lgkmcnt(5)\n"
      "v_mfma_f32_16x16x32_bf16 v[160:163], v[188:191], v[64:67], v[160:163]\n"
      "ds_read_b128 v[180:183], v215 offset:57344\n"
      "v_mfma_f32_16x16x32_bf16 v[164:167], v[188:191], v[68:71], v[164:167]\n"
      "s_waitcnt lgkmcnt(5)\n"
      "v_mfma_f32_16x16x32_bf16 v[160:163], v[192:195], v[72:75], v[160:163]\n"
      "ds_read_b128 v[184:187], v214 offset:59392\n"
      "v_mfma_f32_16x16x32_bf16 v[164:167], v[192:195], v[76:79], v[164:167]\n"
      "s_cmp_lt_u32 s38, 19\n"
      "s_cbranch_scc1 Lskipd01_%=\n"
      "s_add_u32 m0, s43, 47104\n"
      "s_add_u32 s61, s36, 332416\n"
      "buffer_load_dwordx4 v243, s[16:19], s61 offen lds\n"
      "s_add_u32 m0, s43, 49152\n"
      "s_add_u32 s61, s36, 332544\n"
      "buffer_load_dwordx4 v243, s[16:19], s61 offen lds\n"
      "s_add_u32 m0, s43, 51200\n"
      "s_add_u32 s61, s36, 332672\n"
      "buffer_load_dwordx4 v243, s[16:19], s61 offen lds\n"
      "s_add_u32 m0, s43, 53248\n"
      "s_add_u32 s61, s36, 332800\n"
      "buffer_load_dwordx4 v243, s[16:19], s61 offen lds\n"
      "Lskipd01_%=:\n"
      "v_cvt_pk_bf16_f32 v152, v152, v156\n"
      "v_cvt_pk_bf16_f32 v153, v153, v157\n"
      "v_cvt_pk_bf16_f32 v154, v154, v158\n"
      "v_cvt_pk_bf16_f32 v155, v155, v159\n"
      "s_add_u32 s62, s37, 1769472\n"
      "buffer_store_dword v152, v247, s[68:71], s62 offen sc1\n"
      "s_add_u32 s62, s37, 1778688\n"
      "buffer_store_dword v153, v247, s[68:71], s62 offen sc1\n"
      "s_add_u32 s62, s37, 1787904\n"
      "buffer_store_dword v154, v247, s[68:71], s62 offen sc1\n"
      "s_add_u32 s62, s37, 1797120\n"
      "buffer_store_dword v155, v247, s[68:71], s62 offen sc1\n"
      "s_waitcnt vmcnt(8)\n"
      "s_barrier\n"
      "s_waitcnt lgkmcnt(5)\n"
      "v_mfma_f32_16x16x32_bf16 v[160:163], v[196:199], v[80:83], v[160:163]\n"
      "ds_read_b128 v[188:191], v215 offset:59392\n"
      "v_mfma_f32_16x16x32_bf16 v[164:167], v[196:199], v[84:87], v[164:167]\n"
      "s_waitcnt lgkmcnt(5)\n"
      "v_mfma_f32_16x16x32_bf16 v[160:163], v[168:171], v[88:91], v[160:163]\n"
      "ds_read_b128 v[192:195], v214 offset:61440\n"
      "v_mfma_f32_16x16x32_bf16 v[164:167], v[168:171], v[92:95], v[164:167]\n"
      "s_waitcnt lgkmcnt(5)\n"
      "v_mfma_f32_16x16x32_bf16 v[160:163], v[172:175], v[96:99], v[160:163]\n"
      "ds_read_b128 v[196:199], v215 offset:61440\n"
      "v_mfma_f32_16x16x32_bf16 v[164:167], v[172:175], v[100:103], v[164:167]\n"
      "s_waitcnt lgkmcnt(5)\n"
      "v_mfma_f32_16x16x32_bf16 v[160:163], v[176:179], v[104:107], v[160:163]\n"
      "ds_read_b128 v[168:171], v240 offset:0\n"
      "v_mfma_f32_16x16x32_bf16 v[164:167], v[176:179], v[108:111], v[164:167]\n"
      "s_waitcnt lgkmcnt(5)\n"
      "v_mfma_f32_16x16x32_bf16 v[160:163], v[180:183], v[112:115], v[160:163]\n"
      "ds_read_b128 v[172:175], v241 offset:0\n"
      "v_mfma_f32_16x16x32_bf16 v[164:167], v[180:183], v[116:119], v[164:167]\n"
      "s_waitcnt lgkmcnt(5)\n"
      "v_mfma_f32_16x16x32_bf16 v[160:163], v[184:187], v[120:123], v[160:163]\n"
      "ds_read_b128 v[176:179], v240 offset:2048\n"
      "v_mfma_f32_16x16x32_bf16 v[164:167], v[184:187], v[124:127], v[164:167]\n"
      "s_waitcnt lgkmcnt(5)\n"
      "v_mfma_f32_16x16x32_bf16 v[160:163], v[188:191], v[128:131], v[160:163]\n"
      "ds_read_b128 v[180:183], v241 offset:2048\n"
      "v_mfma_f32_16x16x32_bf16 v[164:167], v[188:191], v[132:135], v[164:167]\n"
      "s_waitcnt lgkmcnt(5)\n"
      "v_mfma_f32_16x16x32_bf16 v[160:163], v[192:195], v[136:139], v[160:163]\n"
      "ds_read_b128 v[184:187], v240 offset:4096\n"
      "v_mfma_f32_16x16x32_bf16 v[164:167], v[192:195], v[140:143], v[164:167]\n"
      "s_waitcnt lgkmcnt(5)\n"
      "v_mfma_f32_16x16x32_bf16 v[160:163], v[196:199], v[144:147], v[160:163]\n"
      "ds_read_b128 v[188:191], v241 offset:4096\n"
      "v_mfma_f32_16x16x32_bf16 v[164:167], v[196:199], v[148:151], v[164:167]\n"
      "s_waitcnt lgkmcnt(5)\n"
      "v_mfma_f32_16x16x32_bf16 v[152:155], v[168:171], v[8:11], 0\n"
      "ds_read_b128 v[192:195], v240 offset:6144\n"
      "v_mfma_f32_16x16x32_bf16 v[156:159], v[168:171], v[12:15], 0\n"
      "s_waitcnt lgkmcnt(5)\n"
      "v_mfma_f32_16x16x32_bf16 v[152:155], v[172:175], v[16:19], v[152:155]\n"
      "ds_read_b128 v[196:199], v241 offset:6144\n"
      "v_mfma_f32_16x16x32_bf16 v[156:159], v[172:175], v[20:23], v[156:159]\n"
      "s_waitcnt lgkmcnt(5)\n"
      "v_mfma_f32_16x16x32_bf16 v[152:155], v[176:179], v[24:27], v[152:155]\n"
      "ds_read_b128 v[168:171], v240 offset:8192\n"
      "v_mfma_f32_16x16x32_bf16 v[156:159], v[176:179], v[28:31], v[156:159]\n"
      "s_waitcnt lgkmcnt(5)\n"
      "v_mfma_f32_16x16x32_bf16 v[152:155], v[180:183], v[32:35], v[152:155]\n"
      "ds_read_b128 v[172:175], v241 offset:8192\n"
      "v_mfma_f32_16x16x32_bf16 v[156:159], v[180:183], v[36:39], v[156:159]\n"
      "s_waitcnt lgkmcnt(5)\n"
      "v_mfma_f32_16x16x32_bf16 v[152:155], v[184:187], v[40:43], v[152:155]\n"
      "ds_read_b128 v[176:179], v240 offset:10240\n"
      "v_mfma_f32_16x16x32_bf16 v[156:159], v[184:187], v[44:47], v[156:159]\n"
      "s_waitcnt lgkmcnt(5)\n"
      "v_mfma_f32_16x16x32_bf16 v[152:155], v[188:191], v[48:51], v[152:155]\n"
      "ds_read_b128 v[180:183], v241 offset:10240\n"
      "v_mfma_f32_16x16x32_bf16 v[156:159], v[188:191], v[52:55], v[156:159]\n"
      "s_waitcnt lgkmcnt(5)\n"
      "v_mfma_f32_16x16x32_bf16 v[152:155], v[192:195], v[56:59], v[152:155]\n"
      "ds_read_b128 v[184:187], v240 offset:12288\n"
      "v_mfma_f32_16x16x32_bf16 v[156:159], v[192:195], v[60:63], v[156:159]\n"
      "s_waitcnt lgkmcnt(5)\n"
      "v_mfma_f32_16x16x32_bf16 v[152:155], v[196:199], v[64:67], v[152:155]\n"
      "ds_read_b128 v[188:191], v241 offset:12288\n"
      "v_mfma_f32_16x16x32_bf16 v[156:159], v[196:199], v[68:71], v[156:159]\n"
      "s_waitcnt lgkmcnt(5)\n"
      "v_mfma_f32_16x16x32_bf16 v[152:155], v[168:171], v[72:75], v[152:155]\n"
      "ds_read_b128 v[192:195], v240 offset:14336\n"
      "v_mfma_f32_16x16x32_bf16 v[156:159], v[168:171], v[76:79], v[156:159]\n"
      "v_cvt_pk_bf16_f32 v160, v160, v164\n"
      "v_cvt_pk_bf16_f32 v161, v161, v165\n"
      "v_cvt_pk_bf16_f32 v162, v162, v166\n"
      "v_cvt_pk_bf16_f32 v163, v163, v167\n"
      "s_add_u32 s62, s37, 2064384\n"
      "buffer_store_dword v160, v247, s[68:71], s62 offen sc1\n"
      "s_add_u32 s62, s37, 2073600\n"
      "buffer_store_dword v161, v247, s[68:71], s62 offen sc1\n"
      "s_add_u32 s62, s37, 2082816\n"
      "buffer_store_dword v162, v247, s[68:71], s62 offen sc1\n"
      "s_add_u32 s62, s37, 2092032\n"
      "buffer_store_dword v163, v247, s[68:71], s62 offen sc1\n"
      "s_waitcnt vmcnt(8)\n"
      "s_barrier\n"
      "s_waitcnt lgkmcnt(5)\n"
      "v_mfma_f32_16x16x32_bf16 v[152:155], v[172:175], v[80:83], v[152:155]\n"
      "ds_read_b128 v[196:199], v241 offset:14336\n"
      "v_mfma_f32_16x16x32_bf16 v[156:159], v[172:175], v[84:87], v[156:159]\n"
      "s_waitcnt lgkmcnt(5)\n"
      "v_mfma_f32_16x16x32_bf16 v[152:155], v[176:179], v[88:91], v[152:155]\n"
      "ds_read_b128 v[168:171], v240 offset:16384\n"
      "v_mfma_f32_16x16x32_bf16 v[156:159], v[176:179], v[92:95], v[156:159]\n"
      "s_waitcnt lgkmcnt(5)\n"
      "v_mfma_f32_16x16x32_bf16 v[152:155], v[180:183], v[96:99], v[152:155]\n"
      "ds_read_b128 v[172:175], v241 offset:16384\n"
      "v_mfma_f32_16x16x32_bf16 v[156:159], v[180:183], v[100:103], v[156:159]\n"
      "s_waitcnt lgkmcnt(5)\n"
      "v_mfma_f32_16x16x32_bf16 v[152:155], v[184:187], v[104:107], v[152:155]\n"
      "ds_read_b128 v[176:179], v240 offset:36864\n"
      "v_mfma_f32_16x16x32_bf16 v[156:159], v[184:187], v[108:111], v[156:159]\n"
      "s_waitcnt lgkmcnt(5)\n"
      "v_mfma_f32_16x16x32_bf16 v[152:155], v[188:191], v[112:115], v[152:155]\n"
      "ds_read_b128 v[180:183], v241 offset:36864\n"
      "v_mfma_f32_16x16x32_bf16 v[156:159], v[188:191], v[116:119], v[156:159]\n"
      "s_waitcnt lgkmcnt(5)\n"
      "v_mfma_f32_16x16x32_bf16 v[152:155], v[192:195], v[120:123], v[152:155]\n"
      "ds_read_b128 v[184:187], v240 offset:38912\n"
      "v_mfma_f32_16x16x32_bf16 v[156:159], v[192:195], v[124:127], v[156:159]\n"
      "s_waitcnt lgkmcnt(5)\n"
      "v_mfma_f32_16x16x32_bf16 v[152:155], v[196:199], v[128:131], v[152:155]\n"
      "ds_read_b128 v[188:191], v241 offset:38912\n"
      "v_mfma_f32_16x16x32_bf16 v[156:159], v[196:199], v[132:135], v[156:159]\n"
      "s_waitcnt lgkmcnt(5)\n"
      "v_mfma_f32_16x16x32_bf16 v[152:155], v[168:171], v[136:139], v[152:155]\n"
      "ds_read_b128 v[192:195], v240 offset:40960\n"
      "v_mfma_f32_16x16x32_bf16 v[156:159], v[168:171], v[140:143], v[156:159]\n"
      "s_waitcnt lgkmcnt(5)\n"
      "v_mfma_f32_16x16x32_bf16 v[152:155], v[172:175], v[144:147], v[152:155]\n"
      "ds_read_b128 v[196:199], v241 offset:40960\n"
      "v_mfma_f32_16x16x32_bf16 v[156:159], v[172:175], v[148:151], v[156:159]\n"
      "s_nop 7\n"
      "v_cvt_pk_bf16_f32 v152, v152, v156\n"
      "v_cvt_pk_bf16_f32 v153, v153, v157\n"
      "v_cvt_pk_bf16_f32 v154, v154, v158\n"
      "v_cvt_pk_bf16_f32 v155, v155, v159\n"
      "s_add_u32 s62, s37, 2359296\n"
      "buffer_store_dword v152, v247, s[68:71], s62 offen sc1\n"
      "s_add_u32 s62, s37, 2368512\n"
      "buffer_store_dword v153, v247, s[68:71], s62 offen sc1\n"
      "s_add_u32 s62, s37, 2377728\n"
      "buffer_store_dword v154, v247, s[68:71], s62 offen sc1\n"
      "s_add_u32 s62, s37, 2386944\n"
      "buffer_store_dword v155, v247, s[68:71], s62 offen sc1\n"
      "s_cmp_lt_u32 s38, 19\n"
      "s_cbranch_scc1 Lnox01_%=\n"
      "s_waitcnt lgkmcnt(5)\n"
      "v_mfma_f32_16x16x32_bf16 v[160:163], v[176:179], v[8:11], 0\n"
      "ds_read_b128 v[168:171], v240 offset:43008\n"
      "v_mfma_f32_16x16x32_bf16 v[164:167], v[176:179], v[12:15], 0\n"
      "s_waitcnt lgkmcnt(5)\n"
      "v_mfma_f32_16x16x32_bf16 v[160:163], v[180:183], v[16:19], v[160:163]\n"
      "ds_read_b128 v[172:175], v241 offset:43008\n"
      "v_mfma_f32_16x16x32_bf16 v[164:167], v[180:183], v[20:23], v[164:167]\n"
      "s_waitcnt lgkmcnt(5)\n"
      "v_mfma_f32_16x16x32_bf16 v[160:163], v[184:187], v[24:27], v[160:163]\n"
      "ds_read_b128 v[176:179], v240 offset:45056\n"
      "v_mfma_f32_16x16x32_bf16 v[164:167], v[184:187], v[28:31], v[164:167]\n"
      "s_waitcnt lgkmcnt(5)\n"
      "v_mfma_f32_16x16x32_bf16 v[160:163], v[188:191], v[32:35], v[160:163]\n"
      "ds_read_b128 v[180:183], v241 offset:45056\n"
      "v_mfma_f32_16x16x32_bf16 v[164:167], v[188:191], v[36:39], v[164:167]\n"
      "s_waitcnt lgkmcnt(5)\n"
      "v_mfma_f32_16x16x32_bf16 v[160:163], v[192:195], v[40:43], v[160:163]\n"
      "ds_read_b128 v[184:187], v240 offset:47104\n"
      "v_mfma_f32_16x16x32_bf16 v[164:167], v[192:195], v[44:47], v[164:167]\n"
      "s_waitcnt lgkmcnt(5)\n"
      "v_mfma_f32_16x16x32_bf16 v[160:163], v[196:199], v[48:51], v[160:163]\n"
      "ds_read_b128 v[188:191], v241 offset:47104\n"
      "v_mfma_f32_16x16x32_bf16 v[164:167], v[196:199], v[52:55], v[164:167]\n"
      "s_waitcnt lgkmcnt(5)\n"
      "v_mfma_f32_16x16x32_bf16 v[160:163], v[168:171], v[56:59], v[160:163]\n"
      "ds_read_b128 v[192:195], v240 offset:49152\n"
      "v_mfma_f32_16x16x32_bf16 v[164:167], v[168:171], v[60:63], v[164:167]\n"
      "s_waitcnt lgkmcnt(5)\n"
      "v_mfma_f32_16x16x32_bf16 v[160:163], v[172:175], v[64:67], v[160:163]\n"
      "ds_read_b128 v[196:199], v241 offset:49152\n"
      "v_mfma_f32_16x16x32_bf16 v[164:167], v[172:175], v[68:71], v[164:167]\n"
      "s_waitcnt lgkmcnt(5)\n"
      "v_mfma_f32_16x16x32_bf16 v[160:163], v[176:179], v[72:75], v[160:163]\n"
      "ds_read_b128 v[168:171], v240 offset:51200\n"
      "v_mfma_f32_16x16x32_bf16 v[164:167], v[176:179], v[76:79], v[164:167]\n"
      "s_waitcnt lgkmcnt(5)\n"
      "v_mfma_f32_16x16x32_bf16 v[160:163], v[180:183], v[80:83], v[160:163]\n"
      "ds_read_b128 v[172:175], v241 offset:51200\n"
      "v_mfma_f32_16x16x32_bf16 v[164:167], v[180:183], v[84:87], v[164:167]\n"
      "s_waitcnt lgkmcnt(5)\n"
      "v_mfma_f32_16x16x32_bf16 v[160:163], v[184:187], v[88:91], v[160:163]\n"
      "ds_read_b128 v[176:179], v240 offset:53248\n"
      "v_mfma_f32_16x16x32_bf16 v[164:167], v[184:187], v[92:95], v[164:167]\n"
      "s_waitcnt lgkmcnt(5)\n"
      "v_mfma_f32_16x16x32_bf16 v[160:163], v[188:191], v[96:99], v[160:163]\n"
      "ds_read_b128 v[180:183], v241 offset:53248\n"
      "v_mfma_f32_16x16x32_bf16 v[164:167], v[188:191], v[100:103], v[164:167]\n"
      "s_waitcnt lgkmcnt(5)\n"
      "v_mfma_f32_16x16x32_bf16 v[160:163], v[192:195], v[104:107], v[160:163]\n"
      "v_mfma_f32_16x16x32_bf16 v[164:167], v[192:195], v[108:111], v[164:167]\n"
      "s_waitcnt lgkmcnt(4)\n"
      "v_mfma_f32_16x16x32_bf16 v[160:163], v[196:199], v[112:115], v[160:163]\n"
      "v_mfma_f32_16x16x32_bf16 v[164:167], v[196:199], v[116:119], v[164:167]\n"
      "s_waitcnt lgkmcnt(3)\n"
      "v_mfma_f32_16x16x32_bf16 v[160:163], v[168:171], v[120:123], v[160:163]\n"
      "v_mfma_f32_16x16x32_bf16 v[164:167], v[168:171], v[124:127], v[164:167]\n"
      "s_waitcnt lgkmcnt(2)\n"
      "v_mfma_f32_16x16x32_bf16 v[160:163], v[172:175], v[128:131], v[160:163]\n"
      "v_mfma_f32_16x16x32_bf16 v[164:167], v[172:175], v[132:135], v[164:167]\n"
      "s_waitcnt lgkmcnt(1)\n"
      "v_mfma_f32_16x16x32_bf16 v[160:163], v[176:179], v[136:139], v[160:163]\n"
      "v_mfma_f32_16x16x32_bf16 v[164:167], v[176:179], v[140:143], v[164:167]\n"
      "s_waitcnt lgkmcnt(0)\n"
      "v_mfma_f32_16x16x32_bf16 v[160:163], v[180:183], v[144:147], v[160:163]\n"
      "v_mfma_f32_16x16x32_bf16 v[164:167], v[180:183], v[148:151], v[164:167]\n"
      "s_nop 7\n"
      "v_cvt_pk_bf16_f32 v160, v160, v164\n"
      "v_cvt_pk_bf16_f32 v161, v161, v165\n"
      "v_cvt_pk_bf16_f32 v162, v162, v166\n"
      "v_cvt_pk_bf16_f32 v163, v163, v167\n"
      "s_add_u32 s62, s37, 2654208\n"
      "buffer_store_dword v160, v247, s[68:71], s62 offen sc1\n"
      "s_add_u32 s62, s37, 2663424\n"
      "buffer_store_dword v161, v247, s[68:71], s62 offen sc1\n"
      "s_add_u32 s62, s37, 2672640\n"
      "buffer_store_dword v162, v247, s[68:71], s62 offen sc1\n"
      "s_add_u32 s62, s37, 2681856\n"
      "buffer_store_dword v163, v247, s[68:71], s62 offen sc1\n"
      "Lnox01_%=:\n"
      "s_waitcnt lgkmcnt(0)\n"
      "v_mov_b32 v8, 153600\n"
      "ds_read_b128 v[12:15], v8\n"
      "ds_read_b128 v[16:19], v8 offset:16\n"
      "s_waitcnt lgkmcnt(0)\n"
      "v_or3_b32 v12, v12, v13, v14\n"
      "v_or3_b32 v12, v12, v15, v16\n"
      "v_or3_b32 v12, v12, v17, v18\n"
      "v_or_b32 %[bad], v12, v19\n"
      "s_branch Lend_%=\n"
      "Lp2_%=:\n"
      "s_lshl_b32 s60, s39, 6\n"
      "v_add_u32 v4, s60, v1\n"
      "v_mul_u32_u24 v4, 7, v4\n"
      "v_add_u32 v4, s41, v4\n"
      "v_min_u32 v4, 2303, v4\n"
      "v_lshrrev_b32 v5, 2, v4\n"
      "v_and_b32 v4, 3, v4\n"
      "v_mul_u32_u24 v5, 18432, v5\n"
      "v_lshl_add_u32 v4, v4, 7, v5\n"
      "s_lshl_b32 s60, s40, 2\n"
      "s_mov_b32 m0, 153632\n"
      "s_nop 0\n"
      "buffer_load_dword v4, s[20:23], s60 offen lds\n"
      "v_lshrrev_b32 v4, 1, v2\n"
      "v_xor_b32 v5, v3, v4\n"
      "v_lshlrev_b32 v5, 4, v5\n"
      "v_lshl_add_u32 v240, v2, 7, v5\n"
      "v_xor_b32 v241, 64, v240\n"
      "v_add_u32 v214, 0x10000, v240\n"
      "v_add_u32 v216, 0x20000, v240\n"
      "v_add_u32 v215, 0x10000, v241\n"
      "v_add_u32 v217, 0x20000, v241\n"
      "v_lshrrev_b32 v4, 3, v1\n"
      "v_and_b32 v5, 1, v4\n"
      "v_xor_b32 v5, v1, v5\n"
      "s_lshl_b32 s60, s49, 12\n"
      "s_add_u32 s60, s60, 110592\n"
      "v_lshlrev_b32 v5, 4, v5\n"
      "v_add_u32 v242, s60, v5\n"
      "s_lshl_b32 s60, s48, 3\n"
      "v_add_u32 v4, s60, v4\n"
      "v_lshrrev_b32 v5, 1, v4\n"
      "v_and_b32 v6, 7, v1\n"
      "v_xor_b32 v5, v5, v6\n"
      "v_and_b32 v5, 7, v5\n"
      "v_mul_u32_u24 v4, 1152, v4\n"
      "v_lshl_add_u32 v243, v5, 4, v4\n"
      "s_lshl_b32 s43, s48, 10\n"
      "v_lshrrev_b32 v4, 3, v2\n"
      "v_lshlrev_b32 v4, 12, v4\n"
      "v_lshl_add_u32 v4, v3, 8, v4\n"
      "v_and_b32 v5, 7, v2\n"
      "v_lshl_add_u32 v4, v5, 5, v4\n"
      "s_lshl_b32 s60, s48, 13\n"
      "s_add_u32 s60, s60, 110592\n"
      "v_add_u32 v4, s60, v4\n"
      "v_lshrrev_b32 v5, 2, v2\n"
      "v_and_b32 v5, 1, v5\n"
      "v_lshl_add_u32 v244, v5, 4, v4\n"
      "v_xor_b32 v245, 16, v244\n"
      "v_lshlrev_b32 v4, 3, v2\n"
      "v_add_u32 v246, s44, v4\n"
      "s_lshl_b32 s60, s49, 5\n"
      "s_add_u32 s60, s60, s40\n"
      "s_lshl_b32 s60, s60, 1\n"
      "v_mul_u32_u24 v4, 36864, v3\n"
      "v_lshl_add_u32 v4, v2, 2, v4\n"
      "v_add_u32 v247, s60, v4\n"
      "s_lshl_b32 s60, s39, 2\n"
      "s_add_u32 s45, s60, 153600\n"
      "v_min_u32 v4, 7, v1\n"
      "v_mul_u32_u24 v4, 368640, v4\n"
      "s_mov_b32 m0, 153632\n"
      "s_mov_b32 exec_lo, 0xff\n"
      "s_mov_b32 exec_hi, 0x0\n"
      "buffer_load_dword v4, s[68:71], s37 offen lds\n"
      "s_mov_b64 exec, -1\n"
      "v_mov_b32 v208, 0\n"
      "v_mov_b32 v209, 0\n"
      "s_mov_b32 s63, 3538944\n"
      "buffer_load_dwordx4 v[56:59], v210, s[20:23], s63 offen\n"
      "buffer_load_dwordx4 v[60:63], v211, s[20:23], s63 offen\n"
      "buffer_load_dwordx4 v[64:67], v212, s[20:23], s63 offen\n"
      "buffer_load_dwordx4 v[68:71], v213, s[20:23], s63 offen\n"
      "s_waitcnt vmcnt(14)\n"
      "v_lshlrev_b32 v168, 7, v3\n"
      "v_add_u32 v168, v246, v168\n"
      "ds_read_b64 v[172:173], v168\n"
      "ds_read_b64 v[174:175], v168 offset:640\n"
      "ds_read_b64 v[176:177], v246 offset:384\n"
      "ds_read_b64 v[178:179], v246 offset:1024\n"
      "v_mov_b32 v180, 0\n"
      "v_mov_b32 v181, 0\n"
      "v_mov_b32 v182, 0\n"
      "s_waitcnt lgkmcnt(0)\n"
      "v_lshlrev_b32 v168, 16, v174\n"
      "v_and_b32 v169, 0xffff0000, v174\n"
      "v_max3_f32 v180, |v168|, |v169|, v180\n"
      "v_rndne_f32 v170, v168\n"
      "v_rndne_f32 v171, v169\n"
      "v_sub_f32 v168, v168, v170\n"
      "v_sub_f32 v169, v169, v171\n"
      "v_max3_f32 v181, |v168|, |v169|, v181\n"
      "v_lshlrev_b32 v168, 16, v172\n"
      "v_and_b32 v169, 0xffff0000, v172\n"
      "v_max3_f32 v182, |v168|, |v169|, v182\n"
      "v_lshlrev_b32 v168, 16, v175\n"
      "v_and_b32 v169, 0xffff0000, v175\n"
      "v_max3_f32 v180, |v168|, |v169|, v180\n"
      "v_rndne_f32 v170, v168\n"
      "v_rndne_f32 v171, v169\n"
      "v_sub_f32 v168, v168, v170\n"
      "v_sub_f32 v169, v169, v171\n"
      "v_max3_f32 v181, |v168|, |v169|, v181\n"
      "v_lshlrev_b32 v168, 16, v173\n"
      "v_and_b32 v169, 0xffff0000, v173\n"
      "v_max3_f32 v182, |v168|, |v169|, v182\n"
      "v_lshlrev_b32 v168, 16, v178\n"
      "v_and_b32 v169, 0xffff0000, v178\n"
      "v_max3_f32 v180, |v168|, |v169|, v180\n"
      "v_rndne_f32 v170, v168\n"
      "v_rndne_f32 v171, v169\n"
      "v_sub_f32 v168, v168, v170\n"
      "v_sub_f32 v169, v169, v171\n"
      "v_max3_f32 v181, |v168|, |v169|, v181\n"
      "v_lshlrev_b32 v168, 16, v176\n"
      "v_and_b32 v169, 0xffff0000, v176\n"
      "v_max3_f32 v182, |v168|, |v169|, v182\n"
      "v_lshlrev_b32 v168, 16, v179\n"
      "v_and_b32 v169, 0xffff0000, v179\n"
      "v_max3_f32 v180, |v168|, |v169|, v180\n"
      "v_rndne_f32 v170, v168\n"
      "v_rndne_f32 v171, v169\n"
      "v_sub_f32 v168, v168, v170\n"
      "v_sub_f32 v169, v169, v171\n"
      "v_max3_f32 v181, |v168|, |v169|, v181\n"
      "v_lshlrev_b32 v168, 16, v177\n"
      "v_and_b32 v169, 0xffff0000, v177\n"
      "v_max3_f32 v182, |v168|, |v169|, v182\n"
      "v_cmp_lt_f32 vcc, 0, v181\n"
      "s_mov_b64 s[66:67], vcc\n"
      "v_cmp_lt_f32 vcc, 0x43000000, v180\n"
      "s_or_b64 s[66:67], s[66:67], vcc\n"
      "v_cmp_le_f32 vcc, 0x71800000, v182\n"
      "s_or_b64 s[66:67], s[66:67], vcc\n"
      "ds_read_b64 v[184:185], v246 offset:0\n"
      "ds_read_b64 v[186:187], v246 offset:640\n"
      "s_waitcnt lgkmcnt(0)\n"
      "v_lshlrev_b32 v200, 16, v184\n"
      "v_and_b32 v201, 0xffff0000, v184\n"
      "v_lshlrev_b32 v204, 16, v186\n"
      "v_and_b32 v205, 0xffff0000, v186\n"
      "v_lshlrev_b32 v202, 16, v185\n"
      "v_and_b32 v203, 0xffff0000, v185\n"
      "v_lshlrev_b32 v206, 16, v187\n"
      "v_and_b32 v207, 0xffff0000, v187\n"
      "v_mul_f32_e64 v204, -v204, v200\n"
      "v_mul_f32_e64 v205, -v205, v201\n"
      "v_mul_f32 v200, 0x4b800000, v200\n"
      "v_mul_f32 v201, 0x4b800000, v201\n"
      "v_mul_f32_e64 v206, -v206, v202\n"
      "v_mul_f32_e64 v207, -v207, v203\n"
      "v_mul_f32 v202, 0x4b800000, v202\n"
      "v_mul_f32 v203, 0x4b800000, v203\n"
      "s_barrier\n"
      "ds_read_b128 v[8:11], v242 offset:0\n"
      "ds_read_b128 v[12:15], v242 offset:1024\n"
      "ds_read_b128 v[16:19], v242 offset:2048\n"
      "ds_read_b128 v[20:23], v242 offset:3072\n"
      "s_setprio 1\n"
      "s_waitcnt vmcnt(12)\n"
      "v_or3_b32 v208, v120, v121, v208\n"
      "v_or3_b32 v208, v122, v123, v208\n"
      "v_or3_b32 v209, v124, v125, v209\n"
      "v_or3_b32 v209, v126, v127, v209\n"
      "v_fma_mix_f32 v120, v120, v200, v204 op_sel_hi:[1,0,0]\n"
      "v_fma_mix_f32 v121, v121, v201, v205 op_sel_hi:[1,0,0]\n"
      "v_fma_mix_f32 v122, v122, v202, v206 op_sel_hi:[1,0,0]\n"
      "v_fma_mix_f32 v123, v123, v203, v207 op_sel_hi:[1,0,0]\n"
      "v_fma_mix_f32 v124, v124, v200, v204 op_sel_hi:[1,0,0]\n"
      "v_fma_mix_f32 v125, v125, v201, v205 op_sel_hi:[1,0,0]\n"
      "v_fma_mix_f32 v126, v126, v202, v206 op_sel_hi:[1,0,0]\n"
      "v_fma_mix_f32 v127, v127, v203, v207 op_sel_hi:[1,0,0]\n"
      "v_cvt_pk_bf16_f32 v184, v120, v124\n"
      "v_cvt_pk_bf16_f32 v188, v121, v125\n"
      "v_cvt_pk_bf16_f32 v192, v122, v126\n"
      "v_cvt_pk_bf16_f32 v196, v123, v127\n"
      "s_waitcnt vmcnt(10)\n"
      "v_or3_b32 v208, v128, v129, v208\n"
      "v_or3_b32 v208, v130, v131, v208\n"
      "v_or3_b32 v209, v132, v133, v209\n"
      "v_or3_b32 v209, v134, v135, v209\n"
      "v_fma_mix_f32 v128, v128, v200, v204 op_sel_hi:[1,0,0]\n"
      "v_fma_mix_f32 v129, v129, v201, v205 op_sel_hi:[1,0,0]\n"
      "v_fma_mix_f32 v130, v130, v202, v206 op_sel_hi:[1,0,0]\n"
      "v_fma_mix_f32 v131, v131, v203, v207 op_sel_hi:[1,0,0]\n"
      "v_fma_mix_f32 v132, v132, v200, v204 op_sel_hi:[1,0,0]\n"
      "v_fma_mix_f32 v133, v133, v201, v205 op_sel_hi:[1,0,0]\n"
      "v_fma_mix_f32 v134, v134, v202, v206 op_sel_hi:[1,0,0]\n"
      "v_fma_mix_f32 v135, v135, v203, v207 op_sel_hi:[1,0,0]\n"
      "v_cvt_pk_bf16_f32 v185, v128, v132\n"
      "v_cvt_pk_bf16_f32 v189, v129, v133\n"
      "v_cvt_pk_bf16_f32 v193, v130, v134\n"
      "v_cvt_pk_bf16_f32 v197, v131, v135\n"
      "s_waitcnt vmcnt(6)\n"
      "v_or3_b32 v208, v136, v140, v208\n"
      "v_or3_b32 v209, v144, v148, v209\n"
      "v_fma_mix_f32 v136, v136, v200, v204 op_sel_hi:[1,0,0]\n"
      "v_fma_mix_f32 v140, v140, v200, v204 op_sel_hi:[1,0,0]\n"
      "v_fma_mix_f32 v144, v144, v200, v204 op_sel_hi:[1,0,0]\n"
      "v_fma_mix_f32 v148, v148, v200, v204 op_sel_hi:[1,0,0]\n"
      "v_cvt_pk_bf16_f32 v186, v136, v140\n"
      "v_cvt_pk_bf16_f32 v187, v144, v148\n"
      "ds_write_b128 v244, v[184:187] offset:16384\n"
      "v_or3_b32 v208, v137, v141, v208\n"
      "v_or3_b32 v209, v145, v149, v209\n"
      "v_fma_mix_f32 v137, v137, v201, v205 op_sel_hi:[1,0,0]\n"
      "v_fma_mix_f32 v141, v141, v201, v205 op_sel_hi:[1,0,0]\n"
      "v_fma_mix_f32 v145, v145, v201, v205 op_sel_hi:[1,0,0]\n"
      "v_fma_mix_f32 v149, v149, v201, v205 op_sel_hi:[1,0,0]\n"
      "v_cvt_pk_bf16_f32 v190, v137, v141\n"
      "v_cvt_pk_bf16_f32 v191, v145, v149\n"
      "ds_write_b128 v244, v[188:191] offset:17408\n"
      "v_or3_b32 v208, v138, v142, v208\n"
      "v_or3_b32 v209, v146, v150, v209\n"
      "v_fma_mix_f32 v138, v138, v202, v206 op_sel_hi:[1,0,0]\n"
      "v_fma_mix_f32 v142, v142, v202, v206 op_sel_hi:[1,0,0]\n"
      "v_fma_mix_f32 v146, v146, v202, v206 op_sel_hi:[1,0,0]\n"
      "v_fma_mix_f32 v150, v150, v202, v206 op_sel_hi:[1,0,0]\n"
      "v_cvt_pk_bf16_f32 v194, v138, v142\n"
      "v_cvt_pk_bf16_f32 v195, v146, v150\n"
      "ds_write_b128 v245, v[192:195] offset:16384\n"
      "v_or3_b32 v208, v139, v143, v208\n"
      "v_or3_b32 v209, v147, v151, v209\n"
      "v_fma_mix_f32 v139, v139, v203, v207 op_sel_hi:[1,0,0]\n"
      "v_fma_mix_f32 v143, v143, v203, v207 op_sel_hi:[1,0,0]\n"
      "v_fma_mix_f32 v147, v147, v203, v207 op_sel_hi:[1,0,0]\n"
      "v_fma_mix_f32 v151, v151, v203, v207 op_sel_hi:[1,0,0]\n"
      "v_cvt_pk_bf16_f32 v198, v139, v143\n"
      "v_cvt_pk_bf16_f32 v199, v147, v151\n"
      "ds_write_b128 v245, v[196:199] offset:17408\n"
      "s_setprio 0\n"
      "s_mov_b32 s63, 3612672\n"
      "buffer_load_dwordx4 v[72:75], v210, s[20:23], s63 offen\n"
      "buffer_load_dwordx4 v[76:79], v211, s[20:23], s63 offen\n"
      "buffer_load_dwordx4 v[80:83], v212, s[20:23], s63 offen\n"
      "buffer_load_dwordx4 v[84:87], v213, s[20:23], s63 offen\n"
      "s_waitcnt lgkmcnt(0)\n"
      "s_barrier\n"
      "ds_read_b128 v[24:27], v242 offset:16384\n"
      "ds_read_b128 v[28:31], v242 offset:17408\n"
      "ds_read_b128 v[32:35], v242 offset:18432\n"
      "ds_read_b128 v[36:39], v242 offset:19456\n"
      "s_mov_b32 s63, 5898240\n"
      "buffer_load_dwordx4 v[88:91], v210, s[20:23], s63 offen\n"
      "buffer_load_dwordx4 v[92:95], v211, s[20:23], s63 offen\n"
      "buffer_load_dwordx4 v[96:99], v212, s[20:23], s63 offen\n"
      "buffer_load_dwordx4 v[100:103], v213, s[20:23], s63 offen\n"
      "ds_read_b64 v[184:185], v246 offset:128\n"
      "ds_read_b64 v[186:187], v246 offset:768\n"
      "s_waitcnt lgkmcnt(0)\n"
      "v_lshlrev_b32 v200, 16, v184\n"
      "v_and_b32 v201, 0xffff0000, v184\n"
      "v_lshlrev_b32 v204, 16, v186\n"
      "v_and_b32 v205, 0xffff0000, v186\n"
      "v_lshlrev_b32 v202, 16, v185\n"
      "v_and_b32 v203, 0xffff0000, v185\n"
      "v_lshlrev_b32 v206, 16, v187\n"
      "v_and_b32 v207, 0xffff0000, v187\n"
      "v_mul_f32_e64 v204, -v204, v200\n"
      "v_mul_f32_e64 v205, -v205, v201\n"
      "v_mul_f32 v200, 0x4b800000, v200\n"
      "v_mul_f32 v201, 0x4b800000, v201\n"
      "v_mul_f32_e64 v206, -v206, v202\n"
      "v_mul_f32_e64 v207, -v207, v203\n"
      "v_mul_f32 v202, 0x4b800000, v202\n"
      "v_mul_f32 v203, 0x4b800000, v203\n"
      "s_waitcnt vmcnt(10)\n"
      "v_or3_b32 v208, v56, v57, v208\n"
      "v_or3_b32 v208, v58, v59, v208\n"
      "v_or3_b32 v209, v60, v61, v209\n"
      "v_or3_b32 v209, v62, v63, v209\n"
      "v_fma_mix_f32 v56, v56, v200, v204 op_sel_hi:[1,0,0]\n"
      "v_fma_mix_f32 v57, v57, v201, v205 op_sel_hi:[1,0,0]\n"
      "v_fma_mix_f32 v58, v58, v202, v206 op_sel_hi:[1,0,0]\n"
      "v_fma_mix_f32 v59, v59, v203, v207 op_sel_hi:[1,0,0]\n"
      "v_fma_mix_f32 v60, v60, v200, v204 op_sel_hi:[1,0,0]\n"
      "v_fma_mix_f32 v61, v61, v201, v205 op_sel_hi:[1,0,0]\n"
      "v_fma_mix_f32 v62, v62, v202, v206 op_sel_hi:[1,0,0]\n"
      "v_fma_mix_f32 v63, v63, v203, v207 op_sel_hi:[1,0,0]\n"
      "v_cvt_pk_bf16_f32 v184, v56, v60\n"
      "v_cvt_pk_bf16_f32 v188, v57, v61\n"
      "v_cvt_pk_bf16_f32 v192, v58, v62\n"
      "v_cvt_pk_bf16_f32 v196, v59, v63\n"
      "s_waitcnt vmcnt(8)\n"
      "v_or3_b32 v208, v64, v65, v208\n"
      "v_or3_b32 v208, v66, v67, v208\n"
      "v_or3_b32 v209, v68, v69, v209\n"
      "v_or3_b32 v209, v70, v71, v209\n"
      "v_fma_mix_f32 v64, v64, v200, v204 op_sel_hi:[1,0,0]\n"
      "v_fma_mix_f32 v65, v65, v201, v205 op_sel_hi:[1,0,0]\n"
      "v_fma_mix_f32 v66, v66, v202, v206 op_sel_hi:[1,0,0]\n"
      "v_fma_mix_f32 v67, v67, v203, v207 op_sel_hi:[1,0,0]\n"
      "v_fma_mix_f32 v68, v68, v200, v204 op_sel_hi:[1,0,0]\n"
      "v_fma_mix_f32 v69, v69, v201, v205 op_sel_hi:[1,0,0]\n"
      "v_fma_mix_f32 v70, v70, v202, v206 op_sel_hi:[1,0,0]\n"
      "v_fma_mix_f32 v71, v71, v203, v207 op_sel_hi:[1,0,0]\n"
      "v_cvt_pk_bf16_f32 v185, v64, v68\n"
      "v_cvt_pk_bf16_f32 v189, v65, v69\n"
      "v_cvt_pk_bf16_f32 v193, v66, v70\n"
      "v_cvt_pk_bf16_f32 v197, v67, v71\n"
      "s_barrier\n"
      "ds_read_b128 v[40:43], v242 offset:0\n"
      "ds_read_b128 v[44:47], v242 offset:1024\n"
      "ds_read_b128 v[48:51], v242 offset:2048\n"
      "ds_read_b128 v[52:55], v242 offset:3072\n"
      "s_setprio 1\n"
      "s_waitcnt vmcnt(4)\n"
      "v_or3_b32 v208, v72, v76, v208\n"
      "v_or3_b32 v209, v80, v84, v209\n"
      "v_fma_mix_f32 v72, v72, v200, v204 op_sel_hi:[1,0,0]\n"
      "v_fma_mix_f32 v76, v76, v200, v204 op_sel_hi:[1,0,0]\n"
      "v_fma_mix_f32 v80, v80, v200, v204 op_sel_hi:[1,0,0]\n"
      "v_fma_mix_f32 v84, v84, v200, v204 op_sel_hi:[1,0,0]\n"
      "v_cvt_pk_bf16_f32 v186, v72, v76\n"
      "v_cvt_pk_bf16_f32 v187, v80, v84\n"
      "ds_write_b128 v244, v[184:187] offset:16384\n"
      "v_or3_b32 v208, v73, v77, v208\n"
      "v_or3_b32 v209, v81, v85, v209\n"
      "v_fma_mix_f32 v73, v73, v201, v205 op_sel_hi:[1,0,0]\n"
      "v_fma_mix_f32 v77, v77, v201, v205 op_sel_hi:[1,0,0]\n"
      "v_fma_mix_f32 v81, v81, v201, v205 op_sel_hi:[1,0,0]\n"
      "v_fma_mix_f32 v85, v85, v201, v205 op_sel_hi:[1,0,0]\n"
      "v_cvt_pk_bf16_f32 v190, v73, v77\n"
      "v_cvt_pk_bf16_f32 v191, v81, v85\n"
      "ds_write_b128 v244, v[188:191] offset:17408\n"
      "v_or3_b32 v208, v74, v78, v208\n"
      "v_or3_b32 v209, v82, v86, v209\n"
      "v_fma_mix_f32 v74, v74, v202, v206 op_sel_hi:[1,0,0]\n"
      "v_fma_mix_f32 v78, v78, v202, v206 op_sel_hi:[1,0,0]\n"
      "v_fma_mix_f32 v82, v82, v202, v206 op_sel_hi:[1,0,0]\n"
      "v_fma_mix_f32 v86, v86, v202, v206 op_sel_hi:[1,0,0]\n"
      "v_cvt_pk_bf16_f32 v194, v74, v78\n"
      "v_cvt_pk_bf16_f32 v195, v82, v86\n"
      "ds_write_b128 v245, v[192:195] offset:16384\n"
      "v_or3_b32 v208, v75, v79, v208\n"
      "v_or3_b32 v209, v83, v87, v209\n"
      "v_fma_mix_f32 v75, v75, v203, v207 op_sel_hi:[1,0,0]\n"
      "v_fma_mix_f32 v79, v79, v203, v207 op_sel_hi:[1,0,0]\n"
      "v_fma_mix_f32 v83, v83, v203, v207 op_sel_hi:[1,0,0]\n"
      "v_fma_mix_f32 v87, v87, v203, v207 op_sel_hi:[1,0,0]\n"
      "v_cvt_pk_bf16_f32 v198, v75, v79\n"
      "v_cvt_pk_bf16_f32 v199, v83, v87\n"
      "ds_write_b128 v245, v[196:199] offset:17408\n"
      "s_setprio 0\n"
      "s_mov_b32 s63, 5971968\n"
      "buffer_load_dwordx4 v[104:107], v210, s[20:23], s63 offen\n"
      "buffer_load_dwordx4 v[108:111], v211, s[20:23], s63 offen\n"
      "buffer_load_dwordx4 v[112:115], v212, s[20:23], s63 offen\n"
      "buffer_load_dwordx4 v[116:119], v213, s[20:23], s63 offen\n"
      "s_waitcnt lgkmcnt(0)\n"
      "s_barrier\n"
      "ds_read_b128 v[56:59], v242 offset:16384\n"
      "ds_read_b128 v[60:63], v242 offset:17408\n"
      "ds_read_b128 v[64:67], v242 offset:18432\n"
      "ds_read_b128 v[68:71], v242 offset:19456\n"
      "s_mov_b32 s63, 8257536\n"
      "buffer_load_dwordx4 v[120:123], v210, s[20:23], s63 offen\n"
      "buffer_load_dwordx4 v[124:127], v211, s[20:23], s63 offen\n"
      "buffer_load_dwordx4 v[128:131], v212, s[20:23], s63 offen\n"
      "buffer_load_dwordx4 v[132:135], v213, s[20:23], s63 offen\n"
      "ds_read_b64 v[184:185], v246 offset:256\n"
      "ds_read_b64 v[186:187], v246 offset:896\n"
      "s_waitcnt lgkmcnt(0)\n"
      "v_lshlrev_b32 v200, 16, v184\n"
      "v_and_b32 v201, 0xffff0000, v184\n"
      "v_lshlrev_b32 v204, 16, v186\n"
      "v_and_b32 v205, 0xffff0000, v186\n"
      "v_lshlrev_b32 v202, 16, v185\n"
      "v_and_b32 v203, 0xffff0000, v185\n"
      "v_lshlrev_b32 v206, 16, v187\n"
      "v_and_b32 v207, 0xffff0000, v187\n"
      "v_mul_f32_e64 v204, -v204, v200\n"
      "v_mul_f32_e64 v205, -v205, v201\n"
      "v_mul_f32 v200, 0x4b800000, v200\n"
      "v_mul_f32 v201, 0x4b800000, v201\n"
      "v_mul_f32_e64 v206, -v206, v202\n"
      "v_mul_f32_e64 v207, -v207, v203\n"
      "v_mul_f32 v202, 0x4b800000, v202\n"
      "v_mul_f32 v203, 0x4b800000, v203\n"
      "s_waitcnt vmcnt(10)\n"
      "v_or3_b32 v208, v88, v89, v208\n"
      "v_or3_b32 v208, v90, v91, v208\n"
      "v_or3_b32 v209, v92, v93, v209\n"
      "v_or3_b32 v209, v94, v95, v209\n"
      "v_fma_mix_f32 v88, v88, v200, v204 op_sel_hi:[1,0,0]\n"
      "v_fma_mix_f32 v89, v89, v201, v205 op_sel_hi:[1,0,0]\n"
      "v_fma_mix_f32 v90, v90, v202, v206 op_sel_hi:[1,0,0]\n"
      "v_fma_mix_f32 v91, v91, v203, v207 op_sel_hi:[1,0,0]\n"
      "v_fma_mix_f32 v92, v92, v200, v204 op_sel_hi:[1,0,0]\n"
      "v_fma_mix_f32 v93, v93, v201, v205 op_sel_hi:[1,0,0]\n"
      "v_fma_mix_f32 v94, v94, v202, v206 op_sel_hi:[1,0,0]\n"
      "v_fma_mix_f32 v95, v95, v203, v207 op_sel_hi:[1,0,0]\n"
      "v_cvt_pk_bf16_f32 v184, v88, v92\n"
      "v_cvt_pk_bf16_f32 v188, v89, v93\n"
      "v_cvt_pk_bf16_f32 v192, v90, v94\n"
      "v_cvt_pk_bf16_f32 v196, v91, v95\n"
      "s_waitcnt vmcnt(8)\n"
      "v_or3_b32 v208, v96, v97, v208\n"
      "v_or3_b32 v208, v98, v99, v208\n"
      "v_or3_b32 v209, v100, v101, v209\n"
      "v_or3_b32 v209, v102, v103, v209\n"
      "v_fma_mix_f32 v96, v96, v200, v204 op_sel_hi:[1,0,0]\n"
      "v_fma_mix_f32 v97, v97, v201, v205 op_sel_hi:[1,0,0]\n"
      "v_fma_mix_f32 v98, v98, v202, v206 op_sel_hi:[1,0,0]\n"
      "v_fma_mix_f32 v99, v99, v203, v207 op_sel_hi:[1,0,0]\n"
      "v_fma_mix_f32 v100, v100, v200, v204 op_sel_hi:[1,0,0]\n"
      "v_fma_mix_f32 v101, v101, v201, v205 op_sel_hi:[1,0,0]\n"
      "v_fma_mix_f32 v102, v102, v202, v206 op_sel_hi:[1,0,0]\n"
      "v_fma_mix_f32 v103, v103, v203, v207 op_sel_hi:[1,0,0]\n"
      "v_cvt_pk_bf16_f32 v185, v96, v100\n"
      "v_cvt_pk_bf16_f32 v189, v97, v101\n"
      "v_cvt_pk_bf16_f32 v193, v98, v102\n"
      "v_cvt_pk_bf16_f32 v197, v99, v103\n"
      "s_barrier\n"
      "ds_read_b128 v[72:75], v242 offset:0\n"
      "ds_read_b128 v[76:79], v242 offset:1024\n"
      "ds_read_b128 v[80:83], v242 offset:2048\n"
      "ds_read_b128 v[84:87], v242 offset:3072\n"
      "s_setprio 1\n"
      "s_waitcnt vmcnt(4)\n"
      "v_or3_b32 v208, v104, v108, v208\n"
      "v_or3_b32 v209, v112, v116, v209\n"
      "v_fma_mix_f32 v104, v104, v200, v204 op_sel_hi:[1,0,0]\n"
      "v_fma_mix_f32 v108, v108, v200, v204 op_sel_hi:[1,0,0]\n"
      "v_fma_mix_f32 v112, v112, v200, v204 op_sel_hi:[1,0,0]\n"
      "v_fma_mix_f32 v116, v116, v200, v204 op_sel_hi:[1,0,0]\n"
      "v_cvt_pk_bf16_f32 v186, v104, v108\n"
      "v_cvt_pk_bf16_f32 v187, v112, v116\n"
      "ds_write_b128 v244, v[184:187] offset:16384\n"
      "v_or3_b32 v208, v105, v109, v208\n"
      "v_or3_b32 v209, v113, v117, v209\n"
      "v_fma_mix_f32 v105, v105, v201, v205 op_sel_hi:[1,0,0]\n"
      "v_fma_mix_f32 v109, v109, v201, v205 op_sel_hi:[1,0,0]\n"
      "v_fma_mix_f32 v113, v113, v201, v205 op_sel_hi:[1,0,0]\n"
      "v_fma_mix_f32 v117, v117, v201, v205 op_sel_hi:[1,0,0]\n"
      "v_cvt_pk_bf16_f32 v190, v105, v109\n"
      "v_cvt_pk_bf16_f32 v191, v113, v117\n"
      "ds_write_b128 v244, v[188:191] offset:17408\n"
      "v_or3_b32 v208, v106, v110, v208\n"
      "v_or3_b32 v209, v114, v118, v209\n"
      "v_fma_mix_f32 v106, v106, v202, v206 op_sel_hi:[1,0,0]\n"
      "v_fma_mix_f32 v110, v110, v202, v206 op_sel_hi:[1,0,0]\n"
      "v_fma_mix_f32 v114, v114, v202, v206 op_sel_hi:[1,0,0]\n"
      "v_fma_mix_f32 v118, v118, v202, v206 op_sel_hi:[1,0,0]\n"
      "v_cvt_pk_bf16_f32 v194, v106, v110\n"
      "v_cvt_pk_bf16_f32 v195, v114, v118\n"
      "ds_write_b128 v245, v[192:195] offset:16384\n"
      "v_or3_b32 v208, v107, v111, v208\n"
      "v_or3_b32 v209, v115, v119, v209\n"
      "v_fma_mix_f32 v107, v107, v203, v207 op_sel_hi:[1,0,0]\n"
      "v_fma_mix_f32 v111, v111, v203, v207 op_sel_hi:[1,0,0]\n"
      "v_fma_mix_f32 v115, v115, v203, v207 op_sel_hi:[1,0,0]\n"
      "v_fma_mix_f32 v119, v119, v203, v207 op_sel_hi:[1,0,0]\n"
      "v_cvt_pk_bf16_f32 v198, v107, v111\n"
      "v_cvt_pk_bf16_f32 v199, v115, v119\n"
      "ds_write_b128 v245, v[196:199] offset:17408\n"
      "s_setprio 0\n"
      "s_mov_b32 s63, 8331264\n"
      "buffer_load_dwordx4 v[136:139], v210, s[20:23], s63 offen\n"
      "buffer_load_dwordx4 v[140:143], v211, s[20:23], s63 offen\n"
      "buffer_load_dwordx4 v[144:147], v212, s[20:23], s63 offen\n"
      "buffer_load_dwordx4 v[148:151], v213, s[20:23], s63 offen\n"
      "s_waitcnt lgkmcnt(0)\n"
      "s_barrier\n"
      "ds_read_b128 v[88:91], v242 offset:16384\n"
      "ds_read_b128 v[92:95], v242 offset:17408\n"
      "ds_read_b128 v[96:99], v242 offset:18432\n"
      "ds_read_b128 v[100:103], v242 offset:19456\n"
      "s_add_u32 m0, s43, 10240\n"
      "s_add_u32 s61, s36, 640\n"
      "buffer_load_dwordx4 v243, s[16:19], s61 offen lds\n"
      "s_add_u32 m0, s43, 12288\n"
      "s_add_u32 s61, s36, 768\n"
      "buffer_load_dwordx4 v243, s[16:19], s61 offen lds\n"
      "ds_read_b64 v[184:185], v246 offset:384\n"
      "ds_read_b64 v[186:187], v246 offset:1024\n"
      "s_waitcnt lgkmcnt(0)\n"
      "v_lshlrev_b32 v200, 16, v184\n"
      "v_and_b32 v201, 0xffff0000, v184\n"
      "v_lshlrev_b32 v204, 16, v186\n"
      "v_and_b32 v205, 0xffff0000, v186\n"
      "v_lshlrev_b32 v202, 16, v185\n"
      "v_and_b32 v203, 0xffff0000, v185\n"
      "v_lshlrev_b32 v206, 16, v187\n"
      "v_and_b32 v207, 0xffff0000, v187\n"
      "v_mul_f32_e64 v204, -v204, v200\n"
      "v_mul_f32_e64 v205, -v205, v201\n"
      "v_mul_f32 v200, 0x4b800000, v200\n"
      "v_mul_f32 v201, 0x4b800000, v201\n"
      "v_mul_f32_e64 v206, -v206, v202\n"
      "v_mul_f32_e64 v207, -v207, v203\n"
      "v_mul_f32 v202, 0x4b800000, v202\n"
      "v_mul_f32 v203, 0x4b800000, v203\n"
      "s_waitcnt vmcnt(8)\n"
      "v_or3_b32 v208, v120, v121, v208\n"
      "v_or3_b32 v208, v122, v123, v208\n"
      "v_or3_b32 v209, v124, v125, v209\n"
      "v_or3_b32 v209, v126, v127, v209\n"
      "v_fma_mix_f32 v120, v120, v200, v204 op_sel_hi:[1,0,0]\n"
      "v_fma_mix_f32 v121, v121, v201, v205 op_sel_hi:[1,0,0]\n"
      "v_fma_mix_f32 v122, v122, v202, v206 op_sel_hi:[1,0,0]\n"
      "v_fma_mix_f32 v123, v123, v203, v207 op_sel_hi:[1,0,0]\n"
      "v_fma_mix_f32 v124, v124, v200, v204 op_sel_hi:[1,0,0]\n"
      "v_fma_mix_f32 v125, v125, v201, v205 op_sel_hi:[1,0,0]\n"
      "v_fma_mix_f32 v126, v126, v202, v206 op_sel_hi:[1,0,0]\n"
      "v_fma_mix_f32 v127, v127, v203, v207 op_sel_hi:[1,0,0]\n"
      "v_cvt_pk_bf16_f32 v184, v120, v124\n"
      "v_cvt_pk_bf16_f32 v188, v121, v125\n"
      "v_cvt_pk_bf16_f32 v192, v122, v126\n"
      "v_cvt_pk_bf16_f32 v196, v123, v127\n"
      "s_waitcnt vmcnt(6)\n"
      "v_or3_b32 v208, v128, v129, v208\n"
      "v_or3_b32 v208, v130, v131, v208\n"
      "v_or3_b32 v209, v132, v133, v209\n"
      "v_or3_b32 v209, v134, v135, v209\n"
      "v_fma_mix_f32 v128, v128, v200, v204 op_sel_hi:[1,0,0]\n"
      "v_fma_mix_f32 v129, v129, v201, v205 op_sel_hi:[1,0,0]\n"
      "v_fma_mix_f32 v130, v130, v202, v206 op_sel_hi:[1,0,0]\n"
      "v_fma_mix_f32 v131, v131, v203, v207 op_sel_hi:[1,0,0]\n"
      "v_fma_mix_f32 v132, v132, v200, v204 op_sel_hi:[1,0,0]\n"
      "v_fma_mix_f32 v133, v133, v201, v205 op_sel_hi:[1,0,0]\n"
      "v_fma_mix_f32 v134, v134, v202, v206 op_sel_hi:[1,0,0]\n"
      "v_fma_mix_f32 v135, v135, v203, v207 op_sel_hi:[1,0,0]\n"
      "v_cvt_pk_bf16_f32 v185, v128, v132\n"
      "v_cvt_pk_bf16_f32 v189, v129, v133\n"
      "v_cvt_pk_bf16_f32 v193, v130, v134\n"
      "v_cvt_pk_bf16_f32 v197, v131, v135\n"
      "s_barrier\n"
      "ds_read_b128 v[104:107], v242 offset:0\n"
      "ds_read_b128 v[108:111], v242 offset:1024\n"
      "ds_read_b128 v[112:115], v242 offset:2048\n"
      "ds_read_b128 v[116:119], v242 offset:3072\n"
      "s_setprio 1\n"
      "s_waitcnt vmcnt(2)\n"
      "v_or3_b32 v208, v136, v140, v208\n"
      "v_or3_b32 v209, v144, v148, v209\n"
      "v_fma_mix_f32 v136, v136, v200, v204 op_sel_hi:[1,0,0]\n"
      "v_fma_mix_f32 v140, v140, v200, v204 op_sel_hi:[1,0,0]\n"
      "v_fma_mix_f32 v144, v144, v200, v204 op_sel_hi:[1,0,0]\n"
      "v_fma_mix_f32 v148, v148, v200, v204 op_sel_hi:[1,0,0]\n"
      "v_cvt_pk_bf16_f32 v186, v136, v140\n"
      "v_cvt_pk_bf16_f32 v187, v144, v148\n"
      "ds_write_b128 v244, v[184:187] offset:16384\n"
      "v_or3_b32 v208, v137, v141, v208\n"
      "v_or3_b32 v209, v145, v149, v209\n"
      "v_fma_mix_f32 v137, v137, v201, v205 op_sel_hi:[1,0,0]\n"
      "v_fma_mix_f32 v141, v141, v201, v205 op_sel_hi:[1,0,0]\n"
      "v_fma_mix_f32 v145, v145, v201, v205 op_sel_hi:[1,0,0]\n"
      "v_fma_mix_f32 v149, v149, v201, v205 op_sel_hi:[1,0,0]\n"
      "v_cvt_pk_bf16_f32 v190, v137, v141\n"
      "v_cvt_pk_bf16_f32 v191, v145, v149\n"
      "ds_write_b128 v244, v[188:191] offset:17408\n"
      "v_or3_b32 v208, v138, v142, v208\n"
      "v_or3_b32 v209, v146, v150, v209\n"
      "v_fma_mix_f32 v138, v138, v202, v206 op_sel_hi:[1,0,0]\n"
      "v_fma_mix_f32 v142, v142, v202, v206 op_sel_hi:[1,0,0]\n"
      "v_fma_mix_f32 v146, v146, v202, v206 op_sel_hi:[1,0,0]\n"
      "v_fma_mix_f32 v150, v150, v202, v206 op_sel_hi:[1,0,0]\n"
      "v_cvt_pk_bf16_f32 v194, v138, v142\n"
      "v_cvt_pk_bf16_f32 v195, v146, v150\n"
      "ds_write_b128 v245, v[192:195] offset:16384\n"
      "v_or3_b32 v208, v139, v143, v208\n"
      "v_or3_b32 v209, v147, v151, v209\n"
      "v_fma_mix_f32 v139, v139, v203, v207 op_sel_hi:[1,0,0]\n"
      "v_fma_mix_f32 v143, v143, v203, v207 op_sel_hi:[1,0,0]\n"
      "v_fma_mix_f32 v147, v147, v203, v207 op_sel_hi:[1,0,0]\n"
      "v_fma_mix_f32 v151, v151, v203, v207 op_sel_hi:[1,0,0]\n"
      "v_cvt_pk_bf16_f32 v198, v139, v143\n"
      "v_cvt_pk_bf16_f32 v199, v147, v151\n"
      "ds_write_b128 v245, v[196:199] offset:17408\n"
      "v_or_b32 v208, v208, v209\n"
      "v_cmp_lt_u32 vcc, 0x7f, v208\n"
      "s_or_b64 s[66:67], s[66:67], vcc\n"
      "s_or_b32 s66, s66, s67\n"
      "v_mov_b32 v201, s45\n"
      "v_mov_b32 v200, s66\n"
      "ds_write_b32 v201, v200\n"
      "s_setprio 0\n"
      "s_add_u32 m0, s43, 28672\n"
      "s_add_u32 s61, s36, 19072\n"
      "buffer_load_dwordx4 v243, s[16:19], s61 offen lds\n"
      "s_add_u32 m0, s43, 30720\n"
      "s_add_u32 s61, s36, 19200\n"
      "buffer_load_dwordx4 v243, s[16:19], s61 offen lds\n"
      "s_waitcnt lgkmcnt(0)\n"
      "s_waitcnt vmcnt(2)\n"
      "s_barrier\n"
      "ds_read_b128 v[120:123], v242 offset:16384\n"
      "ds_read_b128 v[124:127], v242 offset:17408\n"
      "ds_read_b128 v[128:131], v242 offset:18432\n"
      "ds_read_b128 v[132:135], v242 offset:19456\n"
      "s_add_u32 m0, s43, 47104\n"
      "s_add_u32 s61, s36, 37504\n"
      "buffer_load_dwordx4 v243, s[16:19], s61 offen lds\n"
      "s_add_u32 m0, s43, 49152\n"
      "s_add_u32 s61, s36, 37632\n"
      "buffer_load_dwordx4 v243, s[16:19], s61 offen lds\n"
      "s_waitcnt lgkmcnt(0)\n"
      "s_waitcnt vmcnt(2)\n"
      "s_barrier\n"
      "ds_read_b128 v[136:139], v242 offset:0\n"
      "ds_read_b128 v[140:143], v242 offset:1024\n"
      "ds_read_b128 v[144:147], v242 offset:2048\n"
      "ds_read_b128 v[148:151], v242 offset:3072\n"
      "s_add_u32 m0, s43, 65536\n"
      "s_add_u32 s61, s36, 55936\n"
      "buffer_load_dwordx4 v243, s[16:19], s61 offen lds\n"
      "s_add_u32 m0, s43, 67584\n"
      "s_add_u32 s61, s36, 56064\n"
      "buffer_load_dwordx4 v243, s[16:19], s61 offen lds\n"
      "s_waitcnt lgkmcnt(0)\n"
      "ds_read_b128 v[168:171], v240 offset:18432\n"
      "ds_read_b128 v[172:175], v241 offset:18432\n"
      "ds_read_b128 v[176:179], v240 offset:20480\n"
      "ds_read_b128 v[180:183], v241 offset:20480\n"
      "ds_read_b128 v[184:187], v240 offset:22528\n"
      "ds_read_b128 v[188:191], v241 offset:22528\n"
      "s_setprio 1\n"
      "s_add_u32 m0, s43, 92160\n"
      "s_add_u32 s61, s36, 92160\n"
      "buffer_load_dwordx4 v243, s[16:19], s61 offen lds\n"
      "s_add_u32 m0, s43, 94208\n"
      "s_add_u32 s61, s36, 92288\n"
      "buffer_load_dwordx4 v243, s[16:19], s61 offen lds\n"
      "s_add_u32 m0, s43, 96256\n"
      "s_add_u32 s61, s36, 92416\n"
      "buffer_load_dwordx4 v243, s[16:19], s61 offen lds\n"
      "s_add_u32 m0, s43, 98304\n"
      "s_add_u32 s61, s36, 92544\n"
      "buffer_load_dwordx4 v243, s[16:19], s61 offen lds\n"
      "s_add_u32 m0, s43, 100352\n"
      "s_add_u32 s61, s36, 92672\n"
      "buffer_load_dwordx4 v243, s[16:19], s61 offen lds\n"
      "s_setprio 0\n"
      "s_waitcnt lgkmcnt(5)\n"
      "v_mfma_f32_16x16x32_bf16 v[152:155], v[168:171], v[8:11], 0\n"
      "ds_read_b128 v[192:195], v240 offset:24576\n"
      "v_mfma_f32_16x16x32_bf16 v[156:159], v[168:171], v[12:15], 0\n"
      "s_waitcnt lgkmcnt(5)\n"
      "v_mfma_f32_16x16x32_bf16 v[152:155], v[172:175], v[16:19], v[152:155]\n"
      "ds_read_b128 v[196:199], v241 offset:24576\n"
      "v_mfma_f32_16x16x32_bf16 v[156:159], v[172:175], v[20:23], v[156:159]\n"
      "s_waitcnt lgkmcnt(5)\n"
      "v_mfma_f32_16x16x32_bf16 v[152:155], v[176:179], v[24:27], v[152:155]\n"
      "ds_read_b128 v[168:171], v240 offset:26624\n"
      "v_mfma_f32_16x16x32_bf16 v[156:159], v[176:179], v[28:31], v[156:159]\n"
      "s_waitcnt lgkmcnt(5)\n"
      "v_mfma_f32_16x16x32_bf16 v[152:155], v[180:183], v[32:35], v[152:155]\n"
      "ds_read_b128 v[172:175], v241 offset:26624\n"
      "v_mfma_f32_16x16x32_bf16 v[156:159], v[180:183], v[36:39], v[156:159]\n"
      "s_waitcnt lgkmcnt(5)\n"
      "v_mfma_f32_16x16x32_bf16 v[152:155], v[184:187], v[40:43], v[152:155]\n"
      "ds_read_b128 v[176:179], v240 offset:28672\n"
      "v_mfma_f32_16x16x32_bf16 v[156:159], v[184:187], v[44:47], v[156:159]\n"
      "s_waitcnt lgkmcnt(5)\n"
      "v_mfma_f32_16x16x32_bf16 v[152:155], v[188:191], v[48:51], v[152:155]\n"
      "ds_read_b128 v[180:183], v241 offset:28672\n"
      "v_mfma_f32_16x16x32_bf16 v[156:159], v[188:191], v[52:55], v[156:159]\n"
      "s_waitcnt lgkmcnt(5)\n"
      "v_mfma_f32_16x16x32_bf16 v[152:155], v[192:195], v[56:59], v[152:155]\n"
      "ds_read_b128 v[184:187], v240 offset:30720\n"
      "v_mfma_f32_16x16x32_bf16 v[156:159], v[192:195], v[60:63], v[156:159]\n"
      "s_waitcnt lgkmcnt(5)\n"
      "v_mfma_f32_16x16x32_bf16 v[152:155], v[196:199], v[64:67], v[152:155]\n"
      "ds_read_b128 v[188:191], v241 offset:30720\n"
      "v_mfma_f32_16x16x32_bf16 v[156:159], v[196:199], v[68:71], v[156:159]\n"
      "s_waitcnt lgkmcnt(5)\n"
      "v_mfma_f32_16x16x32_bf16 v[152:155], v[168:171], v[72:75], v[152:155]\n"
      "ds_read_b128 v[192:195], v240 offset:32768\n"
      "v_mfma_f32_16x16x32_bf16 v[156:159], v[168:171], v[76:79], v[156:159]\n"
      "s_waitcnt vmcnt(5)\n"
      "s_barrier\n"
      "s_setprio 1\n"
      "s_add_u32 m0, s43, 129024\n"
      "s_add_u32 s61, s36, 129024\n"
      "buffer_load_dwordx4 v243, s[16:19], s61 offen lds\n"
      "s_add_u32 m0, s43, 131072\n"
      "s_add_u32 s61, s36, 129152\n"
      "buffer_load_dwordx4 v243, s[16:19], s61 offen lds\n"
      "s_add_u32 m0, s43, 133120\n"
      "s_add_u32 s61, s36, 129280\n"
      "buffer_load_dwordx4 v243, s[16:19], s61 offen lds\n"
      "s_add_u32 m0, s43, 135168\n"
      "s_add_u32 s61, s36, 129408\n"
      "buffer_load_dwordx4 v243, s[16:19], s61 offen lds\n"
      "s_add_u32 m0, s43, 137216\n"
      "s_add_u32 s61, s36, 129536\n"
      "buffer_load_dwordx4 v243, s[16:19], s61 offen lds\n"
      "s_setprio 0\n"
      "s_waitcnt lgkmcnt(5)\n"
      "v_mfma_f32_16x16x32_bf16 v[152:155], v[172:175], v[80:83], v[152:155]\n"
      "ds_read_b128 v[196:199], v241 offset:32768\n"
      "v_mfma_f32_16x16x32_bf16 v[156:159], v[172:175], v[84:87], v[156:159]\n"
      "s_waitcnt lgkmcnt(5)\n"
      "v_mfma_f32_16x16x32_bf16 v[152:155], v[176:179], v[88:91], v[152:155]\n"
      "ds_read_b128 v[168:171], v240 offset:34816\n"
      "v_mfma_f32_16x16x32_bf16 v[156:159], v[176:179], v[92:95], v[156:159]\n"
      "s_waitcnt lgkmcnt(5)\n"
      "v_mfma_f32_16x16x32_bf16 v[152:155], v[180:183], v[96:99], v[152:155]\n"
      "ds_read_b128 v[172:175], v241 offset:34816\n"
      "v_mfma_f32_16x16x32_bf16 v[156:159], v[180:183], v[100:103], v[156:159]\n"
      "s_waitcnt lgkmcnt(5)\n"
      "v_mfma_f32_16x16x32_bf16 v[152:155], v[184:187], v[104:107], v[152:155]\n"
      "ds_read_b128 v[176:179], v240 offset:55296\n"
      "v_mfma_f32_16x16x32_bf16 v[156:159], v[184:187], v[108:111], v[156:159]\n"
      "s_waitcnt lgkmcnt(5)\n"
      "v_mfma_f32_16x16x32_bf16 v[152:155], v[188:191], v[112:115], v[152:155]\n"
      "ds_read_b128 v[180:183], v241 offset:55296\n"
      "v_mfma_f32_16x16x32_bf16 v[156:159], v[188:191], v[116:119], v[156:159]\n"
      "s_waitcnt lgkmcnt(5)\n"
      "v_mfma_f32_16x16x32_bf16 v[152:155], v[192:195], v[120:123], v[152:155]\n"
      "ds_read_b128 v[184:187], v240 offset:57344\n"
      "v_mfma_f32_16x16x32_bf16 v[156:159], v[192:195], v[124:127], v[156:159]\n"
      "s_waitcnt lgkmcnt(5)\n"
      "v_mfma_f32_16x16x32_bf16 v[152:155], v[196:199], v[128:131], v[152:155]\n"
      "ds_read_b128 v[188:191], v241 offset:57344\n"
      "v_mfma_f32_16x16x32_bf16 v[156:159], v[196:199], v[132:135], v[156:159]\n"
      "s_waitcnt lgkmcnt(5)\n"
      "v_mfma_f32_16x16x32_bf16 v[152:155], v[168:171], v[136:139], v[152:155]\n"
      "ds_read_b128 v[192:195], v240 offset:59392\n"
      "v_mfma_f32_16x16x32_bf16 v[156:159], v[168:171], v[140:143], v[156:159]\n"
      "s_waitcnt lgkmcnt(5)\n"
      "v_mfma_f32_16x16x32_bf16 v[152:155], v[172:175], v[144:147], v[152:155]\n"
      "ds_read_b128 v[196:199], v241 offset:59392\n"
      "v_mfma_f32_16x16x32_bf16 v[156:159], v[172:175], v[148:151], v[156:159]\n"
      "s_waitcnt lgkmcnt(5)\n"
      "v_mfma_f32_16x16x32_bf16 v[160:163], v[176:179], v[8:11], 0\n"
      "ds_read_b128 v[168:171], v240 offset:61440\n"
      "v_mfma_f32_16x16x32_bf16 v[164:167], v[176:179], v[12:15], 0\n"
      "s_waitcnt lgkmcnt(5)\n"
      "v_mfma_f32_16x16x32_bf16 v[160:163], v[180:183], v[16:19], v[160:163]\n"
      "ds_read_b128 v[172:175], v241 offset:61440\n"
      "v_mfma_f32_16x16x32_bf16 v[164:167], v[180:183], v[20:23], v[164:167]\n"
      "s_waitcnt lgkmcnt(5)\n"
      "v_mfma_f32_16x16x32_bf16 v[160:163], v[184:187], v[24:27], v[160:163]\n"
      "ds_read_b128 v[176:179], v240 offset:63488\n"
      "v_mfma_f32_16x16x32_bf16 v[164:167], v[184:187], v[28:31], v[164:167]\n"
      "s_waitcnt lgkmcnt(5)\n"
      "v_mfma_f32_16x16x32_bf16 v[160:163], v[188:191], v[32:35], v[160:163]\n"
      "ds_read_b128 v[180:183], v241 offset:63488\n"
      "v_mfma_f32_16x16x32_bf16 v[164:167], v[188:191], v[36:39], v[164:167]\n"
      "s_waitcnt lgkmcnt(5)\n"
      "v_mfma_f32_16x16x32_bf16 v[160:163], v[192:195], v[40:43], v[160:163]\n"
      "ds_read_b128 v[184:187], v214 offset:0\n"
      "v_mfma_f32_16x16x32_bf16 v[164:167], v[192:195], v[44:47], v[164:167]\n"
      "s_waitcnt lgkmcnt(5)\n"
      "v_mfma_f32_16x16x32_bf16 v[160:163], v[196:199], v[48:51], v[160:163]\n"
      "ds_read_b128 v[188:191], v215 offset:0\n"
      "v_mfma_f32_16x16x32_bf16 v[164:167], v[196:199], v[52:55], v[164:167]\n"
      "s_waitcnt lgkmcnt(5)\n"
      "v_mfma_f32_16x16x32_bf16 v[160:163], v[168:171], v[56:59], v[160:163]\n"
      "ds_read_b128 v[192:195], v214 offset:2048\n"
      "v_mfma_f32_16x16x32_bf16 v[164:167], v[168:171], v[60:63], v[164:167]\n"
      "s_waitcnt lgkmcnt(5)\n"
      "v_mfma_f32_16x16x32_bf16 v[160:163], v[172:175], v[64:67], v[160:163]\n"
      "ds_read_b128 v[196:199], v215 offset:2048\n"
      "v_mfma_f32_16x16x32_bf16 v[164:167], v[172:175], v[68:71], v[164:167]\n"
      "s_waitcnt lgkmcnt(5)\n"
      "v_mfma_f32_16x16x32_bf16 v[160:163], v[176:179], v[72:75], v[160:163]\n"
      "ds_read_b128 v[168:171], v214 offset:4096\n"
      "v_mfma_f32_16x16x32_bf16 v[164:167], v[176:179], v[76:79], v[164:167]\n"
      "s_waitcnt vmcnt(5)\n"
      "s_barrier\n"
      "s_setprio 1\n"
      "s_add_u32 m0, s43, 18432\n"
      "s_add_u32 s61, s36, 165888\n"
      "buffer_load_dwordx4 v243, s[16:19], s61 offen lds\n"
      "s_add_u32 m0, s43, 20480\n"
      "s_add_u32 s61, s36, 166016\n"
      "buffer_load_dwordx4 v243, s[16:19], s61 offen lds\n"
      "s_add_u32 m0, s43, 22528\n"
      "s_add_u32 s61, s36, 166144\n"
      "buffer_load_dwordx4 v243, s[16:19], s61 offen lds\n"
      "s_add_u32 m0, s43, 24576\n"
      "s_add_u32 s61, s36, 166272\n"
      "buffer_load_dwordx4 v243, s[16:19], s61 offen lds\n"
      "s_add_u32 m0, s43, 26624\n"
      "s_add_u32 s61, s36, 166400\n"
      "buffer_load_dwordx4 v243, s[16:19], s61 offen lds\n"
      "v_cvt_pk_bf16_f32 v152, v152, v156\n"
      "v_cvt_pk_bf16_f32 v153, v153, v157\n"
      "v_cvt_pk_bf16_f32 v154, v154, v158\n"
      "v_cvt_pk_bf16_f32 v155, v155, v159\n"
      "s_add_u32 s62, s37, 147456\n"
      "buffer_store_dword v152, v247, s[68:71], s62 offen sc1\n"
      "s_add_u32 s62, s37, 156672\n"
      "buffer_store_dword v153, v247, s[68:71], s62 offen sc1\n"
      "s_add_u32 s62, s37, 165888\n"
      "buffer_store_dword v154, v247, s[68:71], s62 offen sc1\n"
      "s_add_u32 s62, s37, 175104\n"
      "buffer_store_dword v155, v247, s[68:71], s62 offen sc1\n"
      "s_setprio 0\n"
      "s_waitcnt lgkmcnt(5)\n"
      "v_mfma_f32_16x16x32_bf16 v[160:163], v[180:183], v[80:83], v[160:163]\n"
      "ds_read_b128 v[172:175], v215 offset:4096\n"
      "v_mfma_f32_16x16x32_bf16 v[164:167], v[180:183], v[84:87], v[164:167]\n"
      "s_waitcnt lgkmcnt(5)\n"
      "v_mfma_f32_16x16x32_bf16 v[160:163], v[184:187], v[88:91], v[160:163]\n"
      "ds_read_b128 v[176:179], v214 offset:6144\n"
      "v_mfma_f32_16x16x32_bf16 v[164:167], v[184:187], v[92:95], v[164:167]\n"
      "s_waitcnt lgkmcnt(5)\n"
      "v_mfma_f32_16x16x32_bf16 v[160:163], v[188:191], v[96:99], v[160:163]\n"
      "ds_read_b128 v[180:183], v215 offset:6144\n"
      "v_mfma_f32_16x16x32_bf16 v[164:167], v[188:191], v[100:103], v[164:167]\n"
      "s_waitcnt lgkmcnt(5)\n"
      "v_mfma_f32_16x16x32_bf16 v[160:163], v[192:195], v[104:107], v[160:163]\n"
      "ds_read_b128 v[184:187], v214 offset:26624\n"
      "v_mfma_f32_16x16x32_bf16 v[164:167], v[192:195], v[108:111], v[164:167]\n"
      "s_waitcnt lgkmcnt(5)\n"
      "v_mfma_f32_16x16x32_bf16 v[160:163], v[196:199], v[112:115], v[160:163]\n"
      "ds_read_b128 v[188:191], v215 offset:26624\n"
      "v_mfma_f32_16x16x32_bf16 v[164:167], v[196:199], v[116:119], v[164:167]\n"
      "s_waitcnt lgkmcnt(5)\n"
      "v_mfma_f32_16x16x32_bf16 v[160:163], v[168:171], v[120:123], v[160:163]\n"
      "ds_read_b128 v[192:195], v214 offset:28672\n"
      "v_mfma_f32_16x16x32_bf16 v[164:167], v[168:171], v[124:127], v[164:167]\n"
      "s_waitcnt lgkmcnt(5)\n"
      "v_mfma_f32_16x16x32_bf16 v[160:163], v[172:175], v[128:131], v[160:163]\n"
      "ds_read_b128 v[196:199], v215 offset:28672\n"
      "v_mfma_f32_16x16x32_bf16 v[164:167], v[172:175], v[132:135], v[164:167]\n"
      "s_waitcnt lgkmcnt(5)\n"
      "v_mfma_f32_16x16x32_bf16 v[160:163], v[176:179], v[136:139], v[160:163]\n"
      "ds_read_b128 v[168:171], v214 offset:30720\n"
      "v_mfma_f32_16x16x32_bf16 v[164:167], v[176:179], v[140:143], v[164:167]\n"
      "s_waitcnt lgkmcnt(5)\n"
      "v_mfma_f32_16x16x32_bf16 v[160:163], v[180:183], v[144:147], v[160:163]\n"
      "ds_read_b128 v[172:175], v215 offset:30720\n"
      "v_mfma_f32_16x16x32_bf16 v[164:167], v[180:183], v[148:151], v[164:167]\n"
      "s_waitcnt lgkmcnt(5)\n"
      "v_mfma_f32_16x16x32_bf16 v[152:155], v[184:187], v[8:11], 0\n"
      "ds_read_b128 v[176:179], v214 offset:32768\n"
      "v_mfma_f32_16x16x32_bf16 v[156:159], v[184:187], v[12:15], 0\n"
      "s_waitcnt lgkmcnt(5)\n"
      "v_mfma_f32_16x16x32_bf16 v[152:155], v[188:191], v[16:19], v[152:155]\n"
      "ds_read_b128 v[180:183], v215 offset:32768\n"
      "v_mfma_f32_16x16x32_bf16 v[156:159], v[188:191], v[20:23], v[156:159]\n"
      "s_waitcnt lgkmcnt(5)\n"
      "v_mfma_f32_16x16x32_bf16 v[152:155], v[192:195], v[24:27], v[152:155]\n"
      "ds_read_b128 v[184:187], v214 offset:34816\n"
      "v_mfma_f32_16x16x32_bf16 v[156:159], v[192:195], v[28:31], v[156:159]\n"
      "s_waitcnt lgkmcnt(5)\n"
      "v_mfma_f32_16x16x32_bf16 v[152:155], v[196:199], v[32:35], v[152:155]\n"
      "ds_read_b128 v[188:191], v215 offset:34816\n"
      "v_mfma_f32_16x16x32_bf16 v[156:159], v[196:199], v[36:39], v[156:159]\n"
      "s_waitcnt lgkmcnt(5)\n"
      "v_mfma_f32_16x16x32_bf16 v[152:155], v[168:171], v[40:43], v[152:155]\n"
      "ds_read_b128 v[192:195], v214 offset:36864\n"
      "v_mfma_f32_16x16x32_bf16 v[156:159], v[168:171], v[44:47], v[156:159]\n"
      "s_waitcnt lgkmcnt(5)\n"
      "v_mfma_f32_16x16x32_bf16 v[152:155], v[172:175], v[48:51], v[152:155]\n"
      "ds_read_b128 v[196:199], v215 offset:36864\n"
      "v_mfma_f32_16x16x32_bf16 v[156:159], v[172:175], v[52:55], v[156:159]\n"
      "s_waitcnt lgkmcnt(5)\n"
      "v_mfma_f32_16x16x32_bf16 v[152:155], v[176:179], v[56:59], v[152:155]\n"
      "ds_read_b128 v[168:171], v214 offset:38912\n"
      "v_mfma_f32_16x16x32_bf16 v[156:159], v[176:179], v[60:63], v[156:159]\n"
      "s_waitcnt lgkmcnt(5)\n"
      "v_mfma_f32_16x16x32_bf16 v[152:155], v[180:183], v[64:67], v[152:155]\n"
      "ds_read_b128 v[172:175], v215 offset:38912\n"
      "v_mfma_f32_16x16x32_bf16 v[156:159], v[180:183], v[68:71], v[156:159]\n"
      "s_waitcnt lgkmcnt(5)\n"
      "v_mfma_f32_16x16x32_bf16 v[152:155], v[184:187], v[72:75], v[152:155]\n"
      "ds_read_b128 v[176:179], v214 offset:40960\n"
      "v_mfma_f32_16x16x32_bf16 v[156:159], v[184:187], v[76:79], v[156:159]\n"
      "s_waitcnt vmcnt(9)\n"
      "s_barrier\n"
      "s_setprio 1\n"
      "s_add_u32 m0, s43, 55296\n"
      "s_add_u32 s61, s36, 202752\n"
      "buffer_load_dwordx4 v243, s[16:19], s61 offen lds\n"
      "s_add_u32 m0, s43, 57344\n"
      "s_add_u32 s61, s36, 202880\n"
      "buffer_load_dwordx4 v243, s[16:19], s61 offen lds\n"
      "s_add_u32 m0, s43, 59392\n"
      "s_add_u32 s61, s36, 203008\n"
      "buffer_load_dwordx4 v243, s[16:19], s61 offen lds\n"
      "s_add_u32 m0, s43, 61440\n"
      "s_add_u32 s61, s36, 203136\n"
      "buffer_load_dwordx4 v243, s[16:19], s61 offen lds\n"
      "s_add_u32 m0, s43, 63488\n"
      "s_add_u32 s61, s36, 203264\n"
      "buffer_load_dwordx4 v243, s[16:19], s61 offen lds\n"
      "v_cvt_pk_bf16_f32 v160, v160, v164\n"
      "v_cvt_pk_bf16_f32 v161, v161, v165\n"
      "v_cvt_pk_bf16_f32 v162, v162, v166\n"
      "v_cvt_pk_bf16_f32 v163, v163, v167\n"
      "s_add_u32 s62, s37, 442368\n"
      "buffer_store_dword v160, v247, s[68:71], s62 offen sc1\n"
      "s_add_u32 s62, s37, 451584\n"
      "buffer_store_dword v161, v247, s[68:71], s62 offen sc1\n"
      "s_add_u32 s62, s37, 460800\n"
      "buffer_store_dword v162, v247, s[68:71], s62 offen sc1\n"
      "s_add_u32 s62, s37, 470016\n"
      "buffer_store_dword v163, v247, s[68:71], s62 offen sc1\n"
      "s_setprio 0\n"
      "s_waitcnt lgkmcnt(5)\n"
      "v_mfma_f32_16x16x32_bf16 v[152:155], v[188:191], v[80:83], v[152:155]\n"
      "ds_read_b128 v[180:183], v215 offset:40960\n"
      "v_mfma_f32_16x16x32_bf16 v[156:159], v[188:191], v[84:87], v[156:159]\n"
      "s_waitcnt lgkmcnt(5)\n"
      "v_mfma_f32_16x16x32_bf16 v[152:155], v[192:195], v[88:91], v[152:155]\n"
      "ds_read_b128 v[184:187], v214 offset:43008\n"
      "v_mfma_f32_16x16x32_bf16 v[156:159], v[192:195], v[92:95], v[156:159]\n"
      "s_waitcnt lgkmcnt(5)\n"
      "v_mfma_f32_16x16x32_bf16 v[152:155], v[196:199], v[96:99], v[152:155]\n"
      "ds_read_b128 v[188:191], v215 offset:43008\n"
      "v_mfma_f32_16x16x32_bf16 v[156:159], v[196:199], v[100:103], v[156:159]\n"
      "s_waitcnt lgkmcnt(5)\n"
      "v_mfma_f32_16x16x32_bf16 v[152:155], v[168:171], v[104:107], v[152:155]\n"
      "ds_read_b128 v[192:195], v214 offset:63488\n"
      "v_mfma_f32_16x16x32_bf16 v[156:159], v[168:171], v[108:111], v[156:159]\n"
      "s_waitcnt lgkmcnt(5)\n"
      "v_mfma_f32_16x16x32_bf16 v[152:155], v[172:175], v[112:115], v[152:155]\n"
      "ds_read_b128 v[196:199], v215 offset:63488\n"
      "v_mfma_f32_16x16x32_bf16 v[156:159], v[172:175], v[116:119], v[156:159]\n"
      "s_waitcnt lgkmcnt(5)\n"
      "v_mfma_f32_16x16x32_bf16 v[152:155], v[176:179], v[120:123], v[152:155]\n"
      "ds_read_b128 v[168:171], v216 offset:0\n"
      "v_mfma_f32_16x16x32_bf16 v[156:159], v[176:179], v[124:127], v[156:159]\n"
      "s_waitcnt lgkmcnt(5)\n"
      "v_mfma_f32_16x16x32_bf16 v[152:155], v[180:183], v[128:131], v[152:155]\n"
      "ds_read_b128 v[172:175], v217 offset:0\n"
      "v_mfma_f32_16x16x32_bf16 v[156:159], v[180:183], v[132:135], v[156:159]\n"
      "s_waitcnt lgkmcnt(5)\n"
      "v_mfma_f32_16x16x32_bf16 v[152:155], v[184:187], v[136:139], v[152:155]\n"
      "ds_read_b128 v[176:179], v216 offset:2048\n"
      "v_mfma_f32_16x16x32_bf16 v[156:159], v[184:187], v[140:143], v[156:159]\n"
      "s_waitcnt lgkmcnt(5)\n"
      "v_mfma_f32_16x16x32_bf16 v[152:155], v[188:191], v[144:147], v[152:155]\n"
      "ds_read_b128 v[180:183], v217 offset:2048\n"
      "v_mfma_f32_16x16x32_bf16 v[156:159], v[188:191], v[148:151], v[156:159]\n"
      "s_waitcnt lgkmcnt(5)\n"
      "v_mfma_f32_16x16x32_bf16 v[160:163], v[192:195], v[8:11], 0\n"
      "ds_read_b128 v[184:187], v216 offset:4096\n"
      "v_mfma_f32_16x16x32_bf16 v[164:167], v[192:195], v[12:15], 0\n"
      "s_waitcnt lgkmcnt(5)\n"
      "v_mfma_f32_16x16x32_bf16 v[160:163], v[196:199], v[16:19], v[160:163]\n"
      "ds_read_b128 v[188:191], v217 offset:4096\n"
      "v_mfma_f32_16x16x32_bf16 v[164:167], v[196:199], v[20:23], v[164:167]\n"
      "s_waitcnt lgkmcnt(5)\n"
      "v_mfma_f32_16x16x32_bf16 v[160:163], v[168:171], v[24:27], v[160:163]\n"
      "ds_read_b128 v[192:195], v216 offset:6144\n"
      "v_mfma_f32_16x16x32_bf16 v[164:167], v[168:171], v[28:31], v[164:167]\n"
      "s_waitcnt lgkmcnt(5)\n"
      "v_mfma_f32_16x16x32_bf16 v[160:163], v[172:175], v[32:35], v[160:163]\n"
      "ds_read_b128 v[196:199], v217 offset:6144\n"
      "v_mfma_f32_16x16x32_bf16 v[164:167], v[172:175], v[36:39], v[164:167]\n"
      "s_waitcnt lgkmcnt(5)\n"
      "v_mfma_f32_16x16x32_bf16 v[160:163], v[176:179], v[40:43], v[160:163]\n"
      "ds_read_b128 v[168:171], v216 offset:8192\n"
      "v_mfma_f32_16x16x32_bf16 v[164:167], v[176:179], v[44:47], v[164:167]\n"
      "s_waitcnt lgkmcnt(5)\n"
      "v_mfma_f32_16x16x32_bf16 v[160:163], v[180:183], v[48:51], v[160:163]\n"
      "ds_read_b128 v[172:175], v217 offset:8192\n"
      "v_mfma_f32_16x16x32_bf16 v[164:167], v[180:183], v[52:55], v[164:167]\n"
      "s_waitcnt lgkmcnt(5)\n"
      "v_mfma_f32_16x16x32_bf16 v[160:163], v[184:187], v[56:59], v[160:163]\n"
      "ds_read_b128 v[176:179], v216 offset:10240\n"
      "v_mfma_f32_16x16x32_bf16 v[164:167], v[184:187], v[60:63], v[164:167]\n"
      "s_waitcnt lgkmcnt(5)\n"
      "v_mfma_f32_16x16x32_bf16 v[160:163], v[188:191], v[64:67], v[160:163]\n"
      "ds_read_b128 v[180:183], v217 offset:10240\n"
      "v_mfma_f32_16x16x32_bf16 v[164:167], v[188:191], v[68:71], v[164:167]\n"
      "s_waitcnt lgkmcnt(5)\n"
      "v_mfma_f32_16x16x32_bf16 v[160:163], v[192:195], v[72:75], v[160:163]\n"
      "ds_read_b128 v[184:187], v216 offset:12288\n"
      "v_mfma_f32_16x16x32_bf16 v[164:167], v[192:195], v[76:79], v[164:167]\n"
      "s_waitcnt vmcnt(13)\n"
      "s_barrier\n"
      "s_setprio 1\n"
      "s_add_u32 m0, s43, 92160\n"
      "s_add_u32 s61, s36, 239616\n"
      "buffer_load_dwordx4 v243, s[16:19], s61 offen lds\n"
      "s_add_u32 m0, s43, 94208\n"
      "s_add_u32 s61, s36, 239744\n"
      "buffer_load_dwordx4 v243, s[16:19], s61 offen lds\n"
      "s_add_u32 m0, s43, 96256\n"
      "s_add_u32 s61, s36, 239872\n"
      "buffer_load_dwordx4 v243, s[16:19], s61 offen lds\n"
      "s_add_u32 m0, s43, 98304\n"
      "s_add_u32 s61, s36, 240000\n"
      "buffer_load_dwordx4 v243, s[16:19], s61 offen lds\n"
      "s_add_u32 m0, s43, 100352\n"
      "s_add_u32 s61, s36, 240128\n"
      "buffer_load_dwordx4 v243, s[16:19], s61 offen lds\n"
      "v_cvt_pk_bf16_f32 v152, v152, v156\n"
      "v_cvt_pk_bf16_f32 v153, v153, v157\n"
      "v_cvt_pk_bf16_f32 v154, v154, v158\n"
      "v_cvt_pk_bf16_f32 v155, v155, v159\n"
      "s_add_u32 s62, s37, 737280\n"
      "buffer_store_dword v152, v247, s[68:71], s62 offen sc1\n"
      "s_add_u32 s62, s37, 746496\n"
      "buffer_store_dword v153, v247, s[68:71], s62 offen sc1\n"
      "s_add_u32 s62, s37, 755712\n"
      "buffer_store_dword v154, v247, s[68:71], s62 offen sc1\n"
      "s_add_u32 s62, s37, 764928\n"
      "buffer_store_dword v155, v247, s[68:71], s62 offen sc1\n"
      "s_setprio 0\n"
      "s_waitcnt lgkmcnt(5)\n"
      "v_mfma_f32_16x16x32_bf16 v[160:163], v[196:199], v[80:83], v[160:163]\n"
      "ds_read_b128 v[188:191], v217 offset:12288\n"
      "v_mfma_f32_16x16x32_bf16 v[164:167], v[196:199], v[84:87], v[164:167]\n"
      "s_waitcnt lgkmcnt(5)\n"
      "v_mfma_f32_16x16x32_bf16 v[160:163], v[168:171], v[88:91], v[160:163]\n"
      "ds_read_b128 v[192:195], v216 offset:14336\n"
      "v_mfma_f32_16x16x32_bf16 v[164:167], v[168:171], v[92:95], v[164:167]\n"
      "s_waitcnt lgkmcnt(5)\n"
      "v_mfma_f32_16x16x32_bf16 v[160:163], v[172:175], v[96:99], v[160:163]\n"
      "ds_read_b128 v[196:199], v217 offset:14336\n"
      "v_mfma_f32_16x16x32_bf16 v[164:167], v[172:175], v[100:103], v[164:167]\n"
      "s_waitcnt lgkmcnt(5)\n"
      "v_mfma_f32_16x16x32_bf16 v[160:163], v[176:179], v[104:107], v[160:163]\n"
      "ds_read_b128 v[168:171], v240 offset:18432\n"
      "v_mfma_f32_16x16x32_bf16 v[164:167], v[176:179], v[108:111], v[164:167]\n"
      "s_waitcnt lgkmcnt(5)\n"
      "v_mfma_f32_16x16x32_bf16 v[160:163], v[180:183], v[112:115], v[160:163]\n"
      "ds_read_b128 v[172:175], v241 offset:18432\n"
      "v_mfma_f32_16x16x32_bf16 v[164:167], v[180:183], v[116:119], v[164:167]\n"
      "s_waitcnt lgkmcnt(5)\n"
      "v_mfma_f32_16x16x32_bf16 v[160:163], v[184:187], v[120:123], v[160:163]\n"
      "ds_read_b128 v[176:179], v240 offset:20480\n"
      "v_mfma_f32_16x16x32_bf16 v[164:167], v[184:187], v[124:127], v[164:167]\n"
      "s_waitcnt lgkmcnt(5)\n"
      "v_mfma_f32_16x16x32_bf16 v[160:163], v[188:191], v[128:131], v[160:163]\n"
      "ds_read_b128 v[180:183], v241 offset:20480\n"
      "v_mfma_f32_16x16x32_bf16 v[164:167], v[188:191], v[132:135], v[164:167]\n"
      "s_waitcnt lgkmcnt(5)\n"
      "v_mfma_f32_16x16x32_bf16 v[160:163], v[192:195], v[136:139], v[160:163]\n"
      "ds_read_b128 v[184:187], v240 offset:22528\n"
      "v_mfma_f32_16x16x32_bf16 v[164:167], v[192:195], v[140:143], v[164:167]\n"
      "s_waitcnt lgkmcnt(5)\n"
      "v_mfma_f32_16x16x32_bf16 v[160:163], v[196:199], v[144:147], v[160:163]\n"
      "ds_read_b128 v[188:191], v241 offset:22528\n"
      "v_mfma_f32_16x16x32_bf16 v[164:167], v[196:199], v[148:151], v[164:167]\n"
      "s_waitcnt lgkmcnt(5)\n"
      "v_mfma_f32_16x16x32_bf16 v[152:155], v[168:171], v[8:11], 0\n"
      "ds_read_b128 v[192:195], v240 offset:24576\n"
      "v_mfma_f32_16x16x32_bf16 v[156:159], v[168:171], v[12:15], 0\n"
      "s_waitcnt lgkmcnt(5)\n"
      "v_mfma_f32_16x16x32_bf16 v[152:155], v[172:175], v[16:19], v[152:155]\n"
      "ds_read_b128 v[196:199], v241 offset:24576\n"
      "v_mfma_f32_16x16x32_bf16 v[156:159], v[172:175], v[20:23], v[156:159]\n"
      "s_waitcnt lgkmcnt(5)\n"
      "v_mfma_f32_16x16x32_bf16 v[152:155], v[176:179], v[24:27], v[152:155]\n"
      "ds_read_b128 v[168:171], v240 offset:26624\n"
      "v_mfma_f32_16x16x32_bf16 v[156:159], v[176:179], v[28:31], v[156:159]\n"
      "s_waitcnt lgkmcnt(5)\n"
      "v_mfma_f32_16x16x32_bf16 v[152:155], v[180:183], v[32:35], v[152:155]\n"
      "ds_read_b128 v[172:175], v241 offset:26624\n"
      "v_mfma_f32_16x16x32_bf16 v[156:159], v[180:183], v[36:39], v[156:159]\n"
      "s_waitcnt lgkmcnt(5)\n"
      "v_mfma_f32_16x16x32_bf16 v[152:155], v[184:187], v[40:43], v[152:155]\n"
      "ds_read_b128 v[176:179], v240 offset:28672\n"
      "v_mfma_f32_16x16x32_bf16 v[156:159], v[184:187], v[44:47], v[156:159]\n"
      "s_waitcnt lgkmcnt(5)\n"
      "v_mfma_f32_16x16x32_bf16 v[152:155], v[188:191], v[48:51], v[152:155]\n"
      "ds_read_b128 v[180:183], v241 offset:28672\n"
      "v_mfma_f32_16x16x32_bf16 v[156:159], v[188:191], v[52:55], v[156:159]\n"
      "s_waitcnt lgkmcnt(5)\n"
      "v_mfma_f32_16x16x32_bf16 v[152:155], v[192:195], v[56:59], v[152:155]\n"
      "ds_read_b128 v[184:187], v240 offset:30720\n"
      "v_mfma_f32_16x16x32_bf16 v[156:159], v[192:195], v[60:63], v[156:159]\n"
      "s_waitcnt lgkmcnt(5)\n"
      "v_mfma_f32_16x16x32_bf16 v[152:155], v[196:199], v[64:67], v[152:155]\n"
      "ds_read_b128 v[188:191], v241 offset:30720\n"
      "v_mfma_f32_16x16x32_bf16 v[156:159], v[196:199], v[68:71], v[156:159]\n"
      "s_waitcnt lgkmcnt(5)\n"
      "v_mfma_f32_16x16x32_bf16 v[152:155], v[168:171], v[72:75], v[152:155]\n"
      "ds_read_b128 v[192:195], v240 offset:32768\n"
      "v_mfma_f32_16x16x32_bf16 v[156:159], v[168:171], v[76:79], v[156:159]\n"
      "s_waitcnt vmcnt(13)\n"
      "s_barrier\n"
      "s_setprio 1\n"
      "s_add_u32 m0, s43, 129024\n"
      "s_add_u32 s61, s36, 276480\n"
      "buffer_load_dwordx4 v243, s[16:19], s61 offen lds\n"
      "s_add_u32 m0, s43, 131072\n"
      "s_add_u32 s61, s36, 276608\n"
      "buffer_load_dwordx4 v243, s[16:19], s61 offen lds\n"
      "s_add_u32 m0, s43, 133120\n"
      "s_add_u32 s61, s36, 276736\n"
      "buffer_load_dwordx4 v243, s[16:19], s61 offen lds\n"
      "s_add_u32 m0, s43, 135168\n"
      "s_add_u32 s61, s36, 276864\n"
      "buffer_load_dwordx4 v243, s[16:19], s61 offen lds\n"
      "s_add_u32 m0, s43, 137216\n"
      "s_add_u32 s61, s36, 276992\n"
      "buffer_load_dwordx4 v243, s[16:19], s61 offen lds\n"
      "v_cvt_pk_bf16_f32 v160, v160, v164\n"
      "v_cvt_pk_bf16_f32 v161, v161, v165\n"
      "v_cvt_pk_bf16_f32 v162, v162, v166\n"
      "v_cvt_pk_bf16_f32 v163, v163, v167\n"
      "s_add_u32 s62, s37, 1032192\n"
      "buffer_store_dword v160, v247, s[68:71], s62 offen sc1\n"
      "s_add_u32 s62, s37, 1041408\n"
      "buffer_store_dword v161, v247, s[68:71], s62 offen sc1\n"
      "s_add_u32 s62, s37, 1050624\n"
      "buffer_store_dword v162, v247, s[68:71], s62 offen sc1\n"
      "s_add_u32 s62, s37, 1059840\n"
      "buffer_store_dword v163, v247, s[68:71], s62 offen sc1\n"
      "s_setprio 0\n"
      "s_waitcnt lgkmcnt(5)\n"
      "v_mfma_f32_16x16x32_bf16 v[152:155], v[172:175], v[80:83], v[152:155]\n"
      "ds_read_b128 v[196:199], v241 offset:32768\n"
      "v_mfma_f32_16x16x32_bf16 v[156:159], v[172:175], v[84:87], v[156:159]\n"
      "s_waitcnt lgkmcnt(5)\n"
      "v_mfma_f32_16x16x32_bf16 v[152:155], v[176:179], v[88:91], v[152:155]\n"
      "ds_read_b128 v[168:171], v240 offset:34816\n"
      "v_mfma_f32_16x16x32_bf16 v[156:159], v[176:179], v[92:95], v[156:159]\n"
      "s_waitcnt lgkmcnt(5)\n"
      "v_mfma_f32_16x16x32_bf16 v[152:155], v[180:183], v[96:99], v[152:155]\n"
      "ds_read_b128 v[172:175], v241 offset:34816\n"
      "v_mfma_f32_16x16x32_bf16 v[156:159], v[180:183], v[100:103], v[156:159]\n"
      "s_waitcnt lgkmcnt(5)\n"
      "v_mfma_f32_16x16x32_bf16 v[152:155], v[184:187], v[104:107], v[152:155]\n"
      "ds_read_b128 v[176:179], v240 offset:55296\n"
      "v_mfma_f32_16x16x32_bf16 v[156:159], v[184:187], v[108:111], v[156:159]\n"
      "s_waitcnt lgkmcnt(5)\n"
      "v_mfma_f32_16x16x32_bf16 v[152:155], v[188:191], v[112:115], v[152:155]\n"
      "ds_read_b128 v[180:183], v241 offset:55296\n"
      "v_mfma_f32_16x16x32_bf16 v[156:159], v[188:191], v[116:119], v[156:159]\n"
      "s_waitcnt lgkmcnt(5)\n"
      "v_mfma_f32_16x16x32_bf16 v[152:155], v[192:195], v[120:123], v[152:155]\n"
      "ds_read_b128 v[184:187], v240 offset:57344\n"
      "v_mfma_f32_16x16x32_bf16 v[156:159], v[192:195], v[124:127], v[156:159]\n"
      "s_waitcnt lgkmcnt(5)\n"
      "v_mfma_f32_16x16x32_bf16 v[152:155], v[196:199], v[128:131], v[152:155]\n"
      "ds_read_b128 v[188:191], v241 offset:57344\n"
      "v_mfma_f32_16x16x32_bf16 v[156:159], v[196:199], v[132:135], v[156:159]\n"
      "s_waitcnt lgkmcnt(5)\n"
      "v_mfma_f32_16x16x32_bf16 v[152:155], v[168:171], v[136:139], v[152:155]\n"
      "ds_read_b128 v[192:195], v240 offset:59392\n"
      "v_mfma_f32_16x16x32_bf16 v[156:159], v[168:171], v[140:143], v[156:159]\n"
      "s_waitcnt lgkmcnt(5)\n"
      "v_mfma_f32_16x16x32_bf16 v[152:155], v[172:175], v[144:147], v[152:155]\n"
      "ds_read_b128 v[196:199], v241 offset:59392\n"
      "v_mfma_f32_16x16x32_bf16 v[156:159], v[172:175], v[148:151], v[156:159]\n"
      "s_waitcnt lgkmcnt(5)\n"
      "v_mfma_f32_16x16x32_bf16 v[160:163], v[176:179], v[8:11], 0\n"
      "ds_read_b128 v[168:171], v240 offset:61440\n"
      "v_mfma_f32_16x16x32_bf16 v[164:167], v[176:179], v[12:15], 0\n"
      "s_waitcnt lgkmcnt(5)\n"
      "v_mfma_f32_16x16x32_bf16 v[160:163], v[180:183], v[16:19], v[160:163]\n"
      "ds_read_b128 v[172:175], v241 offset:61440\n"
      "v_mfma_f32_16x16x32_bf16 v[164:167], v[180:183], v[20:23], v[164:167]\n"
      "s_waitcnt lgkmcnt(5)\n"
      "v_mfma_f32_16x16x32_bf16 v[160:163], v[184:187], v[24:27], v[160:163]\n"
      "ds_read_b128 v[176:179], v240 offset:63488\n"
      "v_mfma_f32_16x16x32_bf16 v[164:167], v[184:187], v[28:31], v[164:167]\n"
      "s_waitcnt lgkmcnt(5)\n"
      "v_mfma_f32_16x16x32_bf16 v[160:163], v[188:191], v[32:35], v[160:163]\n"
      "ds_read_b128 v[180:183], v241 offset:63488\n"
      "v_mfma_f32_16x16x32_bf16 v[164:167], v[188:191], v[36:39], v[164:167]\n"
      "s_waitcnt lgkmcnt(5)\n"
      "v_mfma_f32_16x16x32_bf16 v[160:163], v[192:195], v[40:43], v[160:163]\n"
      "ds_read_b128 v[184:187], v214 offset:0\n"
      "v_mfma_f32_16x16x32_bf16 v[164:167], v[192:195], v[44:47], v[164:167]\n"
      "s_waitcnt lgkmcnt(5)\n"
      "v_mfma_f32_16x16x32_bf16 v[160:163], v[196:199], v[48:51], v[160:163]\n"
      "ds_read_b128 v[188:191], v215 offset:0\n"
      "v_mfma_f32_16x16x32_bf16 v[164:167], v[196:199], v[52:55], v[164:167]\n"
      "s_waitcnt lgkmcnt(5)\n"
      "v_mfma_f32_16x16x32_bf16 v[160:163], v[168:171], v[56:59], v[160:163]\n"
      "ds_read_b128 v[192:195], v214 offset:2048\n"
      "v_mfma_f32_16x16x32_bf16 v[164:167], v[168:171], v[60:63], v[164:167]\n"
      "s_waitcnt lgkmcnt(5)\n"
      "v_mfma_f32_16x16x32_bf16 v[160:163], v[172:175], v[64:67], v[160:163]\n"
      "ds_read_b128 v[196:199], v215 offset:2048\n"
      "v_mfma_f32_16x16x32_bf16 v[164:167], v[172:175], v[68:71], v[164:167]\n"
      "s_waitcnt lgkmcnt(5)\n"
      "v_mfma_f32_16x16x32_bf16 v[160:163], v[176:179], v[72:75], v[160:163]\n"
      "ds_read_b128 v[168:171], v214 offset:4096\n"
      "v_mfma_f32_16x16x32_bf16 v[164:167], v[176:179], v[76:79], v[164:167]\n"
      "s_waitcnt vmcnt(13)\n"
      "s_barrier\n"
      "s_setprio 1\n"
      "s_add_u32 m0, s43, 18432\n"
      "s_add_u32 s61, s36, 313344\n"
      "buffer_load_dwordx4 v243, s[16:19], s61 offen lds\n"
      "s_add_u32 m0, s43, 20480\n"
      "s_add_u32 s61, s36, 313472\n"
      "buffer_load_dwordx4 v243, s[16:19], s61 offen lds\n"
      "s_add_u32 m0, s43, 22528\n"
      "s_add_u32 s61, s36, 313600\n"
      "buffer_load_dwordx4 v243, s[16:19], s61 offen lds\n"
      "s_add_u32 m0, s43, 24576\n"
      "s_add_u32 s61, s36, 313728\n"
      "buffer_load_dwordx4 v243, s[16:19], s61 offen lds\n"
      "s_add_u32 m0, s43, 26624\n"
      "s_add_u32 s61, s36, 313856\n"
      "buffer_load_dwordx4 v243, s[16:19], s61 offen lds\n"
      "v_cvt_pk_bf16_f32 v152, v152, v156\n"
      "v_cvt_pk_bf16_f32 v153, v153, v157\n"
      "v_cvt_pk_bf16_f32 v154, v154, v158\n"
      "v_cvt_pk_bf16_f32 v155, v155, v159\n"
      "s_add_u32 s62, s37, 1327104\n"
      "buffer_store_dword v152, v247, s[68:71], s62 offen sc1\n"
      "s_add_u32 s62, s37, 1336320\n"
      "buffer_store_dword v153, v247, s[68:71], s62 offen sc1\n"
      "s_add_u32 s62, s37, 1345536\n"
      "buffer_store_dword v154, v247, s[68:71], s62 offen sc1\n"
      "s_add_u32 s62, s37, 1354752\n"
      "buffer_store_dword v155, v247, s[68:71], s62 offen sc1\n"
      "s_setprio 0\n"
      "s_waitcnt lgkmcnt(5)\n"
      "v_mfma_f32_16x16x32_bf16 v[160:163], v[180:183], v[80:83], v[160:163]\n"
      "ds_read_b128 v[172:175], v215 offset:4096\n"
      "v_mfma_f32_16x16x32_bf16 v[164:167], v[180:183], v[84:87], v[164:167]\n"
      "s_waitcnt lgkmcnt(5)\n"
      "v_mfma_f32_16x16x32_bf16 v[160:163], v[184:187], v[88:91], v[160:163]\n"
      "ds_read_b128 v[176:179], v214 offset:6144\n"
      "v_mfma_f32_16x16x32_bf16 v[164:167], v[184:187], v[92:95], v[164:167]\n"
      "s_waitcnt lgkmcnt(5)\n"
      "v_mfma_f32_16x16x32_bf16 v[160:163], v[188:191], v[96:99], v[160:163]\n"
      "ds_read_b128 v[180:183], v215 offset:6144\n"
      "v_mfma_f32_16x16x32_bf16 v[164:167], v[188:191], v[100:103], v[164:167]\n"
      "s_waitcnt lgkmcnt(5)\n"
      "v_mfma_f32_16x16x32_bf16 v[160:163], v[192:195], v[104:107], v[160:163]\n"
      "ds_read_b128 v[184:187], v214 offset:26624\n"
      "v_mfma_f32_16x16x32_bf16 v[164:167], v[192:195], v[108:111], v[164:167]\n"
      "s_waitcnt lgkmcnt(5)\n"
      "v_mfma_f32_16x16x32_bf16 v[160:163], v[196:199], v[112:115], v[160:163]\n"
      "ds_read_b128 v[188:191], v215 offset:26624\n"
      "v_mfma_f32_16x16x32_bf16 v[164:167], v[196:199], v[116:119], v[164:167]\n"
      "s_waitcnt lgkmcnt(5)\n"
      "v_mfma_f32_16x16x32_bf16 v[160:163], v[168:171], v[120:123], v[160:163]\n"
      "ds_read_b128 v[192:195], v214 offset:28672\n"
      "v_mfma_f32_16x16x32_bf16 v[164:167], v[168:171], v[124:127], v[164:167]\n"
      "s_waitcnt lgkmcnt(5)\n"
      "v_mfma_f32_16x16x32_bf16 v[160:163], v[172:175], v[128:131], v[160:163]\n"
      "ds_read_b128 v[196:199], v215 offset:28672\n"
      "v_mfma_f32_16x16x32_bf16 v[164:167], v[172:175], v[132:135], v[164:167]\n"
      "s_waitcnt lgkmcnt(5)\n"
      "v_mfma_f32_16x16x32_bf16 v[160:163], v[176:179], v[136:139], v[160:163]\n"
      "ds_read_b128 v[168:171], v214 offset:30720\n"
      "v_mfma_f32_16x16x32_bf16 v[164:167], v[176:179], v[140:143], v[164:167]\n"
      "s_waitcnt lgkmcnt(5)\n"
      "v_mfma_f32_16x16x32_bf16 v[160:163], v[180:183], v[144:147], v[160:163]\n"
      "ds_read_b128 v[172:175], v215 offset:30720\n"
      "v_mfma_f32_16x16x32_bf16 v[164:167], v[180:183], v[148:151], v[164:167]\n"
      "s_waitcnt lgkmcnt(5)\n"
      "v_mfma_f32_16x16x32_bf16 v[152:155], v[184:187], v[8:11], 0\n"
      "ds_read_b128 v[176:179], v214 offset:32768\n"
      "v_mfma_f32_16x16x32_bf16 v[156:159], v[184:187], v[12:15], 0\n"
      "s_waitcnt lgkmcnt(5)\n"
      "v_mfma_f32_16x16x32_bf16 v[152:155], v[188:191], v[16:19], v[152:155]\n"
      "ds_read_b128 v[180:183], v215 offset:32768\n"
      "v_mfma_f32_16x16x32_bf16 v[156:159], v[188:191], v[20:23], v[156:159]\n"
      "s_waitcnt lgkmcnt(5)\n"
      "v_mfma_f32_16x16x32_bf16 v[152:155], v[192:195], v[24:27], v[152:155]\n"
      "ds_read_b128 v[184:187], v214 offset:34816\n"
      "v_mfma_f32_16x16x32_bf16 v[156:159], v[192:195], v[28:31], v[156:159]\n"
      "s_waitcnt lgkmcnt(5)\n"
      "v_mfma_f32_16x16x32_bf16 v[152:155], v[196:199], v[32:35], v[152:155]\n"
      "ds_read_b128 v[188:191], v215 offset:34816\n"
      "v_mfma_f32_16x16x32_bf16 v[156:159], v[196:199], v[36:39], v[156:159]\n"
      "s_waitcnt lgkmcnt(5)\n"
      "v_mfma_f32_16x16x32_bf16 v[152:155], v[168:171], v[40:43], v[152:155]\n"
      "ds_read_b128 v[192:195], v214 offset:36864\n"
      "v_mfma_f32_16x16x32_bf16 v[156:159], v[168:171], v[44:47], v[156:159]\n"
      "s_waitcnt lgkmcnt(5)\n"
      "v_mfma_f32_16x16x32_bf16 v[152:155], v[172:175], v[48:51], v[152:155]\n"
      "ds_read_b128 v[196:199], v215 offset:36864\n"
      "v_mfma_f32_16x16x32_bf16 v[156:159], v[172:175], v[52:55], v[156:159]\n"
      "s_waitcnt lgkmcnt(5)\n"
      "v_mfma_f32_16x16x32_bf16 v[152:155], v[176:179], v[56:59], v[152:155]\n"
      "ds_read_b128 v[168:171], v214 offset:38912\n"
      "v_mfma_f32_16x16x32_bf16 v[156:159], v[176:179], v[60:63], v[156:159]\n"
      "s_waitcnt lgkmcnt(5)\n"
      "v_mfma_f32_16x16x32_bf16 v[152:155], v[180:183], v[64:67], v[152:155]\n"
      "ds_read_b128 v[172:175], v215 offset:38912\n"
      "v_mfma_f32_16x16x32_bf16 v[156:159], v[180:183], v[68:71], v[156:159]\n"
      "s_waitcnt lgkmcnt(5)\n"
      "v_mfma_f32_16x16x32_bf16 v[152:155], v[184:187], v[72:75], v[152:155]\n"
      "ds_read_b128 v[176:179], v214 offset:40960\n"
      "v_mfma_f32_16x16x32_bf16 v[156:159], v[184:187], v[76:79], v[156:159]\n"
      "s_waitcnt vmcnt(13)\n"
      "s_barrier\n"
      "s_setprio 1\n"
      "v_cvt_pk_bf16_f32 v160, v160, v164\n"
      "v_cvt_pk_bf16_f32 v161, v161, v165\n"
      "v_cvt_pk_bf16_f32 v162, v162, v166\n"
      "v_cvt_pk_bf16_f32 v163, v163, v167\n"
      "s_add_u32 s62, s37, 1622016\n"
      "buffer_store_dword v160, v247, s[68:71], s62 offen sc1\n"
      "s_add_u32 s62, s37, 1631232\n"
      "buffer_store_dword v161, v247, s[68:71], s62 offen sc1\n"
      "s_add_u32 s62, s37, 1640448\n"
      "buffer_store_dword v162, v247, s[68:71], s62 offen sc1\n"
      "s_add_u32 s62, s37, 1649664\n"
      "buffer_store_dword v163, v247, s[68:71], s62 offen sc1\n"
      "s_setprio 0\n"
      "s_waitcnt lgkmcnt(5)\n"
      "v_mfma_f32_16x16x32_bf16 v[152:155], v[188:191], v[80:83], v[152:155]\n"
      "ds_read_b128 v[180:183], v215 offset:40960\n"
      "v_mfma_f32_16x16x32_bf16 v[156:159], v[188:191], v[84:87], v[156:159]\n"
      "s_waitcnt lgkmcnt(5)\n"
      "v_mfma_f32_16x16x32_bf16 v[152:155], v[192:195], v[88:91], v[152:155]\n"
      "ds_read_b128 v[184:187], v214 offset:43008\n"
      "v_mfma_f32_16x16x32_bf16 v[156:159], v[192:195], v[92:95], v[156:159]\n"
      "s_waitcnt lgkmcnt(5)\n"
      "v_mfma_f32_16x16x32_bf16 v[152:155], v[196:199], v[96:99], v[152:155]\n"
      "ds_read_b128 v[188:191], v215 offset:43008\n"
      "v_mfma_f32_16x16x32_bf16 v[156:159], v[196:199], v[100:103], v[156:159]\n"
      "s_waitcnt lgkmcnt(5)\n"
      "v_mfma_f32_16x16x32_bf16 v[152:155], v[168:171], v[104:107], v[152:155]\n"
      "ds_read_b128 v[192:195], v214 offset:63488\n"
      "v_mfma_f32_16x16x32_bf16 v[156:159], v[168:171], v[108:111], v[156:159]\n"
      "s_waitcnt lgkmcnt(5)\n"
      "v_mfma_f32_16x16x32_bf16 v[152:155], v[172:175], v[112:115], v[152:155]\n"
      "ds_read_b128 v[196:199], v215 offset:63488\n"
      "v_mfma_f32_16x16x32_bf16 v[156:159], v[172:175], v[116:119], v[156:159]\n"
      "s_waitcnt lgkmcnt(5)\n"
      "v_mfma_f32_16x16x32_bf16 v[152:155], v[176:179], v[120:123], v[152:155]\n"
      "ds_read_b128 v[168:171], v216 offset:0\n"
      "v_mfma_f32_16x16x32_bf16 v[156:159], v[176:179], v[124:127], v[156:159]\n"
      "s_waitcnt lgkmcnt(5)\n"
      "v_mfma_f32_16x16x32_bf16 v[152:155], v[180:183], v[128:131], v[152:155]\n"
      "ds_read_b128 v[172:175], v217 offset:0\n"
      "v_mfma_f32_16x16x32_bf16 v[156:159], v[180:183], v[132:135], v[156:159]\n"
      "s_waitcnt lgkmcnt(5)\n"
      "v_mfma_f32_16x16x32_bf16 v[152:155], v[184:187], v[136:139], v[152:155]\n"
      "ds_read_b128 v[176:179], v216 offset:2048\n"
      "v_mfma_f32_16x16x32_bf16 v[156:159], v[184:187], v[140:143], v[156:159]\n"
      "s_waitcnt lgkmcnt(5)\n"
      "v_mfma_f32_16x16x32_bf16 v[152:155], v[188:191], v[144:147], v[152:155]\n"
      "ds_read_b128 v[180:183], v217 offset:2048\n"
      "v_mfma_f32_16x16x32_bf16 v[156:159], v[188:191], v[148:151], v[156:159]\n"
      "s_waitcnt lgkmcnt(5)\n"
      "v_mfma_f32_16x16x32_bf16 v[160:163], v[192:195], v[8:11], 0\n"
      "ds_read_b128 v[184:187], v216 offset:4096\n"
      "v_mfma_f32_16x16x32_bf16 v[164:167], v[192:195], v[12:15], 0\n"
      "s_waitcnt lgkmcnt(5)\n"
      "v_mfma_f32_16x16x32_bf16 v[160:163], v[196:199], v[16:19], v[160:163]\n"
      "ds_read_b128 v[188:191], v217 offset:4096\n"
      "v_mfma_f32_16x16x32_bf16 v[164:167], v[196:199], v[20:23], v[164:167]\n"
      "s_waitcnt lgkmcnt(5)\n"
      "v_mfma_f32_16x16x32_bf16 v[160:163], v[168:171], v[24:27], v[160:163]\n"
      "ds_read_b128 v[192:195], v216 offset:6144\n"
      "v_mfma_f32_16x16x32_bf16 v[164:167], v[168:171], v[28:31], v[164:167]\n"
      "s_waitcnt lgkmcnt(5)\n"
      "v_mfma_f32_16x16x32_bf16 v[160:163], v[172:175], v[32:35], v[160:163]\n"
      "ds_read_b128 v[196:199], v217 offset:6144\n"
      "v_mfma_f32_16x16x32_bf16 v[164:167], v[172:175], v[36:39], v[164:167]\n"
      "s_waitcnt lgkmcnt(5)\n"
      "v_mfma_f32_16x16x32_bf16 v[160:163], v[176:179], v[40:43], v[160:163]\n"
      "ds_read_b128 v[168:171], v216 offset:8192\n"
      "v_mfma_f32_16x16x32_bf16 v[164:167], v[176:179], v[44:47], v[164:167]\n"
      "s_waitcnt lgkmcnt(5)\n"
      "v_mfma_f32_16x16x32_bf16 v[160:163], v[180:183], v[48:51], v[160:163]\n"
      "ds_read_b128 v[172:175], v217 offset:8192\n"
      "v_mfma_f32_16x16x32_bf16 v[164:167], v[180:183], v[52:55], v[164:167]\n"
      "s_waitcnt lgkmcnt(5)\n"
      "v_mfma_f32_16x16x32_bf16 v[160:163], v[184:187], v[56:59], v[160:163]\n"
      "ds_read_b128 v[176:179], v216 offset:10240\n"
      "v_mfma_f32_16x16x32_bf16 v[164:167], v[184:187], v[60:63], v[164:167]\n"
      "s_waitcnt lgkmcnt(5)\n"
      "v_mfma_f32_16x16x32_bf16 v[160:163], v[188:191], v[64:67], v[160:163]\n"
      "ds_read_b128 v[180:183], v217 offset:10240\n"
      "v_mfma_f32_16x16x32_bf16 v[164:167], v[188:191], v[68:71], v[164:167]\n"
      "s_waitcnt lgkmcnt(5)\n"
      "v_mfma_f32_16x16x32_bf16 v[160:163], v[192:195], v[72:75], v[160:163]\n"
      "ds_read_b128 v[184:187], v216 offset:12288\n"
      "v_mfma_f32_16x16x32_bf16 v[164:167], v[192:195], v[76:79], v[164:167]\n"
      "s_waitcnt vmcnt(8)\n"
      "s_barrier\n"
      "s_setprio 1\n"
      "v_cvt_pk_bf16_f32 v152, v152, v156\n"
      "v_cvt_pk_bf16_f32 v153, v153, v157\n"
      "v_cvt_pk_bf16_f32 v154, v154, v158\n"
      "v_cvt_pk_bf16_f32 v155, v155, v159\n"
      "s_add_u32 s62, s37, 1916928\n"
      "buffer_store_dword v152, v247, s[68:71], s62 offen sc1\n"
      "s_add_u32 s62, s37, 1926144\n"
      "buffer_store_dword v153, v247, s[68:71], s62 offen sc1\n"
      "s_add_u32 s62, s37, 1935360\n"
      "buffer_store_dword v154, v247, s[68:71], s62 offen sc1\n"
      "s_add_u32 s62, s37, 1944576\n"
      "buffer_store_dword v155, v247, s[68:71], s62 offen sc1\n"
      "s_setprio 0\n"
      "s_waitcnt lgkmcnt(5)\n"
      "v_mfma_f32_16x16x32_bf16 v[160:163], v[196:199], v[80:83], v[160:163]\n"
      "ds_read_b128 v[188:191], v217 offset:12288\n"
      "v_mfma_f32_16x16x32_bf16 v[164:167], v[196:199], v[84:87], v[164:167]\n"
      "s_waitcnt lgkmcnt(5)\n"
      "v_mfma_f32_16x16x32_bf16 v[160:163], v[168:171], v[88:91], v[160:163]\n"
      "ds_read_b128 v[192:195], v216 offset:14336\n"
      "v_mfma_f32_16x16x32_bf16 v[164:167], v[168:171], v[92:95], v[164:167]\n"
      "s_waitcnt lgkmcnt(5)\n"
      "v_mfma_f32_16x16x32_bf16 v[160:163], v[172:175], v[96:99], v[160:163]\n"
      "ds_read_b128 v[196:199], v217 offset:14336\n"
      "v_mfma_f32_16x16x32_bf16 v[164:167], v[172:175], v[100:103], v[164:167]\n"
      "s_waitcnt lgkmcnt(5)\n"
      "v_mfma_f32_16x16x32_bf16 v[160:163], v[176:179], v[104:107], v[160:163]\n"
      "ds_read_b128 v[168:171], v240 offset:18432\n"
      "v_mfma_f32_16x16x32_bf16 v[164:167], v[176:179], v[108:111], v[164:167]\n"
      "s_waitcnt lgkmcnt(5)\n"
      "v_mfma_f32_16x16x32_bf16 v[160:163], v[180:183], v[112:115], v[160:163]\n"
      "ds_read_b128 v[172:175], v241 offset:18432\n"
      "v_mfma_f32_16x16x32_bf16 v[164:167], v[180:183], v[116:119], v[164:167]\n"
      "s_waitcnt lgkmcnt(5)\n"
      "v_mfma_f32_16x16x32_bf16 v[160:163], v[184:187], v[120:123], v[160:163]\n"
      "ds_read_b128 v[176:179], v240 offset:20480\n"
      "v_mfma_f32_16x16x32_bf16 v[164:167], v[184:187], v[124:127], v[164:167]\n"
      "s_waitcnt lgkmcnt(5)\n"
      "v_mfma_f32_16x16x32_bf16 v[160:163], v[188:191], v[128:131], v[160:163]\n"
      "ds_read_b128 v[180:183], v241 offset:20480\n"
      "v_mfma_f32_16x16x32_bf16 v[164:167], v[188:191], v[132:135], v[164:167]\n"
      "s_waitcnt lgkmcnt(5)\n"
      "v_mfma_f32_16x16x32_bf16 v[160:163], v[192:195], v[136:139], v[160:163]\n"
      "ds_read_b128 v[184:187], v240 offset:22528\n"
      "v_mfma_f32_16x16x32_bf16 v[164:167], v[192:195], v[140:143], v[164:167]\n"
      "s_waitcnt lgkmcnt(5)\n"
      "v_mfma_f32_16x16x32_bf16 v[160:163], v[196:199], v[144:147], v[160:163]\n"
      "ds_read_b128 v[188:191], v241 offset:22528\n"
      "v_mfma_f32_16x16x32_bf16 v[164:167], v[196:199], v[148:151], v[164:167]\n"
      "s_waitcnt lgkmcnt(5)\n"
      "v_mfma_f32_16x16x32_bf16 v[152:155], v[168:171], v[8:11], 0\n"
      "ds_read_b128 v[192:195], v240 offset:24576\n"
      "v_mfma_f32_16x16x32_bf16 v[156:159], v[168:171], v[12:15], 0\n"
      "s_waitcnt lgkmcnt(5)\n"
      "v_mfma_f32_16x16x32_bf16 v[152:155], v[172:175], v[16:19], v[152:155]\n"
      "ds_read_b128 v[196:199], v241 offset:24576\n"
      "v_mfma_f32_16x16x32_bf16 v[156:159], v[172:175], v[20:23], v[156:159]\n"
      "s_waitcnt lgkmcnt(5)\n"
      "v_mfma_f32_16x16x32_bf16 v[152:155], v[176:179], v[24:27], v[152:155]\n"
      "ds_read_b128 v[168:171], v240 offset:26624\n"
      "v_mfma_f32_16x16x32_bf16 v[156:159], v[176:179], v[28:31], v[156:159]\n"
      "s_waitcnt lgkmcnt(5)\n"
      "v_mfma_f32_16x16x32_bf16 v[152:155], v[180:183], v[32:35], v[152:155]\n"
      "ds_read_b128 v[172:175], v241 offset:26624\n"
      "v_mfma_f32_16x16x32_bf16 v[156:159], v[180:183], v[36:39], v[156:159]\n"
      "s_waitcnt lgkmcnt(5)\n"
      "v_mfma_f32_16x16x32_bf16 v[152:155], v[184:187], v[40:43], v[152:155]\n"
      "ds_read_b128 v[176:179], v240 offset:28672\n"
      "v_mfma_f32_16x16x32_bf16 v[156:159], v[184:187], v[44:47], v[156:159]\n"
      "s_waitcnt lgkmcnt(5)\n"
      "v_mfma_f32_16x16x32_bf16 v[152:155], v[188:191], v[48:51], v[152:155]\n"
      "ds_read_b128 v[180:183], v241 offset:28672\n"
      "v_mfma_f32_16x16x32_bf16 v[156:159], v[188:191], v[52:55], v[156:159]\n"
      "s_waitcnt lgkmcnt(5)\n"
      "v_mfma_f32_16x16x32_bf16 v[152:155], v[192:195], v[56:59], v[152:155]\n"
      "ds_read_b128 v[184:187], v240 offset:30720\n"
      "v_mfma_f32_16x16x32_bf16 v[156:159], v[192:195], v[60:63], v[156:159]\n"
      "s_waitcnt lgkmcnt(5)\n"
      "v_mfma_f32_16x16x32_bf16 v[152:155], v[196:199], v[64:67], v[152:155]\n"
      "ds_read_b128 v[188:191], v241 offset:30720\n"
      "v_mfma_f32_16x16x32_bf16 v[156:159], v[196:199], v[68:71], v[156:159]\n"
      "s_waitcnt lgkmcnt(5)\n"
      "v_mfma_f32_16x16x32_bf16 v[152:155], v[168:171], v[72:75], v[152:155]\n"
      "ds_read_b128 v[192:195], v240 offset:32768\n"
      "v_mfma_f32_16x16x32_bf16 v[156:159], v[168:171], v[76:79], v[156:159]\n"
      "s_barrier\n"
      "s_setprio 1\n"
      "v_cvt_pk_bf16_f32 v160, v160, v164\n"
      "v_cvt_pk_bf16_f32 v161, v161, v165\n"
      "v_cvt_pk_bf16_f32 v162, v162, v166\n"
      "v_cvt_pk_bf16_f32 v163, v163, v167\n"
      "s_add_u32 s62, s37, 2211840\n"
      "buffer_store_dword v160, v247, s[68:71], s62 offen sc1\n"
      "s_add_u32 s62, s37, 2221056\n"
      "buffer_store_dword v161, v247, s[68:71], s62 offen sc1\n"
      "s_add_u32 s62, s37, 2230272\n"
      "buffer_store_dword v162, v247, s[68:71], s62 offen sc1\n"
      "s_add_u32 s62, s37, 2239488\n"
      "buffer_store_dword v163, v247, s[68:71], s62 offen sc1\n"
      "s_setprio 0\n"
      "s_waitcnt lgkmcnt(5)\n"
      "v_mfma_f32_16x16x32_bf16 v[152:155], v[172:175], v[80:83], v[152:155]\n"
      "ds_read_b128 v[196:199], v241 offset:32768\n"
      "v_mfma_f32_16x16x32_bf16 v[156:159], v[172:175], v[84:87], v[156:159]\n"
      "s_waitcnt lgkmcnt(5)\n"
      "v_mfma_f32_16x16x32_bf16 v[152:155], v[176:179], v[88:91], v[152:155]\n"
      "ds_read_b128 v[168:171], v240 offset:34816\n"
      "v_mfma_f32_16x16x32_bf16 v[156:159], v[176:179], v[92:95], v[156:159]\n"
      "s_waitcnt lgkmcnt(5)\n"
      "v_mfma_f32_16x16x32_bf16 v[152:155], v[180:183], v[96:99], v[152:155]\n"
      "ds_read_b128 v[172:175], v241 offset:34816\n"
      "v_mfma_f32_16x16x32_bf16 v[156:159], v[180:183], v[100:103], v[156:159]\n"
      "s_waitcnt lgkmcnt(5)\n"
      "v_mfma_f32_16x16x32_bf16 v[152:155], v[184:187], v[104:107], v[152:155]\n"
      "v_mfma_f32_16x16x32_bf16 v[156:159], v[184:187], v[108:111], v[156:159]\n"
      "s_waitcnt lgkmcnt(4)\n"
      "v_mfma_f32_16x16x32_bf16 v[152:155], v[188:191], v[112:115], v[152:155]\n"
      "v_mfma_f32_16x16x32_bf16 v[156:159], v[188:191], v[116:119], v[156:159]\n"
      "s_waitcnt lgkmcnt(3)\n"
      "v_mfma_f32_16x16x32_bf16 v[152:155], v[192:195], v[120:123], v[152:155]\n"
      "v_mfma_f32_16x16x32_bf16 v[156:159], v[192:195], v[124:127], v[156:159]\n"
      "s_waitcnt lgkmcnt(2)\n"
      "v_mfma_f32_16x16x32_bf16 v[152:155], v[196:199], v[128:131], v[152:155]\n"
      "v_mfma_f32_16x16x32_bf16 v[156:159], v[196:199], v[132:135], v[156:159]\n"
      "s_waitcnt lgkmcnt(1)\n"
      "v_mfma_f32_16x16x32_bf16 v[152:155], v[168:171], v[136:139], v[152:155]\n"
      "v_mfma_f32_16x16x32_bf16 v[156:159], v[168:171], v[140:143], v[156:159]\n"
      "s_waitcnt lgkmcnt(0)\n"
      "v_mfma_f32_16x16x32_bf16 v[152:155], v[172:175], v[144:147], v[152:155]\n"
      "v_mfma_f32_16x16x32_bf16 v[156:159], v[172:175], v[148:151], v[156:159]\n"
      "s_nop 7\n"
      "v_cvt_pk_bf16_f32 v152, v152, v156\n"
      "v_cvt_pk_bf16_f32 v153, v153, v157\n"
      "v_cvt_pk_bf16_f32 v154, v154, v158\n"
      "v_cvt_pk_bf16_f32 v155, v155, v159\n"
      "s_add_u32 s62, s37, 2506752\n"
      "buffer_store_dword v152, v247, s[68:71], s62 offen sc1\n"
      "s_add_u32 s62, s37, 2515968\n"
      "buffer_store_dword v153, v247, s[68:71], s62 offen sc1\n"
      "s_add_u32 s62, s37, 2525184\n"
      "buffer_store_dword v154, v247, s[68:71], s62 offen sc1\n"
      "s_add_u32 s62, s37, 2534400\n"
      "buffer_store_dword v155, v247, s[68:71], s62 offen sc1\n"
      "s_waitcnt lgkmcnt(0)\n"
      "v_mov_b32 v8, 153600\n"
      "ds_read_b128 v[12:15], v8\n"
      "ds_read_b128 v[16:19], v8 offset:16\n"
      "s_waitcnt lgkmcnt(0)\n"
      "v_or3_b32 v12, v12, v13, v14\n"
      "v_or3_b32 v12, v12, v15, v16\n"
      "v_or3_b32 v12, v12, v17, v18\n"
      "v_or_b32 %[bad], v12, v19\n"
      "s_branch Lend_%=\n"
      "Lp3_%=:\n"
      "s_lshl_b32 s60, s48, 6\n"
      "v_add_u32 v4, s60, v1\n"
      "v_mul_u32_u24 v4, 36, v4\n"
      "v_add_u32 v4, s42, v4\n"
      "s_mul_i32 s60, s38, 144\n"
      "s_sub_u32 s60, s60, 1\n"
      "v_min_u32 v4, s60, v4\n"
      "v_lshlrev_b32 v4, 7, v4\n"
      "s_mov_b32 m0, 153632\n"
      "s_nop 0\n"
      "buffer_load_dword v4, s[16:19], s36 offen lds\n"
      "v_lshrrev_b32 v4, 1, v2\n"
      "v_xor_b32 v5, v3, v4\n"
      "v_lshlrev_b32 v5, 4, v5\n"
      "v_lshl_add_u32 v240, v2, 7, v5\n"
      "v_xor_b32 v241, 64, v240\n"
      "v_add_u32 v214, 0x10000, v240\n"
      "v_add_u32 v216, 0x20000, v240\n"
      "v_add_u32 v215, 0x10000, v241\n"
      "v_add_u32 v217, 0x20000, v241\n"
      "v_lshrrev_b32 v4, 3, v1\n"
      "v_and_b32 v5, 1, v4\n"
      "v_xor_b32 v5, v1, v5\n"
      "s_lshl_b32 s60, s49, 12\n"
      "s_add_u32 s60, s60, 110592\n"
      "v_lshlrev_b32 v5, 4, v5\n"
      "v_add_u32 v242, s60, v5\n"
      "s_lshl_b32 s60, s48, 3\n"
      "v_add_u32 v4, s60, v4\n"
      "v_lshrrev_b32 v5, 1, v4\n"
      "v_and_b32 v6, 7, v1\n"
      "v_xor_b32 v5, v5, v6\n"
      "v_and_b32 v5, 7, v5\n"
      "v_mul_u32_u24 v4, 1152, v4\n"
      "v_lshl_add_u32 v243, v5, 4, v4\n"
      "s_lshl_b32 s43, s48, 10\n"
      "v_lshrrev_b32 v4, 3, v2\n"
      "v_lshlrev_b32 v4, 12, v4\n"
      "v_lshl_add_u32 v4, v3, 8, v4\n"
      "v_and_b32 v5, 7, v2\n"
      "v_lshl_add_u32 v4, v5, 5, v4\n"
      "s_lshl_b32 s60, s48, 13\n"
      "s_add_u32 s60, s60, 112640\n"
      "v_add_u32 v4, s60, v4\n"
      "v_lshrrev_b32 v5, 2, v2\n"
      "v_and_b32 v5, 1, v5\n"
      "v_lshl_add_u32 v244, v5, 4, v4\n"
      "v_xor_b32 v245, 16, v244\n"
      "v_lshlrev_b32 v4, 3, v2\n"
      "v_add_u32 v246, s44, v4\n"
      "s_lshl_b32 s60, s49, 5\n"
      "s_add_u32 s60, s60, s40\n"
      "s_lshl_b32 s60, s60, 1\n"
      "v_mul_u32_u24 v4, 36864, v3\n"
      "v_lshl_add_u32 v4, v2, 2, v4\n"
      "v_add_u32 v247, s60, v4\n"
      "s_lshl_b32 s60, s39, 2\n"
      "s_add_u32 s45, s60, 153600\n"
      "v_mov_b32 v208, 0\n"
      "v_mov_b32 v209, 0\n"
      "s_mov_b32 s63, 3538944\n"
      "buffer_load_dwordx4 v[56:59], v210, s[20:23], s63 offen\n"
      "buffer_load_dwordx4 v[60:63], v211, s[20:23], s63 offen\n"
      "buffer_load_dwordx4 v[64:67], v212, s[20:23], s63 offen\n"
      "buffer_load_dwordx4 v[68:71], v213, s[20:23], s63 offen\n"
      "s_waitcnt vmcnt(13)\n"
      "v_lshlrev_b32 v168, 7, v3\n"
      "v_add_u32 v168, v246, v168\n"
      "ds_read_b64 v[172:173], v168\n"
      "ds_read_b64 v[174:175], v168 offset:640\n"
      "ds_read_b64 v[176:177], v246 offset:384\n"
      "ds_read_b64 v[178:179], v246 offset:1024\n"
      "v_mov_b32 v180, 0\n"
      "v_mov_b32 v181, 0\n"
      "v_mov_b32 v182, 0\n"
      "s_waitcnt lgkmcnt(0)\n"
      "v_lshlrev_b32 v168, 16, v174\n"
      "v_and_b32 v169, 0xffff0000, v174\n"
      "v_max3_f32 v180, |v168|, |v169|, v180\n"
      "v_rndne_f32 v170, v168\n"
      "v_rndne_f32 v171, v169\n"
      "v_sub_f32 v168, v168, v170\n"
      "v_sub_f32 v169, v169, v171\n"
      "v_max3_f32 v181, |v168|, |v169|, v181\n"
      "v_lshlrev_b32 v168, 16, v172\n"
      "v_and_b32 v169, 0xffff0000, v172\n"
      "v_max3_f32 v182, |v168|, |v169|, v182\n"
      "v_lshlrev_b32 v168, 16, v175\n"
      "v_and_b32 v169, 0xffff0000, v175\n"
      "v_max3_f32 v180, |v168|, |v169|, v180\n"
      "v_rndne_f32 v170, v168\n"
      "v_rndne_f32 v171, v169\n"
      "v_sub_f32 v168, v168, v170\n"
      "v_sub_f32 v169, v169, v171\n"
      "v_max3_f32 v181, |v168|, |v169|, v181\n"
      "v_lshlrev_b32 v168, 16, v173\n"
      "v_and_b32 v169, 0xffff0000, v173\n"
      "v_max3_f32 v182, |v168|, |v169|, v182\n"
      "v_lshlrev_b32 v168, 16, v178\n"
      "v_and_b32 v169, 0xffff0000, v178\n"
      "v_max3_f32 v180, |v168|, |v169|, v180\n"
      "v_rndne_f32 v170, v168\n"
      "v_rndne_f32 v171, v169\n"
      "v_sub_f32 v168, v168, v170\n"
      "v_sub_f32 v169, v169, v171\n"
      "v_max3_f32 v181, |v168|, |v169|, v181\n"
      "v_lshlrev_b32 v168, 16, v176\n"
      "v_and_b32 v169, 0xffff0000, v176\n"
      "v_max3_f32 v182, |v168|, |v169|, v182\n"
      "v_lshlrev_b32 v168, 16, v179\n"
      "v_and_b32 v169, 0xffff0000, v179\n"
      "v_max3_f32 v180, |v168|, |v169|, v180\n"
      "v_rndne_f32 v170, v168\n"
      "v_rndne_f32 v171, v169\n"
      "v_sub_f32 v168, v168, v170\n"
      "v_sub_f32 v169, v169, v171\n"
      "v_max3_f32 v181, |v168|, |v169|, v181\n"
      "v_lshlrev_b32 v168, 16, v177\n"
      "v_and_b32 v169, 0xffff0000, v177\n"
      "v_max3_f32 v182, |v168|, |v169|, v182\n"
      "v_cmp_lt_f32 vcc, 0, v181\n"
      "s_mov_b64 s[66:67], vcc\n"
      "v_cmp_lt_f32 vcc, 0x43000000, v180\n"
      "s_or_b64 s[66:67], s[66:67], vcc\n"
      "v_cmp_le_f32 vcc, 0x71800000, v182\n"
      "s_or_b64 s[66:67], s[66:67], vcc\n"
      "ds_read_b64 v[184:185], v246 offset:0\n"
      "ds_read_b64 v[186:187], v246 offset:640\n"
      "s_waitcnt lgkmcnt(0)\n"
      "v_lshlrev_b32 v200, 16, v184\n"
      "v_and_b32 v201, 0xffff0000, v184\n"
      "v_lshlrev_b32 v204, 16, v186\n"
      "v_and_b32 v205, 0xffff0000, v186\n"
      "v_lshlrev_b32 v202, 16, v185\n"
      "v_and_b32 v203, 0xffff0000, v185\n"
      "v_lshlrev_b32 v206, 16, v187\n"
      "v_and_b32 v207, 0xffff0000, v187\n"
      "v_mul_f32_e64 v204, -v204, v200\n"
      "v_mul_f32_e64 v205, -v205, v201\n"
      "v_mul_f32 v200, 0x4b800000, v200\n"
      "v_mul_f32 v201, 0x4b800000, v201\n"
      "v_mul_f32_e64 v206, -v206, v202\n"
      "v_mul_f32_e64 v207, -v207, v203\n"
      "v_mul_f32 v202, 0x4b800000, v202\n"
      "v_mul_f32 v203, 0x4b800000, v203\n"
      "s_barrier\n"
      "ds_read_b128 v[8:11], v242 offset:0\n"
      "ds_read_b128 v[12:15], v242 offset:1024\n"
      "ds_read_b128 v[16:19], v242 offset:2048\n"
      "ds_read_b128 v[20:23], v242 offset:3072\n"
      "s_setprio 1\n"
      "s_waitcnt vmcnt(11)\n"
      "v_or3_b32 v208, v120, v121, v208\n"
      "v_or3_b32 v208, v122, v123, v208\n"
      "v_or3_b32 v209, v124, v125, v209\n"
      "v_or3_b32 v209, v126, v127, v209\n"
      "v_fma_mix_f32 v120, v120, v200, v204 op_sel_hi:[1,0,0]\n"
      "v_fma_mix_f32 v121, v121, v201, v205 op_sel_hi:[1,0,0]\n"
      "v_fma_mix_f32 v122, v122, v202, v206 op_sel_hi:[1,0,0]\n"
      "v_fma_mix_f32 v123, v123, v203, v207 op_sel_hi:[1,0,0]\n"
      "v_fma_mix_f32 v124, v124, v200, v204 op_sel_hi:[1,0,0]\n"
      "v_fma_mix_f32 v125, v125, v201, v205 op_sel_hi:[1,0,0]\n"
      "v_fma_mix_f32 v126, v126, v202, v206 op_sel_hi:[1,0,0]\n"
      "v_fma_mix_f32 v127, v127, v203, v207 op_sel_hi:[1,0,0]\n"
      "v_cvt_pk_bf16_f32 v184, v120, v124\n"
      "v_cvt_pk_bf16_f32 v188, v121, v125\n"
      "v_cvt_pk_bf16_f32 v192, v122, v126\n"
      "v_cvt_pk_bf16_f32 v196, v123, v127\n"
      "s_waitcnt vmcnt(9)\n"
      "v_or3_b32 v208, v128, v129, v208\n"
      "v_or3_b32 v208, v130, v131, v208\n"
      "v_or3_b32 v209, v132, v133, v209\n"
      "v_or3_b32 v209, v134, v135, v209\n"
      "v_fma_mix_f32 v128, v128, v200, v204 op_sel_hi:[1,0,0]\n"
      "v_fma_mix_f32 v129, v129, v201, v205 op_sel_hi:[1,0,0]\n"
      "v_fma_mix_f32 v130, v130, v202, v206 op_sel_hi:[1,0,0]\n"
      "v_fma_mix_f32 v131, v131, v203, v207 op_sel_hi:[1,0,0]\n"
      "v_fma_mix_f32 v132, v132, v200, v204 op_sel_hi:[1,0,0]\n"
      "v_fma_mix_f32 v133, v133, v201, v205 op_sel_hi:[1,0,0]\n"
      "v_fma_mix_f32 v134, v134, v202, v206 op_sel_hi:[1,0,0]\n"
      "v_fma_mix_f32 v135, v135, v203, v207 op_sel_hi:[1,0,0]\n"
      "v_cvt_pk_bf16_f32 v185, v128, v132\n"
      "v_cvt_pk_bf16_f32 v189, v129, v133\n"
      "v_cvt_pk_bf16_f32 v193, v130, v134\n"
      "v_cvt_pk_bf16_f32 v197, v131, v135\n"
      "s_waitcnt vmcnt(5)\n"
      "v_or3_b32 v208, v136, v140, v208\n"
      "v_or3_b32 v209, v144, v148, v209\n"
      "v_fma_mix_f32 v136, v136, v200, v204 op_sel_hi:[1,0,0]\n"
      "v_fma_mix_f32 v140, v140, v200, v204 op_sel_hi:[1,0,0]\n"
      "v_fma_mix_f32 v144, v144, v200, v204 op_sel_hi:[1,0,0]\n"
      "v_fma_mix_f32 v148, v148, v200, v204 op_sel_hi:[1,0,0]\n"
      "v_cvt_pk_bf16_f32 v186, v136, v140\n"
      "v_cvt_pk_bf16_f32 v187, v144, v148\n"
      "ds_write_b128 v244, v[184:187] offset:16384\n"
      "v_or3_b32 v208, v137, v141, v208\n"
      "v_or3_b32 v209, v145, v149, v209\n"
      "v_fma_mix_f32 v137, v137, v201, v205 op_sel_hi:[1,0,0]\n"
      "v_fma_mix_f32 v141, v141, v201, v205 op_sel_hi:[1,0,0]\n"
      "v_fma_mix_f32 v145, v145, v201, v205 op_sel_hi:[1,0,0]\n"
      "v_fma_mix_f32 v149, v149, v201, v205 op_sel_hi:[1,0,0]\n"
      "v_cvt_pk_bf16_f32 v190, v137, v141\n"
      "v_cvt_pk_bf16_f32 v191, v145, v149\n"
      "ds_write_b128 v244, v[188:191] offset:17408\n"
      "v_or3_b32 v208, v138, v142, v208\n"
      "v_or3_b32 v209, v146, v150, v209\n"
      "v_fma_mix_f32 v138, v138, v202, v206 op_sel_hi:[1,0,0]\n"
      "v_fma_mix_f32 v142, v142, v202, v206 op_sel_hi:[1,0,0]\n"
      "v_fma_mix_f32 v146, v146, v202, v206 op_sel_hi:[1,0,0]\n"
      "v_fma_mix_f32 v150, v150, v202, v206 op_sel_hi:[1,0,0]\n"
      "v_cvt_pk_bf16_f32 v194, v138, v142\n"
      "v_cvt_pk_bf16_f32 v195, v146, v150\n"
      "ds_write_b128 v245, v[192:195] offset:16384\n"
      "v_or3_b32 v208, v139, v143, v208\n"
      "v_or3_b32 v209, v147, v151, v209\n"
      "v_fma_mix_f32 v139, v139, v203, v207 op_sel_hi:[1,0,0]\n"
      "v_fma_mix_f32 v143, v143, v203, v207 op_sel_hi:[1,0,0]\n"
      "v_fma_mix_f32 v147, v147, v203, v207 op_sel_hi:[1,0,0]\n"
      "v_fma_mix_f32 v151, v151, v203, v207 op_sel_hi:[1,0,0]\n"
      "v_cvt_pk_bf16_f32 v198, v139, v143\n"
      "v_cvt_pk_bf16_f32 v199, v147, v151\n"
      "ds_write_b128 v245, v[196:199] offset:17408\n"
      "s_setprio 0\n"
      "s_mov_b32 s63, 3612672\n"
      "buffer_load_dwordx4 v[72:75], v210, s[20:23], s63 offen\n"
      "buffer_load_dwordx4 v[76:79], v211, s[20:23], s63 offen\n"
      "buffer_load_dwordx4 v[80:83], v212, s[20:23], s63 offen\n"
      "buffer_load_dwordx4 v[84:87], v213, s[20:23], s63 offen\n"
      "s_waitcnt lgkmcnt(0)\n"
      "s_barrier\n"
      "ds_read_b128 v[24:27], v242 offset:16384\n"
      "ds_read_b128 v[28:31], v242 offset:17408\n"
      "ds_read_b128 v[32:35], v242 offset:18432\n"
      "ds_read_b128 v[36:39], v242 offset:19456\n"
      "s_mov_b32 s63, 5898240\n"
      "buffer_load_dwordx4 v[88:91], v210, s[20:23], s63 offen\n"
      "buffer_load_dwordx4 v[92:95], v211, s[20:23], s63 offen\n"
      "buffer_load_dwordx4 v[96:99], v212, s[20:23], s63 offen\n"
      "buffer_load_dwordx4 v[100:103], v213, s[20:23], s63 offen\n"
      "ds_read_b64 v[184:185], v246 offset:128\n"
      "ds_read_b64 v[186:187], v246 offset:768\n"
      "s_waitcnt lgkmcnt(0)\n"
      "v_lshlrev_b32 v200, 16, v184\n"
      "v_and_b32 v201, 0xffff0000, v184\n"
      "v_lshlrev_b32 v204, 16, v186\n"
      "v_and_b32 v205, 0xffff0000, v186\n"
      "v_lshlrev_b32 v202, 16, v185\n"
      "v_and_b32 v203, 0xffff0000, v185\n"
      "v_lshlrev_b32 v206, 16, v187\n"
      "v_and_b32 v207, 0xffff0000, v187\n"
      "v_mul_f32_e64 v204, -v204, v200\n"
      "v_mul_f32_e64 v205, -v205, v201\n"
      "v_mul_f32 v200, 0x4b800000, v200\n"
      "v_mul_f32 v201, 0x4b800000, v201\n"
      "v_mul_f32_e64 v206, -v206, v202\n"
      "v_mul_f32_e64 v207, -v207, v203\n"
      "v_mul_f32 v202, 0x4b800000, v202\n"
      "v_mul_f32 v203, 0x4b800000, v203\n"
      "s_waitcnt vmcnt(10)\n"
      "v_or3_b32 v208, v56, v57, v208\n"
      "v_or3_b32 v208, v58, v59, v208\n"
      "v_or3_b32 v209, v60, v61, v209\n"
      "v_or3_b32 v209, v62, v63, v209\n"
      "v_fma_mix_f32 v56, v56, v200, v204 op_sel_hi:[1,0,0]\n"
      "v_fma_mix_f32 v57, v57, v201, v205 op_sel_hi:[1,0,0]\n"
      "v_fma_mix_f32 v58, v58, v202, v206 op_sel_hi:[1,0,0]\n"
      "v_fma_mix_f32 v59, v59, v203, v207 op_sel_hi:[1,0,0]\n"
      "v_fma_mix_f32 v60, v60, v200, v204 op_sel_hi:[1,0,0]\n"
      "v_fma_mix_f32 v61, v61, v201, v205 op_sel_hi:[1,0,0]\n"
      "v_fma_mix_f32 v62, v62, v202, v206 op_sel_hi:[1,0,0]\n"
      "v_fma_mix_f32 v63, v63, v203, v207 op_sel_hi:[1,0,0]\n"
      "v_cvt_pk_bf16_f32 v184, v56, v60\n"
      "v_cvt_pk_bf16_f32 v188, v57, v61\n"
      "v_cvt_pk_bf16_f32 v192, v58, v62\n"
      "v_cvt_pk_bf16_f32 v196, v59, v63\n"
      "s_waitcnt vmcnt(8)\n"
      "v_or3_b32 v208, v64, v65, v208\n"
      "v_or3_b32 v208, v66, v67, v208\n"
      "v_or3_b32 v209, v68, v69, v209\n"
      "v_or3_b32 v209, v70, v71, v209\n"
      "v_fma_mix_f32 v64, v64, v200, v204 op_sel_hi:[1,0,0]\n"
      "v_fma_mix_f32 v65, v65, v201, v205 op_sel_hi:[1,0,0]\n"
      "v_fma_mix_f32 v66, v66, v202, v206 op_sel_hi:[1,0,0]\n"
      "v_fma_mix_f32 v67, v67, v203, v207 op_sel_hi:[1,0,0]\n"
      "v_fma_mix_f32 v68, v68, v200, v204 op_sel_hi:[1,0,0]\n"
      "v_fma_mix_f32 v69, v69, v201, v205 op_sel_hi:[1,0,0]\n"
      "v_fma_mix_f32 v70, v70, v202, v206 op_sel_hi:[1,0,0]\n"
      "v_fma_mix_f32 v71, v71, v203, v207 op_sel_hi:[1,0,0]\n"
      "v_cvt_pk_bf16_f32 v185, v64, v68\n"
      "v_cvt_pk_bf16_f32 v189, v65, v69\n"
      "v_cvt_pk_bf16_f32 v193, v66, v70\n"
      "v_cvt_pk_bf16_f32 v197, v67, v71\n"
      "s_barrier\n"
      "ds_read_b128 v[40:43], v242 offset:0\n"
      "ds_read_b128 v[44:47], v242 offset:1024\n"
      "ds_read_b128 v[48:51], v242 offset:2048\n"
      "ds_read_b128 v[52:55], v242 offset:3072\n"
      "s_setprio 1\n"
      "s_waitcnt vmcnt(4)\n"
      "v_or3_b32 v208, v72, v76, v208\n"
      "v_or3_b32 v209, v80, v84, v209\n"
      "v_fma_mix_f32 v72, v72, v200, v204 op_sel_hi:[1,0,0]\n"
      "v_fma_mix_f32 v76, v76, v200, v204 op_sel_hi:[1,0,0]\n"
      "v_fma_mix_f32 v80, v80, v200, v204 op_sel_hi:[1,0,0]\n"
      "v_fma_mix_f32 v84, v84, v200, v204 op_sel_hi:[1,0,0]\n"
      "v_cvt_pk_bf16_f32 v186, v72, v76\n"
      "v_cvt_pk_bf16_f32 v187, v80, v84\n"
      "ds_write_b128 v244, v[184:187] offset:16384\n"
      "v_or3_b32 v208, v73, v77, v208\n"
      "v_or3_b32 v209, v81, v85, v209\n"
      "v_fma_mix_f32 v73, v73, v201, v205 op_sel_hi:[1,0,0]\n"
      "v_fma_mix_f32 v77, v77, v201, v205 op_sel_hi:[1,0,0]\n"
      "v_fma_mix_f32 v81, v81, v201, v205 op_sel_hi:[1,0,0]\n"
      "v_fma_mix_f32 v85, v85, v201, v205 op_sel_hi:[1,0,0]\n"
      "v_cvt_pk_bf16_f32 v190, v73, v77\n"
      "v_cvt_pk_bf16_f32 v191, v81, v85\n"
      "ds_write_b128 v244, v[188:191] offset:17408\n"
      "v_or3_b32 v208, v74, v78, v208\n"
      "v_or3_b32 v209, v82, v86, v209\n"
      "v_fma_mix_f32 v74, v74, v202, v206 op_sel_hi:[1,0,0]\n"
      "v_fma_mix_f32 v78, v78, v202, v206 op_sel_hi:[1,0,0]\n"
      "v_fma_mix_f32 v82, v82, v202, v206 op_sel_hi:[1,0,0]\n"
      "v_fma_mix_f32 v86, v86, v202, v206 op_sel_hi:[1,0,0]\n"
      "v_cvt_pk_bf16_f32 v194, v74, v78\n"
      "v_cvt_pk_bf16_f32 v195, v82, v86\n"
      "ds_write_b128 v245, v[192:195] offset:16384\n"
      "v_or3_b32 v208, v75, v79, v208\n"
      "v_or3_b32 v209, v83, v87, v209\n"
      "v_fma_mix_f32 v75, v75, v203, v207 op_sel_hi:[1,0,0]\n"
      "v_fma_mix_f32 v79, v79, v203, v207 op_sel_hi:[1,0,0]\n"
      "v_fma_mix_f32 v83, v83, v203, v207 op_sel_hi:[1,0,0]\n"
      "v_fma_mix_f32 v87, v87, v203, v207 op_sel_hi:[1,0,0]\n"
      "v_cvt_pk_bf16_f32 v198, v75, v79\n"
      "v_cvt_pk_bf16_f32 v199, v83, v87\n"
      "ds_write_b128 v245, v[196:199] offset:17408\n"
      "s_setprio 0\n"
      "s_mov_b32 s63, 5971968\n"
      "buffer_load_dwordx4 v[104:107], v210, s[20:23], s63 offen\n"
      "buffer_load_dwordx4 v[108:111], v211, s[20:23], s63 offen\n"
      "buffer_load_dwordx4 v[112:115], v212, s[20:23], s63 offen\n"
      "buffer_load_dwordx4 v[116:119], v213, s[20:23], s63 offen\n"
      "s_waitcnt lgkmcnt(0)\n"
      "s_barrier\n"
      "ds_read_b128 v[56:59], v242 offset:16384\n"
      "ds_read_b128 v[60:63], v242 offset:17408\n"
      "ds_read_b128 v[64:67], v242 offset:18432\n"
      "ds_read_b128 v[68:71], v242 offset:19456\n"
      "s_mov_b32 s63, 8257536\n"
      "buffer_load_dwordx4 v[120:123], v210, s[20:23], s63 offen\n"
      "buffer_load_dwordx4 v[124:127], v211, s[20:23], s63 offen\n"
      "buffer_load_dwordx4 v[128:131], v212, s[20:23], s63 offen\n"
      "buffer_load_dwordx4 v[132:135], v213, s[20:23], s63 offen\n"
      "ds_read_b64 v[184:185], v246 offset:256\n"
      "ds_read_b64 v[186:187], v246 offset:896\n"
      "s_waitcnt lgkmcnt(0)\n"
      "v_lshlrev_b32 v200, 16, v184\n"
      "v_and_b32 v201, 0xffff0000, v184\n"
      "v_lshlrev_b32 v204, 16, v186\n"
      "v_and_b32 v205, 0xffff0000, v186\n"
      "v_lshlrev_b32 v202, 16, v185\n"
      "v_and_b32 v203, 0xffff0000, v185\n"
      "v_lshlrev_b32 v206, 16, v187\n"
      "v_and_b32 v207, 0xffff0000, v187\n"
      "v_mul_f32_e64 v204, -v204, v200\n"
      "v_mul_f32_e64 v205, -v205, v201\n"
      "v_mul_f32 v200, 0x4b800000, v200\n"
      "v_mul_f32 v201, 0x4b800000, v201\n"
      "v_mul_f32_e64 v206, -v206, v202\n"
      "v_mul_f32_e64 v207, -v207, v203\n"
      "v_mul_f32 v202, 0x4b800000, v202\n"
      "v_mul_f32 v203, 0x4b800000, v203\n"
      "s_waitcnt vmcnt(10)\n"
      "v_or3_b32 v208, v88, v89, v208\n"
      "v_or3_b32 v208, v90, v91, v208\n"
      "v_or3_b32 v209, v92, v93, v209\n"
      "v_or3_b32 v209, v94, v95, v209\n"
      "v_fma_mix_f32 v88, v88, v200, v204 op_sel_hi:[1,0,0]\n"
      "v_fma_mix_f32 v89, v89, v201, v205 op_sel_hi:[1,0,0]\n"
      "v_fma_mix_f32 v90, v90, v202, v206 op_sel_hi:[1,0,0]\n"
      "v_fma_mix_f32 v91, v91, v203, v207 op_sel_hi:[1,0,0]\n"
      "v_fma_mix_f32 v92, v92, v200, v204 op_sel_hi:[1,0,0]\n"
      "v_fma_mix_f32 v93, v93, v201, v205 op_sel_hi:[1,0,0]\n"
      "v_fma_mix_f32 v94, v94, v202, v206 op_sel_hi:[1,0,0]\n"
      "v_fma_mix_f32 v95, v95, v203, v207 op_sel_hi:[1,0,0]\n"
      "v_cvt_pk_bf16_f32 v184, v88, v92\n"
      "v_cvt_pk_bf16_f32 v188, v89, v93\n"
      "v_cvt_pk_bf16_f32 v192, v90, v94\n"
      "v_cvt_pk_bf16_f32 v196, v91, v95\n"
      "s_waitcnt vmcnt(8)\n"
      "v_or3_b32 v208, v96, v97, v208\n"
      "v_or3_b32 v208, v98, v99, v208\n"
      "v_or3_b32 v209, v100, v101, v209\n"
      "v_or3_b32 v209, v102, v103, v209\n"
      "v_fma_mix_f32 v96, v96, v200, v204 op_sel_hi:[1,0,0]\n"
      "v_fma_mix_f32 v97, v97, v201, v205 op_sel_hi:[1,0,0]\n"
      "v_fma_mix_f32 v98, v98, v202, v206 op_sel_hi:[1,0,0]\n"
      "v_fma_mix_f32 v99, v99, v203, v207 op_sel_hi:[1,0,0]\n"
      "v_fma_mix_f32 v100, v100, v200, v204 op_sel_hi:[1,0,0]\n"
      "v_fma_mix_f32 v101, v101, v201, v205 op_sel_hi:[1,0,0]\n"
      "v_fma_mix_f32 v102, v102, v202, v206 op_sel_hi:[1,0,0]\n"
      "v_fma_mix_f32 v103, v103, v203, v207 op_sel_hi:[1,0,0]\n"
      "v_cvt_pk_bf16_f32 v185, v96, v100\n"
      "v_cvt_pk_bf16_f32 v189, v97, v101\n"
      "v_cvt_pk_bf16_f32 v193, v98, v102\n"
      "v_cvt_pk_bf16_f32 v197, v99, v103\n"
      "s_barrier\n"
      "ds_read_b128 v[72:75], v242 offset:0\n"
      "ds_read_b128 v[76:79], v242 offset:1024\n"
      "ds_read_b128 v[80:83], v242 offset:2048\n"
      "ds_read_b128 v[84:87], v242 offset:3072\n"
      "s_setprio 1\n"
      "s_waitcnt vmcnt(4)\n"
      "v_or3_b32 v208, v104, v108, v208\n"
      "v_or3_b32 v209, v112, v116, v209\n"
      "v_fma_mix_f32 v104, v104, v200, v204 op_sel_hi:[1,0,0]\n"
      "v_fma_mix_f32 v108, v108, v200, v204 op_sel_hi:[1,0,0]\n"
      "v_fma_mix_f32 v112, v112, v200, v204 op_sel_hi:[1,0,0]\n"
      "v_fma_mix_f32 v116, v116, v200, v204 op_sel_hi:[1,0,0]\n"
      "v_cvt_pk_bf16_f32 v186, v104, v108\n"
      "v_cvt_pk_bf16_f32 v187, v112, v116\n"
      "ds_write_b128 v244, v[184:187] offset:16384\n"
      "v_or3_b32 v208, v105, v109, v208\n"
      "v_or3_b32 v209, v113, v117, v209\n"
      "v_fma_mix_f32 v105, v105, v201, v205 op_sel_hi:[1,0,0]\n"
      "v_fma_mix_f32 v109, v109, v201, v205 op_sel_hi:[1,0,0]\n"
      "v_fma_mix_f32 v113, v113, v201, v205 op_sel_hi:[1,0,0]\n"
      "v_fma_mix_f32 v117, v117, v201, v205 op_sel_hi:[1,0,0]\n"
      "v_cvt_pk_bf16_f32 v190, v105, v109\n"
      "v_cvt_pk_bf16_f32 v191, v113, v117\n"
      "ds_write_b128 v244, v[188:191] offset:17408\n"
      "v_or3_b32 v208, v106, v110, v208\n"
      "v_or3_b32 v209, v114, v118, v209\n"
      "v_fma_mix_f32 v106, v106, v202, v206 op_sel_hi:[1,0,0]\n"
      "v_fma_mix_f32 v110, v110, v202, v206 op_sel_hi:[1,0,0]\n"
      "v_fma_mix_f32 v114, v114, v202, v206 op_sel_hi:[1,0,0]\n"
      "v_fma_mix_f32 v118, v118, v202, v206 op_sel_hi:[1,0,0]\n"
      "v_cvt_pk_bf16_f32 v194, v106, v110\n"
      "v_cvt_pk_bf16_f32 v195, v114, v118\n"
      "ds_write_b128 v245, v[192:195] offset:16384\n"
      "v_or3_b32 v208, v107, v111, v208\n"
      "v_or3_b32 v209, v115, v119, v209\n"
      "v_fma_mix_f32 v107, v107, v203, v207 op_sel_hi:[1,0,0]\n"
      "v_fma_mix_f32 v111, v111, v203, v207 op_sel_hi:[1,0,0]\n"
      "v_fma_mix_f32 v115, v115, v203, v207 op_sel_hi:[1,0,0]\n"
      "v_fma_mix_f32 v119, v119, v203, v207 op_sel_hi:[1,0,0]\n"
      "v_cvt_pk_bf16_f32 v198, v107, v111\n"
      "v_cvt_pk_bf16_f32 v199, v115, v119\n"
      "ds_write_b128 v245, v[196:199] offset:17408\n"
      "s_setprio 0\n"
      "s_mov_b32 s63, 8331264\n"
      "buffer_load_dwordx4 v[136:139], v210, s[20:23], s63 offen\n"
      "buffer_load_dwordx4 v[140:143], v211, s[20:23], s63 offen\n"
      "buffer_load_dwordx4 v[144:147], v212, s[20:23], s63 offen\n"
      "buffer_load_dwordx4 v[148:151], v213, s[20:23], s63 offen\n"
      "s_waitcnt lgkmcnt(0)\n"
      "s_barrier\n"
      "ds_read_b128 v[88:91], v242 offset:16384\n"
      "ds_read_b128 v[92:95], v242 offset:17408\n"
      "ds_read_b128 v[96:99], v242 offset:18432\n"
      "ds_read_b128 v[100:103], v242 offset:19456\n"
      "s_add_u32 m0, s43, 14336\n"
      "s_add_u32 s61, s36, 896\n"
      "buffer_load_dwordx4 v243, s[16:19], s61 offen lds\n"
      "s_add_u32 m0, s43, 16384\n"
      "s_add_u32 s61, s36, 1024\n"
      "buffer_load_dwordx4 v243, s[16:19], s61 offen lds\n"
      "ds_read_b64 v[184:185], v246 offset:384\n"
      "ds_read_b64 v[186:187], v246 offset:1024\n"
      "s_waitcnt lgkmcnt(0)\n"
      "v_lshlrev_b32 v200, 16, v184\n"
      "v_and_b32 v201, 0xffff0000, v184\n"
      "v_lshlrev_b32 v204, 16, v186\n"
      "v_and_b32 v205, 0xffff0000, v186\n"
      "v_lshlrev_b32 v202, 16, v185\n"
      "v_and_b32 v203, 0xffff0000, v185\n"
      "v_lshlrev_b32 v206, 16, v187\n"
      "v_and_b32 v207, 0xffff0000, v187\n"
      "v_mul_f32_e64 v204, -v204, v200\n"
      "v_mul_f32_e64 v205, -v205, v201\n"
      "v_mul_f32 v200, 0x4b800000, v200\n"
      "v_mul_f32 v201, 0x4b800000, v201\n"
      "v_mul_f32_e64 v206, -v206, v202\n"
      "v_mul_f32_e64 v207, -v207, v203\n"
      "v_mul_f32 v202, 0x4b800000, v202\n"
      "v_mul_f32 v203, 0x4b800000, v203\n"
      "s_waitcnt vmcnt(8)\n"
      "v_or3_b32 v208, v120, v121, v208\n"
      "v_or3_b32 v208, v122, v123, v208\n"
      "v_or3_b32 v209, v124, v125, v209\n"
      "v_or3_b32 v209, v126, v127, v209\n"
      "v_fma_mix_f32 v120, v120, v200, v204 op_sel_hi:[1,0,0]\n"
      "v_fma_mix_f32 v121, v121, v201, v205 op_sel_hi:[1,0,0]\n"
      "v_fma_mix_f32 v122, v122, v202, v206 op_sel_hi:[1,0,0]\n"
      "v_fma_mix_f32 v123, v123, v203, v207 op_sel_hi:[1,0,0]\n"
      "v_fma_mix_f32 v124, v124, v200, v204 op_sel_hi:[1,0,0]\n"
      "v_fma_mix_f32 v125, v125, v201, v205 op_sel_hi:[1,0,0]\n"
      "v_fma_mix_f32 v126, v126, v202, v206 op_sel_hi:[1,0,0]\n"
      "v_fma_mix_f32 v127, v127, v203, v207 op_sel_hi:[1,0,0]\n"
      "v_cvt_pk_bf16_f32 v184, v120, v124\n"
      "v_cvt_pk_bf16_f32 v188, v121, v125\n"
      "v_cvt_pk_bf16_f32 v192, v122, v126\n"
      "v_cvt_pk_bf16_f32 v196, v123, v127\n"
      "s_waitcnt vmcnt(6)\n"
      "v_or3_b32 v208, v128, v129, v208\n"
      "v_or3_b32 v208, v130, v131, v208\n"
      "v_or3_b32 v209, v132, v133, v209\n"
      "v_or3_b32 v209, v134, v135, v209\n"
      "v_fma_mix_f32 v128, v128, v200, v204 op_sel_hi:[1,0,0]\n"
      "v_fma_mix_f32 v129, v129, v201, v205 op_sel_hi:[1,0,0]\n"
      "v_fma_mix_f32 v130, v130, v202, v206 op_sel_hi:[1,0,0]\n"
      "v_fma_mix_f32 v131, v131, v203, v207 op_sel_hi:[1,0,0]\n"
      "v_fma_mix_f32 v132, v132, v200, v204 op_sel_hi:[1,0,0]\n"
      "v_fma_mix_f32 v133, v133, v201, v205 op_sel_hi:[1,0,0]\n"
      "v_fma_mix_f32 v134, v134, v202, v206 op_sel_hi:[1,0,0]\n"
      "v_fma_mix_f32 v135, v135, v203, v207 op_sel_hi:[1,0,0]\n"
      "v_cvt_pk_bf16_f32 v185, v128, v132\n"
      "v_cvt_pk_bf16_f32 v189, v129, v133\n"
      "v_cvt_pk_bf16_f32 v193, v130, v134\n"
      "v_cvt_pk_bf16_f32 v197, v131, v135\n"
      "s_barrier\n"
      "ds_read_b128 v[104:107], v242 offset:0\n"
      "ds_read_b128 v[108:111], v242 offset:1024\n"
      "ds_read_b128 v[112:115], v242 offset:2048\n"
      "ds_read_b128 v[116:119], v242 offset:3072\n"
      "s_setprio 1\n"
      "s_waitcnt vmcnt(2)\n"
      "v_or3_b32 v208, v136, v140, v208\n"
      "v_or3_b32 v209, v144, v148, v209\n"
      "v_fma_mix_f32 v136, v136, v200, v204 op_sel_hi:[1,0,0]\n"
      "v_fma_mix_f32 v140, v140, v200, v204 op_sel_hi:[1,0,0]\n"
      "v_fma_mix_f32 v144, v144, v200, v204 op_sel_hi:[1,0,0]\n"
      "v_fma_mix_f32 v148, v148, v200, v204 op_sel_hi:[1,0,0]\n"
      "v_cvt_pk_bf16_f32 v186, v136, v140\n"
      "v_cvt_pk_bf16_f32 v187, v144, v148\n"
      "ds_write_b128 v244, v[184:187] offset:16384\n"
      "v_or3_b32 v208, v137, v141, v208\n"
      "v_or3_b32 v209, v145, v149, v209\n"
      "v_fma_mix_f32 v137, v137, v201, v205 op_sel_hi:[1,0,0]\n"
      "v_fma_mix_f32 v141, v141, v201, v205 op_sel_hi:[1,0,0]\n"
      "v_fma_mix_f32 v145, v145, v201, v205 op_sel_hi:[1,0,0]\n"
      "v_fma_mix_f32 v149, v149, v201, v205 op_sel_hi:[1,0,0]\n"
      "v_cvt_pk_bf16_f32 v190, v137, v141\n"
      "v_cvt_pk_bf16_f32 v191, v145, v149\n"
      "ds_write_b128 v244, v[188:191] offset:17408\n"
      "v_or3_b32 v208, v138, v142, v208\n"
      "v_or3_b32 v209, v146, v150, v209\n"
      "v_fma_mix_f32 v138, v138, v202, v206 op_sel_hi:[1,0,0]\n"
      "v_fma_mix_f32 v142, v142, v202, v206 op_sel_hi:[1,0,0]\n"
      "v_fma_mix_f32 v146, v146, v202, v206 op_sel_hi:[1,0,0]\n"
      "v_fma_mix_f32 v150, v150, v202, v206 op_sel_hi:[1,0,0]\n"
      "v_cvt_pk_bf16_f32 v194, v138, v142\n"
      "v_cvt_pk_bf16_f32 v195, v146, v150\n"
      "ds_write_b128 v245, v[192:195] offset:16384\n"
      "v_or3_b32 v208, v139, v143, v208\n"
      "v_or3_b32 v209, v147, v151, v209\n"
      "v_fma_mix_f32 v139, v139, v203, v207 op_sel_hi:[1,0,0]\n"
      "v_fma_mix_f32 v143, v143, v203, v207 op_sel_hi:[1,0,0]\n"
      "v_fma_mix_f32 v147, v147, v203, v207 op_sel_hi:[1,0,0]\n"
      "v_fma_mix_f32 v151, v151, v203, v207 op_sel_hi:[1,0,0]\n"
      "v_cvt_pk_bf16_f32 v198, v139, v143\n"
      "v_cvt_pk_bf16_f32 v199, v147, v151\n"
      "ds_write_b128 v245, v[196:199] offset:17408\n"
      "v_or_b32 v208, v208, v209\n"
      "v_cmp_lt_u32 vcc, 0x7f, v208\n"
      "s_or_b64 s[66:67], s[66:67], vcc\n"
      "s_or_b32 s66, s66, s67\n"
      "v_mov_b32 v201, s45\n"
      "v_mov_b32 v200, s66\n"
      "ds_write_b32 v201, v200\n"
      "s_setprio 0\n"
      "s_add_u32 m0, s43, 32768\n"
      "s_add_u32 s61, s36, 19328\n"
      "buffer_load_dwordx4 v243, s[16:19], s61 offen lds\n"
      "s_add_u32 m0, s43, 34816\n"
      "s_add_u32 s61, s36, 19456\n"
      "buffer_load_dwordx4 v243, s[16:19], s61 offen lds\n"
      "s_waitcnt lgkmcnt(0)\n"
      "s_waitcnt vmcnt(2)\n"
      "s_barrier\n"
      "ds_read_b128 v[120:123], v242 offset:16384\n"
      "ds_read_b128 v[124:127], v242 offset:17408\n"
      "ds_read_b128 v[128:131], v242 offset:18432\n"
      "ds_read_b128 v[132:135], v242 offset:19456\n"
      "s_add_u32 m0, s43, 51200\n"
      "s_add_u32 s61, s36, 37760\n"
      "buffer_load_dwordx4 v243, s[16:19], s61 offen lds\n"
      "s_add_u32 m0, s43, 53248\n"
      "s_add_u32 s61, s36, 37888\n"
      "buffer_load_dwordx4 v243, s[16:19], s61 offen lds\n"
      "s_waitcnt lgkmcnt(0)\n"
      "s_waitcnt vmcnt(2)\n"
      "s_barrier\n"
      "ds_read_b128 v[136:139], v242 offset:0\n"
      "ds_read_b128 v[140:143], v242 offset:1024\n"
      "ds_read_b128 v[144:147], v242 offset:2048\n"
      "ds_read_b128 v[148:151], v242 offset:3072\n"
      "s_add_u32 m0, s43, 69632\n"
      "s_add_u32 s61, s36, 56192\n"
      "buffer_load_dwordx4 v243, s[16:19], s61 offen lds\n"
      "s_add_u32 m0, s43, 71680\n"
      "s_add_u32 s61, s36, 56320\n"
      "buffer_load_dwordx4 v243, s[16:19], s61 offen lds\n"
      "s_waitcnt lgkmcnt(0)\n"
      "ds_read_b128 v[168:171], v240 offset:18432\n"
      "ds_read_b128 v[172:175], v241 offset:18432\n"
      "ds_read_b128 v[176:179], v240 offset:20480\n"
      "ds_read_b128 v[180:183], v241 offset:20480\n"
      "ds_read_b128 v[184:187], v240 offset:22528\n"
      "ds_read_b128 v[188:191], v241 offset:22528\n"
      "s_setprio 1\n"
      "s_add_u32 m0, s43, 102400\n"
      "s_add_u32 s61, s36, 92800\n"
      "buffer_load_dwordx4 v243, s[16:19], s61 offen lds\n"
      "s_add_u32 m0, s43, 104448\n"
      "s_add_u32 s61, s36, 92928\n"
      "buffer_load_dwordx4 v243, s[16:19], s61 offen lds\n"
      "s_add_u32 m0, s43, 106496\n"
      "s_add_u32 s61, s36, 93056\n"
      "buffer_load_dwordx4 v243, s[16:19], s61 offen lds\n"
      "s_add_u32 m0, s43, 108544\n"
      "s_add_u32 s61, s36, 93184\n"
      "buffer_load_dwordx4 v243, s[16:19], s61 offen lds\n"
      "s_setprio 0\n"
      "s_waitcnt lgkmcnt(5)\n"
      "v_mfma_f32_16x16x32_bf16 v[152:155], v[168:171], v[8:11], 0\n"
      "ds_read_b128 v[192:195], v240 offset:24576\n"
      "v_mfma_f32_16x16x32_bf16 v[156:159], v[168:171], v[12:15], 0\n"
      "s_waitcnt lgkmcnt(5)\n"
      "v_mfma_f32_16x16x32_bf16 v[152:155], v[172:175], v[16:19], v[152:155]\n"
      "ds_read_b128 v[196:199], v241 offset:24576\n"
      "v_mfma_f32_16x16x32_bf16 v[156:159], v[172:175], v[20:23], v[156:159]\n"
      "s_waitcnt lgkmcnt(5)\n"
      "v_mfma_f32_16x16x32_bf16 v[152:155], v[176:179], v[24:27], v[152:155]\n"
      "ds_read_b128 v[168:171], v240 offset:26624\n"
      "v_mfma_f32_16x16x32_bf16 v[156:159], v[176:179], v[28:31], v[156:159]\n"
      "s_waitcnt lgkmcnt(5)\n"
      "v_mfma_f32_16x16x32_bf16 v[152:155], v[180:183], v[32:35], v[152:155]\n"
      "ds_read_b128 v[172:175], v241 offset:26624\n"
      "v_mfma_f32_16x16x32_bf16 v[156:159], v[180:183], v[36:39], v[156:159]\n"
      "s_waitcnt lgkmcnt(5)\n"
      "v_mfma_f32_16x16x32_bf16 v[152:155], v[184:187], v[40:43], v[152:155]\n"
      "ds_read_b128 v[176:179], v240 offset:28672\n"
      "v_mfma_f32_16x16x32_bf16 v[156:159], v[184:187], v[44:47], v[156:159]\n"
      "s_waitcnt lgkmcnt(5)\n"
      "v_mfma_f32_16x16x32_bf16 v[152:155], v[188:191], v[48:51], v[152:155]\n"
      "ds_read_b128 v[180:183], v241 offset:28672\n"
      "v_mfma_f32_16x16x32_bf16 v[156:159], v[188:191], v[52:55], v[156:159]\n"
      "s_waitcnt lgkmcnt(5)\n"
      "v_mfma_f32_16x16x32_bf16 v[152:155], v[192:195], v[56:59], v[152:155]\n"
      "ds_read_b128 v[184:187], v240 offset:30720\n"
      "v_mfma_f32_16x16x32_bf16 v[156:159], v[192:195], v[60:63], v[156:159]\n"
      "s_waitcnt lgkmcnt(5)\n"
      "v_mfma_f32_16x16x32_bf16 v[152:155], v[196:199], v[64:67], v[152:155]\n"
      "ds_read_b128 v[188:191], v241 offset:30720\n"
      "v_mfma_f32_16x16x32_bf16 v[156:159], v[196:199], v[68:71], v[156:159]\n"
      "s_waitcnt lgkmcnt(5)\n"
      "v_mfma_f32_16x16x32_bf16 v[152:155], v[168:171], v[72:75], v[152:155]\n"
      "ds_read_b128 v[192:195], v240 offset:32768\n"
      "v_mfma_f32_16x16x32_bf16 v[156:159], v[168:171], v[76:79], v[156:159]\n"
      "s_waitcnt vmcnt(4)\n"
      "s_barrier\n"
      "s_setprio 1\n"
      "s_add_u32 m0, s43, 139264\n"
      "s_add_u32 s61, s36, 129664\n"
      "buffer_load_dwordx4 v243, s[16:19], s61 offen lds\n"
      "s_add_u32 m0, s43, 141312\n"
      "s_add_u32 s61, s36, 129792\n"
      "buffer_load_dwordx4 v243, s[16:19], s61 offen lds\n"
      "s_add_u32 m0, s43, 143360\n"
      "s_add_u32 s61, s36, 129920\n"
      "buffer_load_dwordx4 v243, s[16:19], s61 offen lds\n"
      "s_add_u32 m0, s43, 145408\n"
      "s_add_u32 s61, s36, 130048\n"
      "buffer_load_dwordx4 v243, s[16:19], s61 offen lds\n"
      "s_setprio 0\n"
      "s_waitcnt lgkmcnt(5)\n"
      "v_mfma_f32_16x16x32_bf16 v[152:155], v[172:175], v[80:83], v[152:155]\n"
      "ds_read_b128 v[196:199], v241 offset:32768\n"
      "v_mfma_f32_16x16x32_bf16 v[156:159], v[172:175], v[84:87], v[156:159]\n"
      "s_waitcnt lgkmcnt(5)\n"
      "v_mfma_f32_16x16x32_bf16 v[152:155], v[176:179], v[88:91], v[152:155]\n"
      "ds_read_b128 v[168:171], v240 offset:34816\n"
      "v_mfma_f32_16x16x32_bf16 v[156:159], v[176:179], v[92:95], v[156:159]\n"
      "s_waitcnt lgkmcnt(5)\n"
      "v_mfma_f32_16x16x32_bf16 v[152:155], v[180:183], v[96:99], v[152:155]\n"
      "ds_read_b128 v[172:175], v241 offset:34816\n"
      "v_mfma_f32_16x16x32_bf16 v[156:159], v[180:183], v[100:103], v[156:159]\n"
      "s_waitcnt lgkmcnt(5)\n"
      "v_mfma_f32_16x16x32_bf16 v[152:155], v[184:187], v[104:107], v[152:155]\n"
      "ds_read_b128 v[176:179], v240 offset:55296\n"
      "v_mfma_f32_16x16x32_bf16 v[156:159], v[184:187], v[108:111], v[156:159]\n"
      "s_waitcnt lgkmcnt(5)\n"
      "v_mfma_f32_16x16x32_bf16 v[152:155], v[188:191], v[112:115], v[152:155]\n"
      "ds_read_b128 v[180:183], v241 offset:55296\n"
      "v_mfma_f32_16x16x32_bf16 v[156:159], v[188:191], v[116:119], v[156:159]\n"
      "s_waitcnt lgkmcnt(5)\n"
      "v_mfma_f32_16x16x32_bf16 v[152:155], v[192:195], v[120:123], v[152:155]\n"
      "ds_read_b128 v[184:187], v240 offset:57344\n"
      "v_mfma_f32_16x16x32_bf16 v[156:159], v[192:195], v[124:127], v[156:159]\n"
      "s_waitcnt lgkmcnt(5)\n"
      "v_mfma_f32_16x16x32_bf16 v[152:155], v[196:199], v[128:131], v[152:155]\n"
      "ds_read_b128 v[188:191], v241 offset:57344\n"
      "v_mfma_f32_16x16x32_bf16 v[156:159], v[196:199], v[132:135], v[156:159]\n"
      "s_waitcnt lgkmcnt(5)\n"
      "v_mfma_f32_16x16x32_bf16 v[152:155], v[168:171], v[136:139], v[152:155]\n"
      "ds_read_b128 v[192:195], v240 offset:59392\n"
      "v_mfma_f32_16x16x32_bf16 v[156:159], v[168:171], v[140:143], v[156:159]\n"
      "s_waitcnt lgkmcnt(5)\n"
      "v_mfma_f32_16x16x32_bf16 v[152:155], v[172:175], v[144:147], v[152:155]\n"
      "ds_read_b128 v[196:199], v241 offset:59392\n"
      "v_mfma_f32_16x16x32_bf16 v[156:159], v[172:175], v[148:151], v[156:159]\n"
      "s_waitcnt lgkmcnt(5)\n"
      "v_mfma_f32_16x16x32_bf16 v[160:163], v[176:179], v[8:11], 0\n"
      "ds_read_b128 v[168:171], v240 offset:61440\n"
      "v_mfma_f32_16x16x32_bf16 v[164:167], v[176:179], v[12:15], 0\n"
      "s_waitcnt lgkmcnt(5)\n"
      "v_mfma_f32_16x16x32_bf16 v[160:163], v[180:183], v[16:19], v[160:163]\n"
      "ds_read_b128 v[172:175], v241 offset:61440\n"
      "v_mfma_f32_16x16x32_bf16 v[164:167], v[180:183], v[20:23], v[164:167]\n"
      "s_waitcnt lgkmcnt(5)\n"
      "v_mfma_f32_16x16x32_bf16 v[160:163], v[184:187], v[24:27], v[160:163]\n"
      "ds_read_b128 v[176:179], v240 offset:63488\n"
      "v_mfma_f32_16x16x32_bf16 v[164:167], v[184:187], v[28:31], v[164:167]\n"
      "s_waitcnt lgkmcnt(5)\n"
      "v_mfma_f32_16x16x32_bf16 v[160:163], v[188:191], v[32:35], v[160:163]\n"
      "ds_read_b128 v[180:183], v241 offset:63488\n"
      "v_mfma_f32_16x16x32_bf16 v[164:167], v[188:191], v[36:39], v[164:167]\n"
      "s_waitcnt lgkmcnt(5)\n"
      "v_mfma_f32_16x16x32_bf16 v[160:163], v[192:195], v[40:43], v[160:163]\n"
      "ds_read_b128 v[184:187], v214 offset:0\n"
      "v_mfma_f32_16x16x32_bf16 v[164:167], v[192:195], v[44:47], v[164:167]\n"
      "s_waitcnt lgkmcnt(5)\n"
      "v_mfma_f32_16x16x32_bf16 v[160:163], v[196:199], v[48:51], v[160:163]\n"
      "ds_read_b128 v[188:191], v215 offset:0\n"
      "v_mfma_f32_16x16x32_bf16 v[164:167], v[196:199], v[52:55], v[164:167]\n"
      "s_waitcnt lgkmcnt(5)\n"
      "v_mfma_f32_16x16x32_bf16 v[160:163], v[168:171], v[56:59], v[160:163]\n"
      "ds_read_b128 v[192:195], v214 offset:2048\n"
      "v_mfma_f32_16x16x32_bf16 v[164:167], v[168:171], v[60:63], v[164:167]\n"
      "s_waitcnt lgkmcnt(5)\n"
      "v_mfma_f32_16x16x32_bf16 v[160:163], v[172:175], v[64:67], v[160:163]\n"
      "ds_read_b128 v[196:199], v215 offset:2048\n"
      "v_mfma_f32_16x16x32_bf16 v[164:167], v[172:175], v[68:71], v[164:167]\n"
      "s_waitcnt lgkmcnt(5)\n"
      "v_mfma_f32_16x16x32_bf16 v[160:163], v[176:179], v[72:75], v[160:163]\n"
      "ds_read_b128 v[168:171], v214 offset:4096\n"
      "v_mfma_f32_16x16x32_bf16 v[164:167], v[176:179], v[76:79], v[164:167]\n"
      "s_waitcnt vmcnt(4)\n"
      "s_barrier\n"
      "s_setprio 1\n"
      "s_add_u32 m0, s43, 28672\n"
      "s_add_u32 s61, s36, 166528\n"
      "buffer_load_dwordx4 v243, s[16:19], s61 offen lds\n"
      "s_add_u32 m0, s43, 30720\n"
      "s_add_u32 s61, s36, 166656\n"
      "buffer_load_dwordx4 v243, s[16:19], s61 offen lds\n"
      "s_add_u32 m0, s43, 32768\n"
      "s_add_u32 s61, s36, 166784\n"
      "buffer_load_dwordx4 v243, s[16:19], s61 offen lds\n"
      "s_add_u32 m0, s43, 34816\n"
      "s_add_u32 s61, s36, 166912\n"
      "buffer_load_dwordx4 v243, s[16:19], s61 offen lds\n"
      "v_cvt_pk_bf16_f32 v152, v152, v156\n"
      "v_cvt_pk_bf16_f32 v153, v153, v157\n"
      "v_cvt_pk_bf16_f32 v154, v154, v158\n"
      "v_cvt_pk_bf16_f32 v155, v155, v159\n"
      "s_add_u32 s62, s37, 147456\n"
      "buffer_store_dword v152, v247, s[68:71], s62 offen sc1\n"
      "s_add_u32 s62, s37, 156672\n"
      "buffer_store_dword v153, v247, s[68:71], s62 offen sc1\n"
      "s_add_u32 s62, s37, 165888\n"
      "buffer_store_dword v154, v247, s[68:71], s62 offen sc1\n"
      "s_add_u32 s62, s37, 175104\n"
      "buffer_store_dword v155, v247, s[68:71], s62 offen sc1\n"
      "s_setprio 0\n"
      "s_waitcnt lgkmcnt(5)\n"
      "v_mfma_f32_16x16x32_bf16 v[160:163], v[180:183], v[80:83], v[160:163]\n"
      "ds_read_b128 v[172:175], v215 offset:4096\n"
      "v_mfma_f32_16x16x32_bf16 v[164:167], v[180:183], v[84:87], v[164:167]\n"
      "s_waitcnt lgkmcnt(5)\n"
      "v_mfma_f32_16x16x32_bf16 v[160:163], v[184:187], v[88:91], v[160:163]\n"
      "ds_read_b128 v[176:179], v214 offset:6144\n"
      "v_mfma_f32_16x16x32_bf16 v[164:167], v[184:187], v[92:95], v[164:167]\n"
      "s_waitcnt lgkmcnt(5)\n"
      "v_mfma_f32_16x16x32_bf16 v[160:163], v[188:191], v[96:99], v[160:163]\n"
      "ds_read_b128 v[180:183], v215 offset:6144\n"
      "v_mfma_f32_16x16x32_bf16 v[164:167], v[188:191], v[100:103], v[164:167]\n"
      "s_waitcnt lgkmcnt(5)\n"
      "v_mfma_f32_16x16x32_bf16 v[160:163], v[192:195], v[104:107], v[160:163]\n"
      "ds_read_b128 v[184:187], v214 offset:26624\n"
      "v_mfma_f32_16x16x32_bf16 v[164:167], v[192:195], v[108:111], v[164:167]\n"
      "s_waitcnt lgkmcnt(5)\n"
      "v_mfma_f32_16x16x32_bf16 v[160:163], v[196:199], v[112:115], v[160:163]\n"
      "ds_read_b128 v[188:191], v215 offset:26624\n"
      "v_mfma_f32_16x16x32_bf16 v[164:167], v[196:199], v[116:119], v[164:167]\n"
      "s_waitcnt lgkmcnt(5)\n"
      "v_mfma_f32_16x16x32_bf16 v[160:163], v[168:171], v[120:123], v[160:163]\n"
      "ds_read_b128 v[192:195], v214 offset:28672\n"
      "v_mfma_f32_16x16x32_bf16 v[164:167], v[168:171], v[124:127], v[164:167]\n"
      "s_waitcnt lgkmcnt(5)\n"
      "v_mfma_f32_16x16x32_bf16 v[160:163], v[172:175], v[128:131], v[160:163]\n"
      "ds_read_b128 v[196:199], v215 offset:28672\n"
      "v_mfma_f32_16x16x32_bf16 v[164:167], v[172:175], v[132:135], v[164:167]\n"
      "s_waitcnt lgkmcnt(5)\n"
      "v_mfma_f32_16x16x32_bf16 v[160:163], v[176:179], v[136:139], v[160:163]\n"
      "ds_read_b128 v[168:171], v214 offset:30720\n"
      "v_mfma_f32_16x16x32_bf16 v[164:167], v[176:179], v[140:143], v[164:167]\n"
      "s_waitcnt lgkmcnt(5)\n"
      "v_mfma_f32_16x16x32_bf16 v[160:163], v[180:183], v[144:147], v[160:163]\n"
      "ds_read_b128 v[172:175], v215 offset:30720\n"
      "v_mfma_f32_16x16x32_bf16 v[164:167], v[180:183], v[148:151], v[164:167]\n"
      "s_waitcnt lgkmcnt(5)\n"
      "v_mfma_f32_16x16x32_bf16 v[152:155], v[184:187], v[8:11], 0\n"
      "ds_read_b128 v[176:179], v214 offset:32768\n"
      "v_mfma_f32_16x16x32_bf16 v[156:159], v[184:187], v[12:15], 0\n"
      "s_waitcnt lgkmcnt(5)\n"
      "v_mfma_f32_16x16x32_bf16 v[152:155], v[188:191], v[16:19], v[152:155]\n"
      "ds_read_b128 v[180:183], v215 offset:32768\n"
      "v_mfma_f32_16x16x32_bf16 v[156:159], v[188:191], v[20:23], v[156:159]\n"
      "s_waitcnt lgkmcnt(5)\n"
      "v_mfma_f32_16x16x32_bf16 v[152:155], v[192:195], v[24:27], v[152:155]\n"
      "ds_read_b128 v[184:187], v214 offset:34816\n"
      "v_mfma_f32_16x16x32_bf16 v[156:159], v[192:195], v[28:31], v[156:159]\n"
      "s_waitcnt lgkmcnt(5)\n"
      "v_mfma_f32_16x16x32_bf16 v[152:155], v[196:199], v[32:35], v[152:155]\n"
      "ds_read_b128 v[188:191], v215 offset:34816\n"
      "v_mfma_f32_16x16x32_bf16 v[156:159], v[196:199], v[36:39], v[156:159]\n"
      "s_waitcnt lgkmcnt(5)\n"
      "v_mfma_f32_16x16x32_bf16 v[152:155], v[168:171], v[40:43], v[152:155]\n"
      "ds_read_b128 v[192:195], v214 offset:36864\n"
      "v_mfma_f32_16x16x32_bf16 v[156:159], v[168:171], v[44:47], v[156:159]\n"
      "s_waitcnt lgkmcnt(5)\n"
      "v_mfma_f32_16x16x32_bf16 v[152:155], v[172:175], v[48:51], v[152:155]\n"
      "ds_read_b128 v[196:199], v215 offset:36864\n"
      "v_mfma_f32_16x16x32_bf16 v[156:159], v[172:175], v[52:55], v[156:159]\n"
      "s_waitcnt lgkmcnt(5)\n"
      "v_mfma_f32_16x16x32_bf16 v[152:155], v[176:179], v[56:59], v[152:155]\n"
      "ds_read_b128 v[168:171], v214 offset:38912\n"
      "v_mfma_f32_16x16x32_bf16 v[156:159], v[176:179], v[60:63], v[156:159]\n"
      "s_waitcnt lgkmcnt(5)\n"
      "v_mfma_f32_16x16x32_bf16 v[152:155], v[180:183], v[64:67], v[152:155]\n"
      "ds_read_b128 v[172:175], v215 offset:38912\n"
      "v_mfma_f32_16x16x32_bf16 v[156:159], v[180:183], v[68:71], v[156:159]\n"
      "s_waitcnt lgkmcnt(5)\n"
      "v_mfma_f32_16x16x32_bf16 v[152:155], v[184:187], v[72:75], v[152:155]\n"
      "ds_read_b128 v[176:179], v214 offset:40960\n"
      "v_mfma_f32_16x16x32_bf16 v[156:159], v[184:187], v[76:79], v[156:159]\n"
      "s_waitcnt vmcnt(8)\n"
      "s_barrier\n"
      "s_setprio 1\n"
      "s_add_u32 m0, s43, 65536\n"
      "s_add_u32 s61, s36, 203392\n"
      "buffer_load_dwordx4 v243, s[16:19], s61 offen lds\n"
      "s_add_u32 m0, s43, 67584\n"
      "s_add_u32 s61, s36, 203520\n"
      "buffer_load_dwordx4 v243, s[16:19], s61 offen lds\n"
      "s_add_u32 m0, s43, 69632\n"
      "s_add_u32 s61, s36, 203648\n"
      "buffer_load_dwordx4 v243, s[16:19], s61 offen lds\n"
      "s_add_u32 m0, s43, 71680\n"
      "s_add_u32 s61, s36, 203776\n"
      "buffer_load_dwordx4 v243, s[16:19], s61 offen lds\n"
      "v_cvt_pk_bf16_f32 v160, v160, v164\n"
      "v_cvt_pk_bf16_f32 v161, v161, v165\n"
      "v_cvt_pk_bf16_f32 v162, v162, v166\n"
      "v_cvt_pk_bf16_f32 v163, v163, v167\n"
      "s_add_u32 s62, s37, 442368\n"
      "buffer_store_dword v160, v247, s[68:71], s62 offen sc1\n"
      "s_add_u32 s62, s37, 451584\n"
      "buffer_store_dword v161, v247, s[68:71], s62 offen sc1\n"
      "s_add_u32 s62, s37, 460800\n"
      "buffer_store_dword v162, v247, s[68:71], s62 offen sc1\n"
      "s_add_u32 s62, s37, 470016\n"
      "buffer_store_dword v163, v247, s[68:71], s62 offen sc1\n"
      "s_setprio 0\n"
      "s_waitcnt lgkmcnt(5)\n"
      "v_mfma_f32_16x16x32_bf16 v[152:155], v[188:191], v[80:83], v[152:155]\n"
      "ds_read_b128 v[180:183], v215 offset:40960\n"
      "v_mfma_f32_16x16x32_bf16 v[156:159], v[188:191], v[84:87], v[156:159]\n"
      "s_waitcnt lgkmcnt(5)\n"
      "v_mfma_f32_16x16x32_bf16 v[152:155], v[192:195], v[88:91], v[152:155]\n"
      "ds_read_b128 v[184:187], v214 offset:43008\n"
      "v_mfma_f32_16x16x32_bf16 v[156:159], v[192:195], v[92:95], v[156:159]\n"
      "s_waitcnt lgkmcnt(5)\n"
      "v_mfma_f32_16x16x32_bf16 v[152:155], v[196:199], v[96:99], v[152:155]\n"
      "ds_read_b128 v[188:191], v215 offset:43008\n"
      "v_mfma_f32_16x16x32_bf16 v[156:159], v[196:199], v[100:103], v[156:159]\n"
      "s_waitcnt lgkmcnt(5)\n"
      "v_mfma_f32_16x16x32_bf16 v[152:155], v[168:171], v[104:107], v[152:155]\n"
      "ds_read_b128 v[192:195], v214 offset:63488\n"
      "v_mfma_f32_16x16x32_bf16 v[156:159], v[168:171], v[108:111], v[156:159]\n"
      "s_waitcnt lgkmcnt(5)\n"
      "v_mfma_f32_16x16x32_bf16 v[152:155], v[172:175], v[112:115], v[152:155]\n"
      "ds_read_b128 v[196:199], v215 offset:63488\n"
      "v_mfma_f32_16x16x32_bf16 v[156:159], v[172:175], v[116:119], v[156:159]\n"
      "s_waitcnt lgkmcnt(5)\n"
      "v_mfma_f32_16x16x32_bf16 v[152:155], v[176:179], v[120:123], v[152:155]\n"
      "ds_read_b128 v[168:171], v216 offset:0\n"
      "v_mfma_f32_16x16x32_bf16 v[156:159], v[176:179], v[124:127], v[156:159]\n"
      "s_waitcnt lgkmcnt(5)\n"
      "v_mfma_f32_16x16x32_bf16 v[152:155], v[180:183], v[128:131], v[152:155]\n"
      "ds_read_b128 v[172:175], v217 offset:0\n"
      "v_mfma_f32_16x16x32_bf16 v[156:159], v[180:183], v[132:135], v[156:159]\n"
      "s_waitcnt lgkmcnt(5)\n"
      "v_mfma_f32_16x16x32_bf16 v[152:155], v[184:187], v[136:139], v[152:155]\n"
      "ds_read_b128 v[176:179], v216 offset:2048\n"
      "v_mfma_f32_16x16x32_bf16 v[156:159], v[184:187], v[140:143], v[156:159]\n"
      "s_waitcnt lgkmcnt(5)\n"
      "v_mfma_f32_16x16x32_bf16 v[152:155], v[188:191], v[144:147], v[152:155]\n"
      "ds_read_b128 v[180:183], v217 offset:2048\n"
      "v_mfma_f32_16x16x32_bf16 v[156:159], v[188:191], v[148:151], v[156:159]\n"
      "s_waitcnt lgkmcnt(5)\n"
      "v_mfma_f32_16x16x32_bf16 v[160:163], v[192:195], v[8:11], 0\n"
      "ds_read_b128 v[184:187], v216 offset:4096\n"
      "v_mfma_f32_16x16x32_bf16 v[164:167], v[192:195], v[12:15], 0\n"
      "s_waitcnt lgkmcnt(5)\n"
      "v_mfma_f32_16x16x32_bf16 v[160:163], v[196:199], v[16:19], v[160:163]\n"
      "ds_read_b128 v[188:191], v217 offset:4096\n"
      "v_mfma_f32_16x16x32_bf16 v[164:167], v[196:199], v[20:23], v[164:167]\n"
      "s_waitcnt lgkmcnt(5)\n"
      "v_mfma_f32_16x16x32_bf16 v[160:163], v[168:171], v[24:27], v[160:163]\n"
      "ds_read_b128 v[192:195], v216 offset:6144\n"
      "v_mfma_f32_16x16x32_bf16 v[164:167], v[168:171], v[28:31], v[164:167]\n"
      "s_waitcnt lgkmcnt(5)\n"
      "v_mfma_f32_16x16x32_bf16 v[160:163], v[172:175], v[32:35], v[160:163]\n"
      "ds_read_b128 v[196:199], v217 offset:6144\n"
      "v_mfma_f32_16x16x32_bf16 v[164:167], v[172:175], v[36:39], v[164:167]\n"
      "s_waitcnt lgkmcnt(5)\n"
      "v_mfma_f32_16x16x32_bf16 v[160:163], v[176:179], v[40:43], v[160:163]\n"
      "ds_read_b128 v[168:171], v216 offset:8192\n"
      "v_mfma_f32_16x16x32_bf16 v[164:167], v[176:179], v[44:47], v[164:167]\n"
      "s_waitcnt lgkmcnt(5)\n"
      "v_mfma_f32_16x16x32_bf16 v[160:163], v[180:183], v[48:51], v[160:163]\n"
      "ds_read_b128 v[172:175], v217 offset:8192\n"
      "v_mfma_f32_16x16x32_bf16 v[164:167], v[180:183], v[52:55], v[164:167]\n"
      "s_waitcnt lgkmcnt(5)\n"
      "v_mfma_f32_16x16x32_bf16 v[160:163], v[184:187], v[56:59], v[160:163]\n"
      "ds_read_b128 v[176:179], v216 offset:10240\n"
      "v_mfma_f32_16x16x32_bf16 v[164:167], v[184:187], v[60:63], v[164:167]\n"
      "s_waitcnt lgkmcnt(5)\n"
      "v_mfma_f32_16x16x32_bf16 v[160:163], v[188:191], v[64:67], v[160:163]\n"
      "ds_read_b128 v[180:183], v217 offset:10240\n"
      "v_mfma_f32_16x16x32_bf16 v[164:167], v[188:191], v[68:71], v[164:167]\n"
      "s_waitcnt lgkmcnt(5)\n"
      "v_mfma_f32_16x16x32_bf16 v[160:163], v[192:195], v[72:75], v[160:163]\n"
      "ds_read_b128 v[184:187], v216 offset:12288\n"
      "v_mfma_f32_16x16x32_bf16 v[164:167], v[192:195], v[76:79], v[164:167]\n"
      "s_waitcnt vmcnt(12)\n"
      "s_barrier\n"
      "s_setprio 1\n"
      "s_add_u32 m0, s43, 102400\n"
      "s_add_u32 s61, s36, 240256\n"
      "buffer_load_dwordx4 v243, s[16:19], s61 offen lds\n"
      "s_add_u32 m0, s43, 104448\n"
      "s_add_u32 s61, s36, 240384\n"
      "buffer_load_dwordx4 v243, s[16:19], s61 offen lds\n"
      "s_add_u32 m0, s43, 106496\n"
      "s_add_u32 s61, s36, 240512\n"
      "buffer_load_dwordx4 v243, s[16:19], s61 offen lds\n"
      "s_add_u32 m0, s43, 108544\n"
      "s_add_u32 s61, s36, 240640\n"
      "buffer_load_dwordx4 v243, s[16:19], s61 offen lds\n"
      "v_cvt_pk_bf16_f32 v152, v152, v156\n"
      "v_cvt_pk_bf16_f32 v153, v153, v157\n"
      "v_cvt_pk_bf16_f32 v154, v154, v158\n"
      "v_cvt_pk_bf16_f32 v155, v155, v159\n"
      "s_add_u32 s62, s37, 737280\n"
      "buffer_store_dword v152, v247, s[68:71], s62 offen sc1\n"
      "s_add_u32 s62, s37, 746496\n"
      "buffer_store_dword v153, v247, s[68:71], s62 offen sc1\n"
      "s_add_u32 s62, s37, 755712\n"
      "buffer_store_dword v154, v247, s[68:71], s62 offen sc1\n"
      "s_add_u32 s62, s37, 764928\n"
      "buffer_store_dword v155, v247, s[68:71], s62 offen sc1\n"
      "s_setprio 0\n"
      "s_waitcnt lgkmcnt(5)\n"
      "v_mfma_f32_16x16x32_bf16 v[160:163], v[196:199], v[80:83], v[160:163]\n"
      "ds_read_b128 v[188:191], v217 offset:12288\n"
      "v_mfma_f32_16x16x32_bf16 v[164:167], v[196:199], v[84:87], v[164:167]\n"
      "s_waitcnt lgkmcnt(5)\n"
      "v_mfma_f32_16x16x32_bf16 v[160:163], v[168:171], v[88:91], v[160:163]\n"
      "ds_read_b128 v[192:195], v216 offset:14336\n"
      "v_mfma_f32_16x16x32_bf16 v[164:167], v[168:171], v[92:95], v[164:167]\n"
      "s_waitcnt lgkmcnt(5)\n"
      "v_mfma_f32_16x16x32_bf16 v[160:163], v[172:175], v[96:99], v[160:163]\n"
      "ds_read_b128 v[196:199], v217 offset:14336\n"
      "v_mfma_f32_16x16x32_bf16 v[164:167], v[172:175], v[100:103], v[164:167]\n"
      "s_waitcnt lgkmcnt(5)\n"
      "v_mfma_f32_16x16x32_bf16 v[160:163], v[176:179], v[104:107], v[160:163]\n"
      "ds_read_b128 v[168:171], v240 offset:18432\n"
      "v_mfma_f32_16x16x32_bf16 v[164:167], v[176:179], v[108:111], v[164:167]\n"
      "s_waitcnt lgkmcnt(5)\n"
      "v_mfma_f32_16x16x32_bf16 v[160:163], v[180:183], v[112:115], v[160:163]\n"
      "ds_read_b128 v[172:175], v241 offset:18432\n"
      "v_mfma_f32_16x16x32_bf16 v[164:167], v[180:183], v[116:119], v[164:167]\n"
      "s_waitcnt lgkmcnt(5)\n"
      "v_mfma_f32_16x16x32_bf16 v[160:163], v[184:187], v[120:123], v[160:163]\n"
      "ds_read_b128 v[176:179], v240 offset:20480\n"
      "v_mfma_f32_16x16x32_bf16 v[164:167], v[184:187], v[124:127], v[164:167]\n"
      "s_waitcnt lgkmcnt(5)\n"
      "v_mfma_f32_16x16x32_bf16 v[160:163], v[188:191], v[128:131], v[160:163]\n"
      "ds_read_b128 v[180:183], v241 offset:20480\n"
      "v_mfma_f32_16x16x32_bf16 v[164:167], v[188:191], v[132:135], v[164:167]\n"
      "s_waitcnt lgkmcnt(5)\n"
      "v_mfma_f32_16x16x32_bf16 v[160:163], v[192:195], v[136:139], v[160:163]\n"
      "ds_read_b128 v[184:187], v240 offset:22528\n"
      "v_mfma_f32_16x16x32_bf16 v[164:167], v[192:195], v[140:143], v[164:167]\n"
      "s_waitcnt lgkmcnt(5)\n"
      "v_mfma_f32_16x16x32_bf16 v[160:163], v[196:199], v[144:147], v[160:163]\n"
      "ds_read_b128 v[188:191], v241 offset:22528\n"
      "v_mfma_f32_16x16x32_bf16 v[164:167], v[196:199], v[148:151], v[164:167]\n"
      "s_waitcnt lgkmcnt(5)\n"
      "v_mfma_f32_16x16x32_bf16 v[152:155], v[168:171], v[8:11], 0\n"
      "ds_read_b128 v[192:195], v240 offset:24576\n"
      "v_mfma_f32_16x16x32_bf16 v[156:159], v[168:171], v[12:15], 0\n"
      "s_waitcnt lgkmcnt(5)\n"
      "v_mfma_f32_16x16x32_bf16 v[152:155], v[172:175], v[16:19], v[152:155]\n"
      "ds_read_b128 v[196:199], v241 offset:24576\n"
      "v_mfma_f32_16x16x32_bf16 v[156:159], v[172:175], v[20:23], v[156:159]\n"
      "s_waitcnt lgkmcnt(5)\n"
      "v_mfma_f32_16x16x32_bf16 v[152:155], v[176:179], v[24:27], v[152:155]\n"
      "ds_read_b128 v[168:171], v240 offset:26624\n"
      "v_mfma_f32_16x16x32_bf16 v[156:159], v[176:179], v[28:31], v[156:159]\n"
      "s_waitcnt lgkmcnt(5)\n"
      "v_mfma_f32_16x16x32_bf16 v[152:155], v[180:183], v[32:35], v[152:155]\n"
      "ds_read_b128 v[172:175], v241 offset:26624\n"
      "v_mfma_f32_16x16x32_bf16 v[156:159], v[180:183], v[36:39], v[156:159]\n"
      "s_waitcnt lgkmcnt(5)\n"
      "v_mfma_f32_16x16x32_bf16 v[152:155], v[184:187], v[40:43], v[152:155]\n"
      "ds_read_b128 v[176:179], v240 offset:28672\n"
      "v_mfma_f32_16x16x32_bf16 v[156:159], v[184:187], v[44:47], v[156:159]\n"
      "s_waitcnt lgkmcnt(5)\n"
      "v_mfma_f32_16x16x32_bf16 v[152:155], v[188:191], v[48:51], v[152:155]\n"
      "ds_read_b128 v[180:183], v241 offset:28672\n"
      "v_mfma_f32_16x16x32_bf16 v[156:159], v[188:191], v[52:55], v[156:159]\n"
      "s_waitcnt lgkmcnt(5)\n"
      "v_mfma_f32_16x16x32_bf16 v[152:155], v[192:195], v[56:59], v[152:155]\n"
      "ds_read_b128 v[184:187], v240 offset:30720\n"
      "v_mfma_f32_16x16x32_bf16 v[156:159], v[192:195], v[60:63], v[156:159]\n"
      "s_waitcnt lgkmcnt(5)\n"
      "v_mfma_f32_16x16x32_bf16 v[152:155], v[196:199], v[64:67], v[152:155]\n"
      "ds_read_b128 v[188:191], v241 offset:30720\n"
      "v_mfma_f32_16x16x32_bf16 v[156:159], v[196:199], v[68:71], v[156:159]\n"
      "s_waitcnt lgkmcnt(5)\n"
      "v_mfma_f32_16x16x32_bf16 v[152:155], v[168:171], v[72:75], v[152:155]\n"
      "ds_read_b128 v[192:195], v240 offset:32768\n"
      "v_mfma_f32_16x16x32_bf16 v[156:159], v[168:171], v[76:79], v[156:159]\n"
      "s_waitcnt vmcnt(12)\n"
      "s_barrier\n"
      "s_setprio 1\n"
      "s_add_u32 m0, s43, 139264\n"
      "s_add_u32 s61, s36, 277120\n"
      "buffer_load_dwordx4 v243, s[16:19], s61 offen lds\n"
      "s_add_u32 m0, s43, 141312\n"
      "s_add_u32 s61, s36, 277248\n"
      "buffer_load_dwordx4 v243, s[16:19], s61 offen lds\n"
      "s_add_u32 m0, s43, 143360\n"
      "s_add_u32 s61, s36, 277376\n"
      "buffer_load_dwordx4 v243, s[16:19], s61 offen lds\n"
      "s_add_u32 m0, s43, 145408\n"
      "s_add_u32 s61, s36, 277504\n"
      "buffer_load_dwordx4 v243, s[16:19], s61 offen lds\n"
      "v_cvt_pk_bf16_f32 v160, v160, v164\n"
      "v_cvt_pk_bf16_f32 v161, v161, v165\n"
      "v_cvt_pk_bf16_f32 v162, v162, v166\n"
      "v_cvt_pk_bf16_f32 v163, v163, v167\n"
      "s_add_u32 s62, s37, 1032192\n"
      "buffer_store_dword v160, v247, s[68:71], s62 offen sc1\n"
      "s_add_u32 s62, s37, 1041408\n"
      "buffer_store_dword v161, v247, s[68:71], s62 offen sc1\n"
      "s_add_u32 s62, s37, 1050624\n"
      "buffer_store_dword v162, v247, s[68:71], s62 offen sc1\n"
      "s_add_u32 s62, s37, 1059840\n"
      "buffer_store_dword v163, v247, s[68:71], s62 offen sc1\n"
      "s_setprio 0\n"
      "s_waitcnt lgkmcnt(5)\n"
      "v_mfma_f32_16x16x32_bf16 v[152:155], v[172:175], v[80:83], v[152:155]\n"
      "ds_read_b128 v[196:199], v241 offset:32768\n"
      "v_mfma_f32_16x16x32_bf16 v[156:159], v[172:175], v[84:87], v[156:159]\n"
      "s_waitcnt lgkmcnt(5)\n"
      "v_mfma_f32_16x16x32_bf16 v[152:155], v[176:179], v[88:91], v[152:155]\n"
      "ds_read_b128 v[168:171], v240 offset:34816\n"
      "v_mfma_f32_16x16x32_bf16 v[156:159], v[176:179], v[92:95], v[156:159]\n"
      "s_waitcnt lgkmcnt(5)\n"
      "v_mfma_f32_16x16x32_bf16 v[152:155], v[180:183], v[96:99], v[152:155]\n"
      "ds_read_b128 v[172:175], v241 offset:34816\n"
      "v_mfma_f32_16x16x32_bf16 v[156:159], v[180:183], v[100:103], v[156:159]\n"
      "s_waitcnt lgkmcnt(5)\n"
      "v_mfma_f32_16x16x32_bf16 v[152:155], v[184:187], v[104:107], v[152:155]\n"
      "ds_read_b128 v[176:179], v240 offset:55296\n"
      "v_mfma_f32_16x16x32_bf16 v[156:159], v[184:187], v[108:111], v[156:159]\n"
      "s_waitcnt lgkmcnt(5)\n"
      "v_mfma_f32_16x16x32_bf16 v[152:155], v[188:191], v[112:115], v[152:155]\n"
      "ds_read_b128 v[180:183], v241 offset:55296\n"
      "v_mfma_f32_16x16x32_bf16 v[156:159], v[188:191], v[116:119], v[156:159]\n"
      "s_waitcnt lgkmcnt(5)\n"
      "v_mfma_f32_16x16x32_bf16 v[152:155], v[192:195], v[120:123], v[152:155]\n"
      "ds_read_b128 v[184:187], v240 offset:57344\n"
      "v_mfma_f32_16x16x32_bf16 v[156:159], v[192:195], v[124:127], v[156:159]\n"
      "s_waitcnt lgkmcnt(5)\n"
      "v_mfma_f32_16x16x32_bf16 v[152:155], v[196:199], v[128:131], v[152:155]\n"
      "ds_read_b128 v[188:191], v241 offset:57344\n"
      "v_mfma_f32_16x16x32_bf16 v[156:159], v[196:199], v[132:135], v[156:159]\n"
      "s_waitcnt lgkmcnt(5)\n"
      "v_mfma_f32_16x16x32_bf16 v[152:155], v[168:171], v[136:139], v[152:155]\n"
      "ds_read_b128 v[192:195], v240 offset:59392\n"
      "v_mfma_f32_16x16x32_bf16 v[156:159], v[168:171], v[140:143], v[156:159]\n"
      "s_waitcnt lgkmcnt(5)\n"
      "v_mfma_f32_16x16x32_bf16 v[152:155], v[172:175], v[144:147], v[152:155]\n"
      "ds_read_b128 v[196:199], v241 offset:59392\n"
      "v_mfma_f32_16x16x32_bf16 v[156:159], v[172:175], v[148:151], v[156:159]\n"
      "s_waitcnt lgkmcnt(5)\n"
      "v_mfma_f32_16x16x32_bf16 v[160:163], v[176:179], v[8:11], 0\n"
      "ds_read_b128 v[168:171], v240 offset:61440\n"
      "v_mfma_f32_16x16x32_bf16 v[164:167], v[176:179], v[12:15], 0\n"
      "s_waitcnt lgkmcnt(5)\n"
      "v_mfma_f32_16x16x32_bf16 v[160:163], v[180:183], v[16:19], v[160:163]\n"
      "ds_read_b128 v[172:175], v241 offset:61440\n"
      "v_mfma_f32_16x16x32_bf16 v[164:167], v[180:183], v[20:23], v[164:167]\n"
      "s_waitcnt lgkmcnt(5)\n"
      "v_mfma_f32_16x16x32_bf16 v[160:163], v[184:187], v[24:27], v[160:163]\n"
      "ds_read_b128 v[176:179], v240 offset:63488\n"
      "v_mfma_f32_16x16x32_bf16 v[164:167], v[184:187], v[28:31], v[164:167]\n"
      "s_waitcnt lgkmcnt(5)\n"
      "v_mfma_f32_16x16x32_bf16 v[160:163], v[188:191], v[32:35], v[160:163]\n"
      "ds_read_b128 v[180:183], v241 offset:63488\n"
      "v_mfma_f32_16x16x32_bf16 v[164:167], v[188:191], v[36:39], v[164:167]\n"
      "s_waitcnt lgkmcnt(5)\n"
      "v_mfma_f32_16x16x32_bf16 v[160:163], v[192:195], v[40:43], v[160:163]\n"
      "ds_read_b128 v[184:187], v214 offset:0\n"
      "v_mfma_f32_16x16x32_bf16 v[164:167], v[192:195], v[44:47], v[164:167]\n"
      "s_waitcnt lgkmcnt(5)\n"
      "v_mfma_f32_16x16x32_bf16 v[160:163], v[196:199], v[48:51], v[160:163]\n"
      "ds_read_b128 v[188:191], v215 offset:0\n"
      "v_mfma_f32_16x16x32_bf16 v[164:167], v[196:199], v[52:55], v[164:167]\n"
      "s_waitcnt lgkmcnt(5)\n"
      "v_mfma_f32_16x16x32_bf16 v[160:163], v[168:171], v[56:59], v[160:163]\n"
      "ds_read_b128 v[192:195], v214 offset:2048\n"
      "v_mfma_f32_16x16x32_bf16 v[164:167], v[168:171], v[60:63], v[164:167]\n"
      "s_waitcnt lgkmcnt(5)\n"
      "v_mfma_f32_16x16x32_bf16 v[160:163], v[172:175], v[64:67], v[160:163]\n"
      "ds_read_b128 v[196:199], v215 offset:2048\n"
      "v_mfma_f32_16x16x32_bf16 v[164:167], v[172:175], v[68:71], v[164:167]\n"
      "s_waitcnt lgkmcnt(5)\n"
      "v_mfma_f32_16x16x32_bf16 v[160:163], v[176:179], v[72:75], v[160:163]\n"
      "ds_read_b128 v[168:171], v214 offset:4096\n"
      "v_mfma_f32_16x16x32_bf16 v[164:167], v[176:179], v[76:79], v[164:167]\n"
      "s_waitcnt vmcnt(12)\n"
      "s_barrier\n"
      "s_setprio 1\n"
      "s_add_u32 m0, s43, 28672\n"
      "s_add_u32 s61, s36, 313984\n"
      "buffer_load_dwordx4 v243, s[16:19], s61 offen lds\n"
      "s_add_u32 m0, s43, 30720\n"
      "s_add_u32 s61, s36, 314112\n"
      "buffer_load_dwordx4 v243, s[16:19], s61 offen lds\n"
      "s_add_u32 m0, s43, 32768\n"
      "s_add_u32 s61, s36, 314240\n"
      "buffer_load_dwordx4 v243, s[16:19], s61 offen lds\n"
      "s_add_u32 m0, s43, 34816\n"
      "s_add_u32 s61, s36, 314368\n"
      "buffer_load_dwordx4 v243, s[16:19], s61 offen lds\n"
      "v_cvt_pk_bf16_f32 v152, v152, v156\n"
      "v_cvt_pk_bf16_f32 v153, v153, v157\n"
      "v_cvt_pk_bf16_f32 v154, v154, v158\n"
      "v_cvt_pk_bf16_f32 v155, v155, v159\n"
      "s_add_u32 s62, s37, 1327104\n"
      "buffer_store_dword v152, v247, s[68:71], s62 offen sc1\n"
      "s_add_u32 s62, s37, 1336320\n"
      "buffer_store_dword v153, v247, s[68:71], s62 offen sc1\n"
      "s_add_u32 s62, s37, 1345536\n"
      "buffer_store_dword v154, v247, s[68:71], s62 offen sc1\n"
      "s_add_u32 s62, s37, 1354752\n"
      "buffer_store_dword v155, v247, s[68:71], s62 offen sc1\n"
      "s_setprio 0\n"
      "s_waitcnt lgkmcnt(5)\n"
      "v_mfma_f32_16x16x32_bf16 v[160:163], v[180:183], v[80:83], v[160:163]\n"
      "ds_read_b128 v[172:175], v215 offset:4096\n"
      "v_mfma_f32_16x16x32_bf16 v[164:167], v[180:183], v[84:87], v[164:167]\n"
      "s_waitcnt lgkmcnt(5)\n"
      "v_mfma_f32_16x16x32_bf16 v[160:163], v[184:187], v[88:91], v[160:163]\n"
      "ds_read_b128 v[176:179], v214 offset:6144\n"
      "v_mfma_f32_16x16x32_bf16 v[164:167], v[184:187], v[92:95], v[164:167]\n"
      "s_waitcnt lgkmcnt(5)\n"
      "v_mfma_f32_16x16x32_bf16 v[160:163], v[188:191], v[96:99], v[160:163]\n"
      "ds_read_b128 v[180:183], v215 offset:6144\n"
      "v_mfma_f32_16x16x32_bf16 v[164:167], v[188:191], v[100:103], v[164:167]\n"
      "s_waitcnt lgkmcnt(5)\n"
      "v_mfma_f32_16x16x32_bf16 v[160:163], v[192:195], v[104:107], v[160:163]\n"
      "ds_read_b128 v[184:187], v214 offset:26624\n"
      "v_mfma_f32_16x16x32_bf16 v[164:167], v[192:195], v[108:111], v[164:167]\n"
      "s_waitcnt lgkmcnt(5)\n"
      "v_mfma_f32_16x16x32_bf16 v[160:163], v[196:199], v[112:115], v[160:163]\n"
      "ds_read_b128 v[188:191], v215 offset:26624\n"
      "v_mfma_f32_16x16x32_bf16 v[164:167], v[196:199], v[116:119], v[164:167]\n"
      "s_waitcnt lgkmcnt(5)\n"
      "v_mfma_f32_16x16x32_bf16 v[160:163], v[168:171], v[120:123], v[160:163]\n"
      "ds_read_b128 v[192:195], v214 offset:28672\n"
      "v_mfma_f32_16x16x32_bf16 v[164:167], v[168:171], v[124:127], v[164:167]\n"
      "s_waitcnt lgkmcnt(5)\n"
      "v_mfma_f32_16x16x32_bf16 v[160:163], v[172:175], v[128:131], v[160:163]\n"
      "ds_read_b128 v[196:199], v215 offset:28672\n"
      "v_mfma_f32_16x16x32_bf16 v[164:167], v[172:175], v[132:135], v[164:167]\n"
      "s_waitcnt lgkmcnt(5)\n"
      "v_mfma_f32_16x16x32_bf16 v[160:163], v[176:179], v[136:139], v[160:163]\n"
      "ds_read_b128 v[168:171], v214 offset:30720\n"
      "v_mfma_f32_16x16x32_bf16 v[164:167], v[176:179], v[140:143], v[164:167]\n"
      "s_waitcnt lgkmcnt(5)\n"
      "v_mfma_f32_16x16x32_bf16 v[160:163], v[180:183], v[144:147], v[160:163]\n"
      "ds_read_b128 v[172:175], v215 offset:30720\n"
      "v_mfma_f32_16x16x32_bf16 v[164:167], v[180:183], v[148:151], v[164:167]\n"
      "s_waitcnt lgkmcnt(5)\n"
      "v_mfma_f32_16x16x32_bf16 v[152:155], v[184:187], v[8:11], 0\n"
      "ds_read_b128 v[176:179], v214 offset:32768\n"
      "v_mfma_f32_16x16x32_bf16 v[156:159], v[184:187], v[12:15], 0\n"
      "s_waitcnt lgkmcnt(5)\n"
      "v_mfma_f32_16x16x32_bf16 v[152:155], v[188:191], v[16:19], v[152:155]\n"
      "ds_read_b128 v[180:183], v215 offset:32768\n"
      "v_mfma_f32_16x16x32_bf16 v[156:159], v[188:191], v[20:23], v[156:159]\n"
      "s_waitcnt lgkmcnt(5)\n"
      "v_mfma_f32_16x16x32_bf16 v[152:155], v[192:195], v[24:27], v[152:155]\n"
      "ds_read_b128 v[184:187], v214 offset:34816\n"
      "v_mfma_f32_16x16x32_bf16 v[156:159], v[192:195], v[28:31], v[156:159]\n"
      "s_waitcnt lgkmcnt(5)\n"
      "v_mfma_f32_16x16x32_bf16 v[152:155], v[196:199], v[32:35], v[152:155]\n"
      "ds_read_b128 v[188:191], v215 offset:34816\n"
      "v_mfma_f32_16x16x32_bf16 v[156:159], v[196:199], v[36:39], v[156:159]\n"
      "s_waitcnt lgkmcnt(5)\n"
      "v_mfma_f32_16x16x32_bf16 v[152:155], v[168:171], v[40:43], v[152:155]\n"
      "ds_read_b128 v[192:195], v214 offset:36864\n"
      "v_mfma_f32_16x16x32_bf16 v[156:159], v[168:171], v[44:47], v[156:159]\n"
      "s_waitcnt lgkmcnt(5)\n"
      "v_mfma_f32_16x16x32_bf16 v[152:155], v[172:175], v[48:51], v[152:155]\n"
      "ds_read_b128 v[196:199], v215 offset:36864\n"
      "v_mfma_f32_16x16x32_bf16 v[156:159], v[172:175], v[52:55], v[156:159]\n"
      "s_waitcnt lgkmcnt(5)\n"
      "v_mfma_f32_16x16x32_bf16 v[152:155], v[176:179], v[56:59], v[152:155]\n"
      "ds_read_b128 v[168:171], v214 offset:38912\n"
      "v_mfma_f32_16x16x32_bf16 v[156:159], v[176:179], v[60:63], v[156:159]\n"
      "s_waitcnt lgkmcnt(5)\n"
      "v_mfma_f32_16x16x32_bf16 v[152:155], v[180:183], v[64:67], v[152:155]\n"
      "ds_read_b128 v[172:175], v215 offset:38912\n"
      "v_mfma_f32_16x16x32_bf16 v[156:159], v[180:183], v[68:71], v[156:159]\n"
      "s_waitcnt lgkmcnt(5)\n"
      "v_mfma_f32_16x16x32_bf16 v[152:155], v[184:187], v[72:75], v[152:155]\n"
      "ds_read_b128 v[176:179], v214 offset:40960\n"
      "v_mfma_f32_16x16x32_bf16 v[156:159], v[184:187], v[76:79], v[156:159]\n"
      "s_waitcnt vmcnt(12)\n"
      "s_barrier\n"
      "s_setprio 1\n"
      "v_cvt_pk_bf16_f32 v160, v160, v164\n"
      "v_cvt_pk_bf16_f32 v161, v161, v165\n"
      "v_cvt_pk_bf16_f32 v162, v162, v166\n"
      "v_cvt_pk_bf16_f32 v163, v163, v167\n"
      "s_add_u32 s62, s37, 1622016\n"
      "buffer_store_dword v160, v247, s[68:71], s62 offen sc1\n"
      "s_add_u32 s62, s37, 1631232\n"
      "buffer_store_dword v161, v247, s[68:71], s62 offen sc1\n"
      "s_add_u32 s62, s37, 1640448\n"
      "buffer_store_dword v162, v247, s[68:71], s62 offen sc1\n"
      "s_add_u32 s62, s37, 1649664\n"
      "buffer_store_dword v163, v247, s[68:71], s62 offen sc1\n"
      "s_setprio 0\n"
      "s_waitcnt lgkmcnt(5)\n"
      "v_mfma_f32_16x16x32_bf16 v[152:155], v[188:191], v[80:83], v[152:155]\n"
      "ds_read_b128 v[180:183], v215 offset:40960\n"
      "v_mfma_f32_16x16x32_bf16 v[156:159], v[188:191], v[84:87], v[156:159]\n"
      "s_waitcnt lgkmcnt(5)\n"
      "v_mfma_f32_16x16x32_bf16 v[152:155], v[192:195], v[88:91], v[152:155]\n"
      "ds_read_b128 v[184:187], v214 offset:43008\n"
      "v_mfma_f32_16x16x32_bf16 v[156:159], v[192:195], v[92:95], v[156:159]\n"
      "s_waitcnt lgkmcnt(5)\n"
      "v_mfma_f32_16x16x32_bf16 v[152:155], v[196:199], v[96:99], v[152:155]\n"
      "ds_read_b128 v[188:191], v215 offset:43008\n"
      "v_mfma_f32_16x16x32_bf16 v[156:159], v[196:199], v[100:103], v[156:159]\n"
      "s_waitcnt lgkmcnt(5)\n"
      "v_mfma_f32_16x16x32_bf16 v[152:155], v[168:171], v[104:107], v[152:155]\n"
      "ds_read_b128 v[192:195], v214 offset:63488\n"
      "v_mfma_f32_16x16x32_bf16 v[156:159], v[168:171], v[108:111], v[156:159]\n"
      "s_waitcnt lgkmcnt(5)\n"
      "v_mfma_f32_16x16x32_bf16 v[152:155], v[172:175], v[112:115], v[152:155]\n"
      "ds_read_b128 v[196:199], v215 offset:63488\n"
      "v_mfma_f32_16x16x32_bf16 v[156:159], v[172:175], v[116:119], v[156:159]\n"
      "s_waitcnt lgkmcnt(5)\n"
      "v_mfma_f32_16x16x32_bf16 v[152:155], v[176:179], v[120:123], v[152:155]\n"
      "ds_read_b128 v[168:171], v216 offset:0\n"
      "v_mfma_f32_16x16x32_bf16 v[156:159], v[176:179], v[124:127], v[156:159]\n"
      "s_waitcnt lgkmcnt(5)\n"
      "v_mfma_f32_16x16x32_bf16 v[152:155], v[180:183], v[128:131], v[152:155]\n"
      "ds_read_b128 v[172:175], v217 offset:0\n"
      "v_mfma_f32_16x16x32_bf16 v[156:159], v[180:183], v[132:135], v[156:159]\n"
      "s_waitcnt lgkmcnt(5)\n"
      "v_mfma_f32_16x16x32_bf16 v[152:155], v[184:187], v[136:139], v[152:155]\n"
      "ds_read_b128 v[176:179], v216 offset:2048\n"
      "v_mfma_f32_16x16x32_bf16 v[156:159], v[184:187], v[140:143], v[156:159]\n"
      "s_waitcnt lgkmcnt(5)\n"
      "v_mfma_f32_16x16x32_bf16 v[152:155], v[188:191], v[144:147], v[152:155]\n"
      "ds_read_b128 v[180:183], v217 offset:2048\n"
      "v_mfma_f32_16x16x32_bf16 v[156:159], v[188:191], v[148:151], v[156:159]\n"
      "s_waitcnt lgkmcnt(5)\n"
      "v_mfma_f32_16x16x32_bf16 v[160:163], v[192:195], v[8:11], 0\n"
      "ds_read_b128 v[184:187], v216 offset:4096\n"
      "v_mfma_f32_16x16x32_bf16 v[164:167], v[192:195], v[12:15], 0\n"
      "s_waitcnt lgkmcnt(5)\n"
      "v_mfma_f32_16x16x32_bf16 v[160:163], v[196:199], v[16:19], v[160:163]\n"
      "ds_read_b128 v[188:191], v217 offset:4096\n"
      "v_mfma_f32_16x16x32_bf16 v[164:167], v[196:199], v[20:23], v[164:167]\n"
      "s_waitcnt lgkmcnt(5)\n"
      "v_mfma_f32_16x16x32_bf16 v[160:163], v[168:171], v[24:27], v[160:163]\n"
      "ds_read_b128 v[192:195], v216 offset:6144\n"
      "v_mfma_f32_16x16x32_bf16 v[164:167], v[168:171], v[28:31], v[164:167]\n"
      "s_waitcnt lgkmcnt(5)\n"
      "v_mfma_f32_16x16x32_bf16 v[160:163], v[172:175], v[32:35], v[160:163]\n"
      "ds_read_b128 v[196:199], v217 offset:6144\n"
      "v_mfma_f32_16x16x32_bf16 v[164:167], v[172:175], v[36:39], v[164:167]\n"
      "s_waitcnt lgkmcnt(5)\n"
      "v_mfma_f32_16x16x32_bf16 v[160:163], v[176:179], v[40:43], v[160:163]\n"
      "ds_read_b128 v[168:171], v216 offset:8192\n"
      "v_mfma_f32_16x16x32_bf16 v[164:167], v[176:179], v[44:47], v[164:167]\n"
      "s_waitcnt lgkmcnt(5)\n"
      "v_mfma_f32_16x16x32_bf16 v[160:163], v[180:183], v[48:51], v[160:163]\n"
      "ds_read_b128 v[172:175], v217 offset:8192\n"
      "v_mfma_f32_16x16x32_bf16 v[164:167], v[180:183], v[52:55], v[164:167]\n"
      "s_waitcnt lgkmcnt(5)\n"
      "v_mfma_f32_16x16x32_bf16 v[160:163], v[184:187], v[56:59], v[160:163]\n"
      "ds_read_b128 v[176:179], v216 offset:10240\n"
      "v_mfma_f32_16x16x32_bf16 v[164:167], v[184:187], v[60:63], v[164:167]\n"
      "s_waitcnt lgkmcnt(5)\n"
      "v_mfma_f32_16x16x32_bf16 v[160:163], v[188:191], v[64:67], v[160:163]\n"
      "ds_read_b128 v[180:183], v217 offset:10240\n"
      "v_mfma_f32_16x16x32_bf16 v[164:167], v[188:191], v[68:71], v[164:167]\n"
      "s_waitcnt lgkmcnt(5)\n"
      "v_mfma_f32_16x16x32_bf16 v[160:163], v[192:195], v[72:75], v[160:163]\n"
      "ds_read_b128 v[184:187], v216 offset:12288\n"
      "v_mfma_f32_16x16x32_bf16 v[164:167], v[192:195], v[76:79], v[164:167]\n"
      "s_waitcnt vmcnt(8)\n"
      "s_barrier\n"
      "s_setprio 1\n"
      "v_cvt_pk_bf16_f32 v152, v152, v156\n"
      "v_cvt_pk_bf16_f32 v153, v153, v157\n"
      "v_cvt_pk_bf16_f32 v154, v154, v158\n"
      "v_cvt_pk_bf16_f32 v155, v155, v159\n"
      "s_add_u32 s62, s37, 1916928\n"
      "buffer_store_dword v152, v247, s[68:71], s62 offen sc1\n"
      "s_add_u32 s62, s37, 1926144\n"
      "buffer_store_dword v153, v247, s[68:71], s62 offen sc1\n"
      "s_add_u32 s62, s37, 1935360\n"
      "buffer_store_dword v154, v247, s[68:71], s62 offen sc1\n"
      "s_add_u32 s62, s37, 1944576\n"
      "buffer_store_dword v155, v247, s[68:71], s62 offen sc1\n"
      "s_setprio 0\n"
      "s_waitcnt lgkmcnt(5)\n"
      "v_mfma_f32_16x16x32_bf16 v[160:163], v[196:199], v[80:83], v[160:163]\n"
      "ds_read_b128 v[188:191], v217 offset:12288\n"
      "v_mfma_f32_16x16x32_bf16 v[164:167], v[196:199], v[84:87], v[164:167]\n"
      "s_waitcnt lgkmcnt(5)\n"
      "v_mfma_f32_16x16x32_bf16 v[160:163], v[168:171], v[88:91], v[160:163]\n"
      "ds_read_b128 v[192:195], v216 offset:14336\n"
      "v_mfma_f32_16x16x32_bf16 v[164:167], v[168:171], v[92:95], v[164:167]\n"
      "s_waitcnt lgkmcnt(5)\n"
      "v_mfma_f32_16x16x32_bf16 v[160:163], v[172:175], v[96:99], v[160:163]\n"
      "ds_read_b128 v[196:199], v217 offset:14336\n"
      "v_mfma_f32_16x16x32_bf16 v[164:167], v[172:175], v[100:103], v[164:167]\n"
      "s_waitcnt lgkmcnt(5)\n"
      "v_mfma_f32_16x16x32_bf16 v[160:163], v[176:179], v[104:107], v[160:163]\n"
      "ds_read_b128 v[168:171], v240 offset:18432\n"
      "v_mfma_f32_16x16x32_bf16 v[164:167], v[176:179], v[108:111], v[164:167]\n"
      "s_waitcnt lgkmcnt(5)\n"
      "v_mfma_f32_16x16x32_bf16 v[160:163], v[180:183], v[112:115], v[160:163]\n"
      "ds_read_b128 v[172:175], v241 offset:18432\n"
      "v_mfma_f32_16x16x32_bf16 v[164:167], v[180:183], v[116:119], v[164:167]\n"
      "s_waitcnt lgkmcnt(5)\n"
      "v_mfma_f32_16x16x32_bf16 v[160:163], v[184:187], v[120:123], v[160:163]\n"
      "ds_read_b128 v[176:179], v240 offset:20480\n"
      "v_mfma_f32_16x16x32_bf16 v[164:167], v[184:187], v[124:127], v[164:167]\n"
      "s_waitcnt lgkmcnt(5)\n"
      "v_mfma_f32_16x16x32_bf16 v[160:163], v[188:191], v[128:131], v[160:163]\n"
      "ds_read_b128 v[180:183], v241 offset:20480\n"
      "v_mfma_f32_16x16x32_bf16 v[164:167], v[188:191], v[132:135], v[164:167]\n"
      "s_waitcnt lgkmcnt(5)\n"
      "v_mfma_f32_16x16x32_bf16 v[160:163], v[192:195], v[136:139], v[160:163]\n"
      "ds_read_b128 v[184:187], v240 offset:22528\n"
      "v_mfma_f32_16x16x32_bf16 v[164:167], v[192:195], v[140:143], v[164:167]\n"
      "s_waitcnt lgkmcnt(5)\n"
      "v_mfma_f32_16x16x32_bf16 v[160:163], v[196:199], v[144:147], v[160:163]\n"
      "ds_read_b128 v[188:191], v241 offset:22528\n"
      "v_mfma_f32_16x16x32_bf16 v[164:167], v[196:199], v[148:151], v[164:167]\n"
      "s_waitcnt lgkmcnt(5)\n"
      "v_mfma_f32_16x16x32_bf16 v[152:155], v[168:171], v[8:11], 0\n"
      "ds_read_b128 v[192:195], v240 offset:24576\n"
      "v_mfma_f32_16x16x32_bf16 v[156:159], v[168:171], v[12:15], 0\n"
      "s_waitcnt lgkmcnt(5)\n"
      "v_mfma_f32_16x16x32_bf16 v[152:155], v[172:175], v[16:19], v[152:155]\n"
      "ds_read_b128 v[196:199], v241 offset:24576\n"
      "v_mfma_f32_16x16x32_bf16 v[156:159], v[172:175], v[20:23], v[156:159]\n"
      "s_waitcnt lgkmcnt(5)\n"
      "v_mfma_f32_16x16x32_bf16 v[152:155], v[176:179], v[24:27], v[152:155]\n"
      "ds_read_b128 v[168:171], v240 offset:26624\n"
      "v_mfma_f32_16x16x32_bf16 v[156:159], v[176:179], v[28:31], v[156:159]\n"
      "s_waitcnt lgkmcnt(5)\n"
      "v_mfma_f32_16x16x32_bf16 v[152:155], v[180:183], v[32:35], v[152:155]\n"
      "ds_read_b128 v[172:175], v241 offset:26624\n"
      "v_mfma_f32_16x16x32_bf16 v[156:159], v[180:183], v[36:39], v[156:159]\n"
      "s_waitcnt lgkmcnt(5)\n"
      "v_mfma_f32_16x16x32_bf16 v[152:155], v[184:187], v[40:43], v[152:155]\n"
      "ds_read_b128 v[176:179], v240 offset:28672\n"
      "v_mfma_f32_16x16x32_bf16 v[156:159], v[184:187], v[44:47], v[156:159]\n"
      "s_waitcnt lgkmcnt(5)\n"
      "v_mfma_f32_16x16x32_bf16 v[152:155], v[188:191], v[48:51], v[152:155]\n"
      "ds_read_b128 v[180:183], v241 offset:28672\n"
      "v_mfma_f32_16x16x32_bf16 v[156:159], v[188:191], v[52:55], v[156:159]\n"
      "s_waitcnt lgkmcnt(5)\n"
      "v_mfma_f32_16x16x32_bf16 v[152:155], v[192:195], v[56:59], v[152:155]\n"
      "ds_read_b128 v[184:187], v240 offset:30720\n"
      "v_mfma_f32_16x16x32_bf16 v[156:159], v[192:195], v[60:63], v[156:159]\n"
      "s_waitcnt lgkmcnt(5)\n"
      "v_mfma_f32_16x16x32_bf16 v[152:155], v[196:199], v[64:67], v[152:155]\n"
      "ds_read_b128 v[188:191], v241 offset:30720\n"
      "v_mfma_f32_16x16x32_bf16 v[156:159], v[196:199], v[68:71], v[156:159]\n"
      "s_waitcnt lgkmcnt(5)\n"
      "v_mfma_f32_16x16x32_bf16 v[152:155], v[168:171], v[72:75], v[152:155]\n"
      "ds_read_b128 v[192:195], v240 offset:32768\n"
      "v_mfma_f32_16x16x32_bf16 v[156:159], v[168:171], v[76:79], v[156:159]\n"
      "s_barrier\n"
      "s_setprio 1\n"
      "v_cvt_pk_bf16_f32 v160, v160, v164\n"
      "v_cvt_pk_bf16_f32 v161, v161, v165\n"
      "v_cvt_pk_bf16_f32 v162, v162, v166\n"
      "v_cvt_pk_bf16_f32 v163, v163, v167\n"
      "s_add_u32 s62, s37, 2211840\n"
      "buffer_store_dword v160, v247, s[68:71], s62 offen sc1\n"
      "s_add_u32 s62, s37, 2221056\n"
      "buffer_store_dword v161, v247, s[68:71], s62 offen sc1\n"
      "s_add_u32 s62, s37, 2230272\n"
      "buffer_store_dword v162, v247, s[68:71], s62 offen sc1\n"
      "s_add_u32 s62, s37, 2239488\n"
      "buffer_store_dword v163, v247, s[68:71], s62 offen sc1\n"
      "s_setprio 0\n"
      "s_waitcnt lgkmcnt(5)\n"
      "v_mfma_f32_16x16x32_bf16 v[152:155], v[172:175], v[80:83], v[152:155]\n"
      "ds_read_b128 v[196:199], v241 offset:32768\n"
      "v_mfma_f32_16x16x32_bf16 v[156:159], v[172:175], v[84:87], v[156:159]\n"
      "s_waitcnt lgkmcnt(5)\n"
      "v_mfma_f32_16x16x32_bf16 v[152:155], v[176:179], v[88:91], v[152:155]\n"
      "ds_read_b128 v[168:171], v240 offset:34816\n"
      "v_mfma_f32_16x16x32_bf16 v[156:159], v[176:179], v[92:95], v[156:159]\n"
      "s_waitcnt lgkmcnt(5)\n"
      "v_mfma_f32_16x16x32_bf16 v[152:155], v[180:183], v[96:99], v[152:155]\n"
      "ds_read_b128 v[172:175], v241 offset:34816\n"
      "v_mfma_f32_16x16x32_bf16 v[156:159], v[180:183], v[100:103], v[156:159]\n"
      "s_waitcnt lgkmcnt(5)\n"
      "v_mfma_f32_16x16x32_bf16 v[152:155], v[184:187], v[104:107], v[152:155]\n"
      "v_mfma_f32_16x16x32_bf16 v[156:159], v[184:187], v[108:111], v[156:159]\n"
      "s_waitcnt lgkmcnt(4)\n"
      "v_mfma_f32_16x16x32_bf16 v[152:155], v[188:191], v[112:115], v[152:155]\n"
      "v_mfma_f32_16x16x32_bf16 v[156:159], v[188:191], v[116:119], v[156:159]\n"
      "s_waitcnt lgkmcnt(3)\n"
      "v_mfma_f32_16x16x32_bf16 v[152:155], v[192:195], v[120:123], v[152:155]\n"
      "v_mfma_f32_16x16x32_bf16 v[156:159], v[192:195], v[124:127], v[156:159]\n"
      "s_waitcnt lgkmcnt(2)\n"
      "v_mfma_f32_16x16x32_bf16 v[152:155], v[196:199], v[128:131], v[152:155]\n"
      "v_mfma_f32_16x16x32_bf16 v[156:159], v[196:199], v[132:135], v[156:159]\n"
      "s_waitcnt lgkmcnt(1)\n"
      "v_mfma_f32_16x16x32_bf16 v[152:155], v[168:171], v[136:139], v[152:155]\n"
      "v_mfma_f32_16x16x32_bf16 v[156:159], v[168:171], v[140:143], v[156:159]\n"
      "s_waitcnt lgkmcnt(0)\n"
      "v_mfma_f32_16x16x32_bf16 v[152:155], v[172:175], v[144:147], v[152:155]\n"
      "v_mfma_f32_16x16x32_bf16 v[156:159], v[172:175], v[148:151], v[156:159]\n"
      "s_nop 7\n"
      "v_cvt_pk_bf16_f32 v152, v152, v156\n"
      "v_cvt_pk_bf16_f32 v153, v153, v157\n"
      "v_cvt_pk_bf16_f32 v154, v154, v158\n"
      "v_cvt_pk_bf16_f32 v155, v155, v159\n"
      "s_add_u32 s62, s37, 2506752\n"
      "buffer_store_dword v152, v247, s[68:71], s62 offen sc1\n"
      "s_add_u32 s62, s37, 2515968\n"
      "buffer_store_dword v153, v247, s[68:71], s62 offen sc1\n"
      "s_add_u32 s62, s37, 2525184\n"
      "buffer_store_dword v154, v247, s[68:71], s62 offen sc1\n"
      "s_add_u32 s62, s37, 2534400\n"
      "buffer_store_dword v155, v247, s[68:71], s62 offen sc1\n"
      "s_waitcnt lgkmcnt(0)\n"
      "v_mov_b32 v8, 153600\n"
      "ds_read_b128 v[12:15], v8\n"
      "ds_read_b128 v[16:19], v8 offset:16\n"
      "s_waitcnt lgkmcnt(0)\n"
      "v_or3_b32 v12, v12, v13, v14\n"
      "v_or3_b32 v12, v12, v15, v16\n"
      "v_or3_b32 v12, v12, v17, v18\n"
      "v_or_b32 %[bad], v12, v19\n"
      "Lend_%=:\n"
      : [bad] "=&v"(bad)
      : [karg] "s"(__builtin_amdgcn_kernarg_segment_ptr()), [bid] "s"(blockIdx.x), [tid] "v"(threadIdx.x),
        [ldsb] "s"((unsigned)(size_t)lds)
      : "memory", "scc", "vcc", "v1", "v2", "v3", "v4", "v5", "v6", "v8", "v9", "v10", "v11", "v12", "v13", "v14", "v15", "v16", "v17", "v18", "v19", "v20", "v21", "v22", "v23", "v24", "v25", "v26", "v27", "v28", "v29", "v30", "v31", "v32", "v33", "v34", "v35", "v36", "v37", "v38", "v39", "v40", "v41", "v42", "v43", "v44", "v45", "v46", "v47", "v48", "v49", "v50", "v51", "v52", "v53", "v54", "v55", "v56", "v57", "v58", "v59", "v60", "v61", "v62", "v63", "v64", "v65", "v66", "v67", "v68", "v69", "v70", "v71", "v72", "v73", "v74", "v75", "v76", "v77", "v78", "v79", "v80", "v81", "v82", "v83", "v84", "v85", "v86", "v87", "v88", "v89", "v90", "v91", "v92", "v93", "v94", "v95", "v96", "v97", "v98", "v99", "v100", "v101", "v102", "v103", "v104", "v105", "v106", "v107", "v108", "v109", "v110", "v111", "v112", "v113", "v114", "v115", "v116", "v117", "v118", "v119", "v120", "v121", "v122", "v123", "v124", "v125", "v126", "v127", "v128", "v129", "v130", "v131", "v132", "v133", "v134", "v135", "v136", "v137", "v138", "v139", "v140", "v141", "v142", "v143", "v144", "v145", "v146", "v147", "v148", "v149", "v150", "v151", "v152", "v153", "v154", "v155", "v156", "v157", "v158", "v159", "v160", "v161", "v162", "v163", "v164", "v165", "v166", "v167", "v168", "v169", "v170", "v171", "v172", "v173", "v174", "v175", "v176", "v177", "v178", "v179", "v180", "v181", "v182", "v183", "v184", "v185", "v186", "v187", "v188", "v189", "v190", "v191", "v192", "v193", "v194", "v195", "v196", "v197", "v198", "v199", "v200", "v201", "v202", "v203", "v204", "v205", "v206", "v207", "v208", "v209", "v210", "v211", "v212", "v213", "v214", "v215", "v216", "v217", "v240", "v241", "v242", "v243", "v244", "v245", "v246", "v247", "v248", "s4", "s5", "s6", "s7", "s8", "s9", "s10", "s11", "s12", "s13", "s16", "s17", "s18", "s19", "s20", "s21", "s22", "s23", "s24", "s25", "s26", "s27", "s28", "s29", "s30", "s31", "s36", "s37", "s38", "s39", "s40", "s41", "s42", "s43", "s44", "s45", "s46", "s47", "s48", "s49", "s50", "s51", "s52", "s53", "s54", "s55", "s56", "s57", "s58", "s59", "s60", "s61", "s62", "s63", "s66", "s67", "s68", "s69", "s70", "s71");
  if (__builtin_amdgcn_readfirstlane(bad) != 0) {
    asm volatile("s_waitcnt vmcnt(0)" ::: "memory");
    __syncthreads();
    const int bid = blockIdx.x, tid = threadIdx.x;
    const int xcd = bid & 7, off = bid >> 3;
    const int id = xcd * 32 - (xcd > 4 ? xcd - 4 : 0) + off;
    const int cb = id / 7, rb = id - cb * 7;
    const int n0 = cb * 128, mt0 = rb * 18 + (rb < 2 ? rb : 2), rows = (rb < 2 ? 19 : 18) * 16;
    for (int o = tid; o < rows * 128; o += 512) {
      const int m = mt0 * 16 + (o >> 7), n = n0 + (o & 127);
      float acc = 0.f;
      for (int k = 0; k < K_; ++k) {
        const int gi = k >> 5;
        const float qv = rbf((float)qw[(size_t)k * N_ + n]);
        const float d = rbf(qv - bf2f(zp[gi * N_ + n]));
        const float w = rbf(d * bf2f(sc[gi * N_ + n]));
        acc = fmaf(bf2f(x[(size_t)m * K_ + k]), w, acc);
      }
      const __hip_bfloat16 r = __float2bfloat16(acc);
      out[(size_t)m * N_ + n] = *reinterpret_cast<const u16*>(&r);
    }
  }
}

extern "C" void kernel_launch(void* const* d_in, const int* in_sizes, int n_in, void* d_out, int out_size, void* d_ws,
                              size_t ws_size, hipStream_t stream) {
  (void)in_sizes; (void)n_in; (void)out_size; (void)ws_size; (void)d_ws;
  wq_gemm_kernel<<<dim3(252), dim3(512), 0, stream>>>((const u16*)d_in[0], (const int*)d_in[1], (const u16*)d_in[2],
                                                      (const u16*)d_in[3], (u16*)d_out);
}
```
